```python
import torch

def _softmax(x, axis=-1):
    return torch.softmax(x, dim=axis)

def _gelu(x):
    return torch.nn.functional.gelu(x, approximate='tanh')

def _silu(x):
    return torch.nn.functional.silu(x)

def _sigmoid(x):
    return torch.sigmoid(x)

def _layernorm(x, axis=-1, eps=1e-05):
    m = x.mean(dim=axis, keepdim=True)
    v = x.var(dim=axis, keepdim=True, unbiased=False)
    return (x - m) / torch.sqrt(v + eps)

def _scatter_add(tgt, idx, src, axis=0):
    return tgt.clone().index_add_(axis, idx, src)

def _segment_sum(src, seg, n):
    out = torch.zeros((n,) + src.shape[1:], dtype=src.dtype)
    return out.index_add_(0, seg, src)

def _pad(x, pw):
    flat = tuple((v for pair in reversed(pw) for v in pair))
    return torch.nn.functional.pad(x, flat)
kernel_name = 'adaLN_cond__spectral_gfnet__moe_arch_mod__prompt'

def setup_inputs():

    _w = lambda a: (lambda t: t.to(torch.bfloat16) if t.is_floating_point() else t)(torch.as_tensor(a))

    def _A_setup():
        B, S, D = (1, 2048, 640)
        Df = D // 2 + 1
        return {'x': _w((lambda *s: torch.randn(*s, dtype=torch.bfloat16) * 0.05)(B, S, D)), 'w_r': _w((lambda *s: torch.randn(*s, dtype=torch.bfloat16) * 3.2)(S, Df) * 0.02), 'w_i': _w((lambda *s: torch.randn(*s, dtype=torch.bfloat16) * 3.2)(S, Df) * 0.02)}
    _A_in = _A_setup()

    def _B_setup():
        B, S, D, F, C = (1, 2048, 640, 1728, 8)
        return {'x': _w((lambda *s: torch.randn(*s, dtype=torch.bfloat16) * 0.05)(B, S, D)), 'w_r': _w((lambda *s: torch.randn(*s, dtype=torch.bfloat16) * 0.2)(D)), 'w_gate': _w((lambda *s: torch.randn(*s, dtype=torch.bfloat16) * 0.2)(D, F)), 'w_up': _w((lambda *s: torch.randn(*s, dtype=torch.bfloat16) * 0.2)(D, F)), 'w_down': _w((lambda *s: torch.randn(*s, dtype=torch.bfloat16) * 0.2)(F, D))}
    _B_in = _B_setup()
    return {'A_x': _A_in['x'], 'A_w_r': _A_in['w_r'], 'A_w_i': _A_in['w_i'], 'B_x': _B_in['x'], 'B_w_r': _B_in['w_r'], 'B_w_gate': _B_in['w_gate'], 'B_w_up': _B_in['w_up'], 'B_w_down': _B_in['w_down'], 'c': _w((lambda *s: torch.randn(*s, dtype=torch.bfloat16) * 0.1)(1, 640) * 8.0), 'w_mod': _w((lambda *s: torch.randn(*s, dtype=torch.bfloat16) * 0.1)(640, 6 * 640) * 640 ** (-0.5) * 8.0)}

def reference(A_x, A_w_r, A_w_i, B_x, B_w_r, B_w_gate, B_w_up, B_w_down, c, w_mod):

    def _A_ref(x, w_r, w_i):
        B, S, D = x.shape
        X = (lambda x, axes=(-2, -1): torch.fft.rfft2(x.float(), dim=axes))(x, axes=(-2, -1))
        W = w_r + 1j * w_i
        X = X * W
        y = (lambda x, s, axes=(-2, -1): torch.fft.irfft2(x, s=s, dim=axes).to(torch.bfloat16))(X, (S, D), axes=(-2, -1))
        return y

    def _B_ref(x, w_r, w_gate, w_up, w_down):
        B, S, D = x.shape
        C = 8
        r = x @ w_r
        idx = torch.argsort(-r, axis=-1)[:, :C]
        idx_e = (lambda x, n, axis: torch.repeat_interleave(x, n, dim=axis))(idx.reshape(B, C, 1), D, axis=2)
        x_sel = (lambda x, i, axis: torch.take_along_dim(x, i, dim=axis))(x, idx_e, axis=1)
        g = _silu(x_sel @ w_gate)
        u = x_sel @ w_up
        y = g * u @ w_down
        r_sel = _sigmoid((lambda x, i, axis: torch.take_along_dim(x, i, dim=axis))(r, idx, axis=1))
        y = y * r_sel.reshape(B, C, 1)
        b_off = (torch.arange(B) * S).reshape(B, 1)
        flat_idx = (idx + b_off).reshape(B * C)
        zero = (x * 0.0).reshape(B * S, D)
        upd = _scatter_add(zero, flat_idx, y.reshape(B * C, D), axis=0)
        return x + upd.reshape(B, S, D)
    x = A_x
    mod = (_silu(c) @ w_mod).reshape(c.shape[0], 6, 1, -1)
    sa, ca, ga, sb, cb, gb = (mod[:, 0].clone(), mod[:, 1].clone(), mod[:, 2].clone(), mod[:, 3].clone(), mod[:, 4].clone(), mod[:, 5].clone())
    h = _layernorm(x, axis=-1) * (1 + ca) + sa
    x = x + ga * _A_ref(h, A_w_r, A_w_i)
    h = _layernorm(x, axis=-1) * (1 + cb) + sb
    x = x + gb * _B_ref(h, B_w_r, B_w_gate, B_w_up, B_w_down)
    return x
```

```cpp
#include <hip/hip_runtime.h>
#include <stdint.h>

typedef unsigned short u16;
typedef unsigned int u32;
typedef unsigned long long u64;
typedef float f2 __attribute__((ext_vector_type(2)));
typedef float f4 __attribute__((ext_vector_type(4)));
typedef u32 u2 __attribute__((ext_vector_type(2)));
typedef u32 u4 __attribute__((ext_vector_type(4)));
typedef _Float16 hh2 __attribute__((ext_vector_type(2)));
typedef _Float16 hh8 __attribute__((ext_vector_type(8)));
typedef __bf16 bb2 __attribute__((ext_vector_type(2)));
typedef __bf16 bb8 __attribute__((ext_vector_type(8)));
typedef __amdgpu_buffer_rsrc_t rsrc_t;

constexpr int T_B1 = 0;
constexpr int T_TW1 = 1024;
constexpr int T_F10 = 2048;
constexpr int T_F16 = 2560;
constexpr int T_TW2 = 3072;
constexpr int T_F16I = 3584;
constexpr int T_F10I = 4096;
constexpr int T_T1I = 4608;
constexpr int T_TW1I = 5120;
constexpr int T_P2 = 6656;
constexpr int T_ADR = 23040;
constexpr int T_ADP = 28160;
constexpr int TL_SP = 1184, TL_A = 2208, TL_B = 7328;
__device__ __attribute__((aligned(64))) const unsigned TAB[29696] = {
0x3f803f80,0x3f803f80,0x3f803f80,0x3f803f80,0x3f353f80,0xbf350000,0xbf35bf80,0x3f350000,0x3f80,0xbf80,0x3f80,0xbf80,
0xbf353f80,0x3f350000,0x3f35bf80,0xbf350000,0x3f803f80,0x3f803f80,0x3f803f80,0x3f803f80,0x3f353f80,0xbf350000,0xbf35bf80,0x3f350000,
0x3f80,0xbf80,0x3f80,0xbf80,0xbf353f80,0x3f350000,0x3f35bf80,0xbf350000,0x3f353f80,0xbf350000,0xbf35bf80,0x3f350000,
0x3f80,0xbf80,0x3f80,0xbf80,0xbf353f80,0x3f350000,0x3f35bf80,0xbf350000,0x3f803f80,0x3f803f80,0x3f803f80,0x3f803f80,
0x3f353f80,0xbf350000,0xbf35bf80,0x3f350000,0x3f80,0xbf80,0x3f80,0xbf80,0xbf353f80,0x3f350000,0x3f35bf80,0xbf350000,
0xbf803f80,0xbf803f80,0xbf803f80,0xbf803f80,0x3f803f80,0x3f803f80,0x3f803f80,0x3f803f80,0x3f353f80,0xbf350000,0xbf35bf80,0x3f350000,
0x3f80,0xbf80,0x3f80,0xbf80,0xbf353f80,0x3f350000,0x3f35bf80,0xbf350000,0xbf80bf80,0xbf80bf80,0xbf80bf80,0xbf80bf80,
0xbf35bf80,0x3f350000,0x3f353f80,0xbf350000,0xbf80,0x3f80,0xbf80,0x3f80,0x3f35bf80,0xbf350000,0xbf353f80,0x3f350000,
0xbf350000,0xbf35bf80,0x3f350000,0x3f353f80,0xbf800000,0x3f800000,0xbf800000,0x3f800000,0xbf350000,0xbf353f80,0x3f350000,0x3f35bf80,
0,0,0,0,0x3f350000,0x3f353f80,0xbf350000,0xbf35bf80,0x3f800000,0xbf800000,0x3f800000,0xbf800000,
0x3f350000,0x3f35bf80,0xbf350000,0xbf353f80,0,0,0,0,0x3f803f80,0x3f803f80,0x3f803f80,0x3f803f80,
0x3f353f80,0xbf350000,0xbf35bf80,0x3f350000,0x3f80,0xbf80,0x3f80,0xbf80,0xbf353f80,0x3f350000,0x3f35bf80,0xbf350000,
0x3f803f80,0x3f803f80,0x3f803f80,0x3f803f80,0x3f353f80,0xbf350000,0xbf35bf80,0x3f350000,0x3f80,0xbf80,0x3f80,0xbf80,
0xbf353f80,0x3f350000,0x3f35bf80,0xbf350000,0xbf35bf80,0x3f350000,0x3f353f80,0xbf350000,0xbf80,0x3f80,0xbf80,0x3f80,
0x3f35bf80,0xbf350000,0xbf353f80,0x3f350000,0xbf80bf80,0xbf80bf80,0xbf80bf80,0xbf80bf80,0xbf35bf80,0x3f350000,0x3f353f80,0xbf350000,
0xbf80,0x3f80,0xbf80,0x3f80,0x3f35bf80,0xbf350000,0xbf353f80,0x3f350000,0x3f80bf80,0x3f80bf80,0x3f80bf80,0x3f80bf80,
0x3f803f80,0x3f803f80,0x3f803f80,0x3f803f80,0x3f353f80,0xbf350000,0xbf35bf80,0x3f350000,0x3f80,0xbf80,0x3f80,0xbf80,
0xbf353f80,0x3f350000,0x3f35bf80,0xbf350000,0xbf80bf80,0xbf80bf80,0xbf80bf80,0xbf80bf80,0xbf35bf80,0x3f350000,0x3f353f80,0xbf350000,
0xbf80,0x3f80,0xbf80,0x3f80,0x3f35bf80,0xbf350000,0xbf353f80,0x3f350000,0x3f350000,0x3f353f80,0xbf350000,0xbf35bf80,
0x3f800000,0xbf800000,0x3f800000,0xbf800000,0x3f350000,0x3f35bf80,0xbf350000,0xbf353f80,0,0,0,0,
0xbf350000,0xbf35bf80,0x3f350000,0x3f353f80,0xbf800000,0x3f800000,0xbf800000,0x3f800000,0xbf350000,0xbf353f80,0x3f350000,0x3f35bf80,
0,0,0,0,0xbf803f80,0xbf803f80,0xbf803f80,0xbf803f80,0xbf350000,0xbf35bf80,0x3f350000,0x3f353f80,
0xbf800000,0x3f800000,0xbf800000,0x3f800000,0xbf350000,0xbf353f80,0x3f350000,0x3f35bf80,0xbf803f80,0xbf803f80,0xbf803f80,0xbf803f80,
0xbf350000,0xbf35bf80,0x3f350000,0x3f353f80,0xbf800000,0x3f800000,0xbf800000,0x3f800000,0xbf350000,0xbf353f80,0x3f350000,0x3f35bf80,
0xbf350000,0xbf35bf80,0x3f350000,0x3f353f80,0xbf800000,0x3f800000,0xbf800000,0x3f800000,0xbf350000,0xbf353f80,0x3f350000,0x3f35bf80,
0,0,0,0,0x3f350000,0x3f353f80,0xbf350000,0xbf35bf80,0x3f800000,0xbf800000,0x3f800000,0xbf800000,
0x3f350000,0x3f35bf80,0xbf350000,0xbf353f80,0,0,0,0,0xbf803f80,0xbf803f80,0xbf803f80,0xbf803f80,
0xbf350000,0xbf35bf80,0x3f350000,0x3f353f80,0xbf800000,0x3f800000,0xbf800000,0x3f800000,0xbf350000,0xbf353f80,0x3f350000,0x3f35bf80,
0x3f80bf80,0x3f80bf80,0x3f80bf80,0x3f80bf80,0x3f350000,0x3f353f80,0xbf350000,0xbf35bf80,0x3f800000,0xbf800000,0x3f800000,0xbf800000,
0x3f350000,0x3f35bf80,0xbf350000,0xbf353f80,0xbf35bf80,0x3f350000,0x3f353f80,0xbf350000,0xbf80,0x3f80,0xbf80,0x3f80,
0x3f35bf80,0xbf350000,0xbf353f80,0x3f350000,0xbf80bf80,0xbf80bf80,0xbf80bf80,0xbf80bf80,0xbf35bf80,0x3f350000,0x3f353f80,0xbf350000,
0xbf80,0x3f80,0xbf80,0x3f80,0x3f35bf80,0xbf350000,0xbf353f80,0x3f350000,0x3f80bf80,0x3f80bf80,0x3f80bf80,0x3f80bf80,
0xbf803f80,0xbf803f80,0xbf803f80,0xbf803f80,0xbf350000,0xbf35bf80,0x3f350000,0x3f353f80,0xbf800000,0x3f800000,0xbf800000,0x3f800000,
0xbf350000,0xbf353f80,0x3f350000,0x3f35bf80,0xbf803f80,0xbf803f80,0xbf803f80,0xbf803f80,0xbf350000,0xbf35bf80,0x3f350000,0x3f353f80,
0xbf800000,0x3f800000,0xbf800000,0x3f800000,0xbf350000,0xbf353f80,0x3f350000,0x3f35bf80,0x3f350000,0x3f353f80,0xbf350000,0xbf35bf80,
0x3f800000,0xbf800000,0x3f800000,0xbf800000,0x3f350000,0x3f35bf80,0xbf350000,0xbf353f80,0,0,0,0,
0xbf350000,0xbf35bf80,0x3f350000,0x3f353f80,0xbf800000,0x3f800000,0xbf800000,0x3f800000,0xbf350000,0xbf353f80,0x3f350000,0x3f35bf80,
0,0,0,0,0xbf803f80,0xbf803f80,0xbf803f80,0xbf803f80,0xbf350000,0xbf35bf80,0x3f350000,0x3f353f80,
0xbf800000,0x3f800000,0xbf800000,0x3f800000,0xbf350000,0xbf353f80,0x3f350000,0x3f35bf80,0x3f80bf80,0x3f80bf80,0x3f80bf80,0x3f80bf80,
0x3f350000,0x3f353f80,0xbf350000,0xbf35bf80,0x3f800000,0xbf800000,0x3f800000,0xbf800000,0x3f350000,0x3f35bf80,0xbf350000,0xbf353f80,
0x3f353f80,0xbf350000,0xbf35bf80,0x3f350000,0x3f80,0xbf80,0x3f80,0xbf80,0xbf353f80,0x3f350000,0x3f35bf80,0xbf350000,
0x3f803f80,0x3f803f80,0x3f803f80,0x3f803f80,0x3f353f80,0xbf350000,0xbf35bf80,0x3f350000,0x3f80,0xbf80,0x3f80,0xbf80,
0xbf353f80,0x3f350000,0x3f35bf80,0xbf350000,0xbf803f80,0xbf803f80,0xbf803f80,0xbf803f80,0,0,0,0,
0x389e0000,0xb89e0000,0xb89e0000,0x389e0000,0,0,0,0,0xb89e0000,0x389e0000,0x389e0000,0xb89e0000,
0,0,0,0,0x389e0000,0xb89e0000,0xb89e0000,0x389e0000,0,0,0,0,
0xb89e0000,0x389e0000,0x389e0000,0xb89e0000,0x389e0000,0xb89e0000,0xb89e0000,0x389e0000,0,0,0,0,
0xb89e0000,0x389e0000,0x389e0000,0xb89e0000,0,0,0,0,0x389e0000,0xb89e0000,0xb89e0000,0x389e0000,
0,0,0,0,0xb89e0000,0x389e0000,0x389e0000,0xb89e0000,0,0,0,0,
0,0,0,0,0x389e0000,0xb89e0000,0xb89e0000,0x389e0000,0,0,0,0,
0xb89e0000,0x389e0000,0x389e0000,0xb89e0000,0,0,0,0,0xb89e0000,0x389e0000,0x389e0000,0xb89e0000,
0,0,0,0,0x389e0000,0xb89e0000,0xb89e0000,0x389e0000,0xb89e0000,0xb89e0000,0x389e0000,0x389e0000,
0,0,0,0,0xb89e0000,0xb89e0000,0x389e0000,0x389e0000,0,0,0,0,
0x389e0000,0x389e0000,0xb89e0000,0xb89e0000,0,0,0,0,0x389e0000,0x389e0000,0xb89e0000,0xb89e0000,
0,0,0,0,0,0,0,0,0x389e0000,0xb89e0000,0xb89e0000,0x389e0000,
0,0,0,0,0xb89e0000,0x389e0000,0x389e0000,0xb89e0000,0,0,0,0,
0x389e0000,0xb89e0000,0xb89e0000,0x389e0000,0,0,0,0,0xb89e0000,0x389e0000,0x389e0000,0xb89e0000,
0xb89e0000,0x389e0000,0x389e0000,0xb89e0000,0,0,0,0,0x389e0000,0xb89e0000,0xb89e0000,0x389e0000,
0,0,0,0,0xb89e0000,0x389e0000,0x389e0000,0xb89e0000,0,0,0,0,
0x389e0000,0xb89e0000,0xb89e0000,0x389e0000,0,0,0,0,0,0,0,0,
0x389e0000,0xb89e0000,0xb89e0000,0x389e0000,0,0,0,0,0xb89e0000,0x389e0000,0x389e0000,0xb89e0000,
0,0,0,0,0xb89e0000,0x389e0000,0x389e0000,0xb89e0000,0,0,0,0,
0x389e0000,0xb89e0000,0xb89e0000,0x389e0000,0x389e0000,0x389e0000,0xb89e0000,0xb89e0000,0,0,0,0,
0x389e0000,0x389e0000,0xb89e0000,0xb89e0000,0,0,0,0,0xb89e0000,0xb89e0000,0x389e0000,0x389e0000,
0,0,0,0,0xb89e0000,0xb89e0000,0x389e0000,0x389e0000,0,0,0,0,
0,0,0,0,0xb89e0000,0xb89e0000,0x389e0000,0x389e0000,0,0,0,0,
0xb89e0000,0xb89e0000,0x389e0000,0x389e0000,0,0,0,0,0xb89e0000,0xb89e0000,0x389e0000,0x389e0000,
0,0,0,0,0xb89e0000,0xb89e0000,0x389e0000,0x389e0000,0xb89e0000,0xb89e0000,0x389e0000,0x389e0000,
0,0,0,0,0xb89e0000,0xb89e0000,0x389e0000,0x389e0000,0,0,0,0,
0x389e0000,0x389e0000,0xb89e0000,0xb89e0000,0,0,0,0,0x389e0000,0x389e0000,0xb89e0000,0xb89e0000,
0,0,0,0,0,0,0,0,0xb89e0000,0xb89e0000,0x389e0000,0x389e0000,
0,0,0,0,0xb89e0000,0xb89e0000,0x389e0000,0x389e0000,0,0,0,0,
0x389e0000,0x389e0000,0xb89e0000,0xb89e0000,0,0,0,0,0x389e0000,0x389e0000,0xb89e0000,0xb89e0000,
0xb89e0000,0x389e0000,0x389e0000,0xb89e0000,0,0,0,0,0x389e0000,0xb89e0000,0xb89e0000,0x389e0000,
0,0,0,0,0xb89e0000,0x389e0000,0x389e0000,0xb89e0000,0,0,0,0,
0x389e0000,0xb89e0000,0xb89e0000,0x389e0000,0,0,0,0,0,0,0,0,
0xb89e0000,0xb89e0000,0x389e0000,0x389e0000,0,0,0,0,0xb89e0000,0xb89e0000,0x389e0000,0x389e0000,
0,0,0,0,0xb89e0000,0xb89e0000,0x389e0000,0x389e0000,0,0,0,0,
0xb89e0000,0xb89e0000,0x389e0000,0x389e0000,0x389e0000,0x389e0000,0xb89e0000,0xb89e0000,0,0,0,0,
0x389e0000,0x389e0000,0xb89e0000,0xb89e0000,0,0,0,0,0xb89e0000,0xb89e0000,0x389e0000,0x389e0000,
0,0,0,0,0xb89e0000,0xb89e0000,0x389e0000,0x389e0000,0,0,0,0,
0,0,0,0,0xb89e0000,0xb89e0000,0x389e0000,0x389e0000,0,0,0,0,
0xb89e0000,0xb89e0000,0x389e0000,0x389e0000,0,0,0,0,0x389e0000,0x389e0000,0xb89e0000,0xb89e0000,
0,0,0,0,0x389e0000,0x389e0000,0xb89e0000,0xb89e0000,0x389e0000,0xb89e0000,0xb89e0000,0x389e0000,
0,0,0,0,0xb89e0000,0x389e0000,0x389e0000,0xb89e0000,0,0,0,0,
0x389e0000,0xb89e0000,0xb89e0000,0x389e0000,0,0,0,0,0xb89e0000,0x389e0000,0x389e0000,0xb89e0000,
0,0,0,0,0x3e3504f3,0x3e3504f3,0x3e3504f3,0x3e3504f3,0x3e3504f3,0x3e3504f3,0x3e3504f3,0x3e3504f3,
0x3e3504f3,0x3e3504f3,0x3e3504f3,0x3e3504f3,0x3e3504f3,0x3e3504f3,0x3e3504f3,0x3e3504f3,0,0,0,0,
0,0,0,0,0,0,0,0,0,0,0,0,
0x3e3504f3,0x3e3504f3,0x3e3504f3,0x3e3504f3,0x3e3504f3,0x3e3504f3,0x3e3504f3,0x3e3504f3,0x3e3504f3,0x3e3504f3,0x3e3504f3,0x3e3504f3,
0x3e3504f3,0x3e3504f3,0x3e3504f3,0x3e3504f3,0,0,0,0,0,0,0,0,
0,0,0,0,0,0,0,0,0x3e3504f3,0x3e3504f3,0x3e3504f3,0x3e3504f3,
0x3e3504f3,0x3e3504f3,0x3e3504f3,0x3e3504f3,0x3e3504f3,0x3e3504f3,0x3e3504f3,0x3e3504f3,0x3e3504f3,0x3e3504f3,0x3e3504f3,0x3e3504f3,
0,0,0,0,0,0,0,0,0,0,0,0,
0,0,0,0,0x3e3504f3,0x3e3504f3,0x3e3504f3,0x3e3504f3,0x3e3504f3,0x3e3504f3,0x3e3504f3,0x3e3504f3,
0x3e3504f3,0x3e3504f3,0x3e3504f3,0x3e3504f3,0x3e3504f3,0x3e3504f3,0x3e3504f3,0x3e3504f3,0,0,0,0,
0,0,0,0,0,0,0,0,0,0,0,0,
0x3e3504f3,0x3e3504f3,0x3e3504f3,0x3e3504f3,0x3e3504f3,0x3e3504f3,0x3e3504f3,0x3e3504f3,0x3e3504f3,0x3e3504f3,0x3e3504f3,0x3e3504f3,
0x3e3504f3,0x3e3504f3,0x3e3504f3,0x3e3504f3,0,0,0,0,0,0,0,0,
0,0,0,0,0,0,0,0,0x3e3504f3,0x3e3504f3,0x3e3504f3,0x3e3504f3,
0x3e3504f3,0x3e3504f3,0x3e3504f3,0x3e3504f3,0x3e3504f3,0x3e3504f3,0x3e3504f3,0x3e3504f3,0x3e3504f3,0x3e3504f3,0x3e3504f3,0x3e3504f3,
0,0,0,0,0,0,0,0,0,0,0,0,
0,0,0,0,0x3e3504f3,0x3e3504f3,0x3e3504f3,0x3e3504f3,0x3e3504f3,0x3e3504f3,0x3e3504f3,0x3e3504f3,
0x3e3504f3,0x3e3504f3,0x3e3504f3,0x3e3504f3,0x3e3504f3,0x3e3504f3,0x3e3504f3,0x3e3504f3,0,0,0,0,
0,0,0,0,0,0,0,0,0,0,0,0,
0x3e3504f3,0x3e3504f3,0x3e3504f3,0x3e3504f3,0x3e3504f3,0x3e3504f3,0x3e3504f3,0x3e3504f3,0x3e3504f3,0x3e3504f3,0x3e3504f3,0x3e3504f3,
0x3e3504f3,0x3e3504f3,0x3e3504f3,0x3e3504f3,0,0,0,0,0,0,0,0,
0,0,0,0,0,0,0,0,0x3e3504f3,0x3e3504f3,0x3e3504f3,0x3e3504f3,
0x3e3504f3,0x3e3504f3,0x3e3504f3,0x3e3504f3,0x3e3504f3,0x3e3504f3,0x3e3504f3,0x3e3504f3,0x3e3504f3,0x3e3504f3,0x3e3504f3,0x3e3504f3,
0,0,0,0,0,0,0,0,0,0,0,0,
0,0,0,0,0x3e3504f3,0x3e3504f3,0x3e3504f3,0x3e3504f3,0x3e3504f3,0x3e3504f3,0x3e3504f3,0x3e3504f3,
0x3e3504f3,0x3e3504f3,0x3e3504f3,0x3e3504f3,0x3e3504f3,0x3e3504f3,0x3e3504f3,0x3e3504f3,0,0,0,0,
0,0,0,0,0,0,0,0,0,0,0,0,
0x3e3504f3,0x3e3502b7,0x3e34fc04,0x3e34f0da,0x3e34e139,0x3e34cd21,0x3e34b494,0x3e349791,0x3e347619,0x3e34502d,0x3e3425ce,0x3e33f6fe,
0x3e33c3bc,0x3e338c0c,0x3e334fed,0x3e330f61,0,0xbae378d2,0xbb637603,0xbbaa9501,0xbbe36aca,0xbc0e1d7b,0xbc2a8210,0xbc46e270,
0xbc633de8,0xbc7f93c4,0xbc8df1a9,0xbc9c15ef,0xbcaa365b,0xbcb85295,0xbcc66a42,0xbcd47d09,0x3e32ca6b,0x3e32810b,0x3e323343,0x3e31e116,
0x3e318a86,0x3e312f94,0x3e30d042,0x3e306c94,0x3e30048c,0x3e2f982b,0x3e2f2776,0x3e2eb26e,0x3e2e3917,0x3e2dbb73,0x3e2d3986,0x3e2cb353,
0xbce28a93,0xbcf09285,0xbcfe9488,0xbd064822,0xbd0d42af,0xbd1439c0,0xbd1b2d29,0xbd221cbd,0xbd290852,0xbd2fefbb,0xbd36d2cc,0xbd3db15b,
0xbd448b3c,0xbd4b6043,0xbd523045,0xbd58fb18,0x3e2c28dd,0x3e2b9a27,0x3e2b0736,0x3e2a700c,0x3e29d4ae,0x3e29351e,0x3e289162,0x3e27e97e,
0x3e273d75,0x3e268d4b,0x3e25d905,0x3e2520a8,0x3e246438,0x3e23a3b9,0x3e22df31,0x3e2216a4,0xbd5fc090,0xbd668083,0xbd6d3ac6,0xbd73ef2f,
0xbd7a9d93,0xbd80a2e4,0xbd83f3d2,0xbd87417e,0xbd8a8bd4,0xbd8dd2bf,0xbd91162a,0xbd945600,0xbd97922e,0xbd9aca9e,0xbd9dff3d,0xbda12ff5,
0x3e214a17,0x3e20798f,0x3e1fa512,0x3e1ecca4,0x3e1df04b,0x3e1d100d,0x3e1c2bee,0x3e1b43f5,0x3e1a5828,0x3e19688b,0x3e187526,0x3e177dfd,
0x3e168317,0x3e15847b,0x3e14822f,0x3e137c38,0xbda45cb4,0xbda78564,0xbdaaa9f2,0xbdadca4a,0xbdb0e658,0xbdb3fe09,0xbdb71149,0xbdba2005,
0xbdbd2a29,0xbdc02fa2,0xbdc3305d,0xbdc62c48,0xbdc9234e,0xbdcc155e,0xbdcf0265,0xbdd1ea50,0x3e12729e,0x3e116566,0x3e105499,0x3e0f403b,
0x3e0e2855,0x3e0d0ced,0x3e0bee0a,0x3e0acbb3,0x3e09a5ef,0x3e087cc6,0x3e07503f,0x3e062061,0x3e04ed34,0x3e03b6bf,0x3e027d0a,0x3e01401d,
0xbdd4cd0e,0xbdd7aa8b,0xbdda82b6,0xbddd557d,0xbde022ce,0xbde2ea97,0xbde5acc6,0xbde8694b,0xbdeb2014,0xbdedd110,0xbdf07c2e,0xbdf3215c,
0xbdf5c08b,0xbdf859aa,0xbdfaeca7,0xbdfd7974,0x3e000000,0x3dfd7974,0x3dfaeca7,0x3df859aa,0x3df5c08b,0x3df3215c,0x3df07c2e,0x3dedd110,
0x3deb2014,0x3de8694b,0x3de5acc6,0x3de2ea97,0x3de022ce,0x3ddd557d,0x3dda82b6,0x3dd7aa8b,0xbe000000,0xbe01401d,0xbe027d0a,0xbe03b6bf,
0xbe04ed34,0xbe062061,0xbe07503f,0xbe087cc6,0xbe09a5ef,0xbe0acbb3,0xbe0bee0a,0xbe0d0ced,0xbe0e2855,0xbe0f403b,0xbe105499,0xbe116566,
0x3dd4cd0e,0x3dd1ea50,0x3dcf0265,0x3dcc155e,0x3dc9234e,0x3dc62c48,0x3dc3305d,0x3dc02fa2,0x3dbd2a29,0x3dba2005,0x3db71149,0x3db3fe09,
0x3db0e658,0x3dadca4a,0x3daaa9f2,0x3da78564,0xbe12729e,0xbe137c38,0xbe14822f,0xbe15847b,0xbe168317,0xbe177dfd,0xbe187526,0xbe19688b,
0xbe1a5828,0xbe1b43f5,0xbe1c2bee,0xbe1d100d,0xbe1df04b,0xbe1ecca4,0xbe1fa512,0xbe20798f,0x3da45cb4,0x3da12ff5,0x3d9dff3d,0x3d9aca9e,
0x3d97922e,0x3d945600,0x3d91162a,0x3d8dd2bf,0x3d8a8bd4,0x3d87417e,0x3d83f3d2,0x3d80a2e4,0x3d7a9d93,0x3d73ef2f,0x3d6d3ac6,0x3d668083,
0xbe214a17,0xbe2216a4,0xbe22df31,0xbe23a3b9,0xbe246438,0xbe2520a8,0xbe25d905,0xbe268d4b,0xbe273d75,0xbe27e97e,0xbe289162,0xbe29351e,
0xbe29d4ae,0xbe2a700c,0xbe2b0736,0xbe2b9a27,0x3d5fc090,0x3d58fb18,0x3d523045,0x3d4b6043,0x3d448b3c,0x3d3db15b,0x3d36d2cc,0x3d2fefbb,
0x3d290852,0x3d221cbd,0x3d1b2d29,0x3d1439c0,0x3d0d42af,0x3d064822,0x3cfe9488,0x3cf09285,0xbe2c28dd,0xbe2cb353,0xbe2d3986,0xbe2dbb73,
0xbe2e3917,0xbe2eb26e,0xbe2f2776,0xbe2f982b,0xbe30048c,0xbe306c94,0xbe30d042,0xbe312f94,0xbe318a86,0xbe31e116,0xbe323343,0xbe32810b,
0x3ce28a93,0x3cd47d09,0x3cc66a42,0x3cb85295,0x3caa365b,0x3c9c15ef,0x3c8df1a9,0x3c7f93c4,0x3c633de8,0x3c46e270,0x3c2a8210,0x3c0e1d7b,
0x3be36aca,0x3baa9501,0x3b637603,0x3ae378d2,0xbe32ca6b,0xbe330f61,0xbe334fed,0xbe338c0c,0xbe33c3bc,0xbe33f6fe,0xbe3425ce,0xbe34502d,
0xbe347619,0xbe349791,0xbe34b494,0xbe34cd21,0xbe34e139,0xbe34f0da,0xbe34fc04,0xbe3502b7,0x3e3504f3,0x3e34fc04,0x3e34e139,0x3e34b494,
0x3e347619,0x3e3425ce,0x3e33c3bc,0x3e334fed,0x3e32ca6b,0x3e323343,0x3e318a86,0x3e30d042,0x3e30048c,0x3e2f2776,0x3e2e3917,0x3e2d3986,
0,0xbb637603,0xbbe36aca,0xbc2a8210,0xbc633de8,0xbc8df1a9,0xbcaa365b,0xbcc66a42,0xbce28a93,0xbcfe9488,0xbd0d42af,0xbd1b2d29,
0xbd290852,0xbd36d2cc,0xbd448b3c,0xbd523045,0x3e2c28dd,0x3e2b0736,0x3e29d4ae,0x3e289162,0x3e273d75,0x3e25d905,0x3e246438,0x3e22df31,
0x3e214a17,0x3e1fa512,0x3e1df04b,0x3e1c2bee,0x3e1a5828,0x3e187526,0x3e168317,0x3e14822f,0xbd5fc090,0xbd6d3ac6,0xbd7a9d93,0xbd83f3d2,
0xbd8a8bd4,0xbd91162a,0xbd97922e,0xbd9dff3d,0xbda45cb4,0xbdaaa9f2,0xbdb0e658,0xbdb71149,0xbdbd2a29,0xbdc3305d,0xbdc9234e,0xbdcf0265,
0x3e12729e,0x3e105499,0x3e0e2855,0x3e0bee0a,0x3e09a5ef,0x3e07503f,0x3e04ed34,0x3e027d0a,0x3e000000,0x3dfaeca7,0x3df5c08b,0x3df07c2e,
0x3deb2014,0x3de5acc6,0x3de022ce,0x3dda82b6,0xbdd4cd0e,0xbdda82b6,0xbde022ce,0xbde5acc6,0xbdeb2014,0xbdf07c2e,0xbdf5c08b,0xbdfaeca7,
0xbe000000,0xbe027d0a,0xbe04ed34,0xbe07503f,0xbe09a5ef,0xbe0bee0a,0xbe0e2855,0xbe105499,0x3dd4cd0e,0x3dcf0265,0x3dc9234e,0x3dc3305d,
0x3dbd2a29,0x3db71149,0x3db0e658,0x3daaa9f2,0x3da45cb4,0x3d9dff3d,0x3d97922e,0x3d91162a,0x3d8a8bd4,0x3d83f3d2,0x3d7a9d93,0x3d6d3ac6,
0xbe12729e,0xbe14822f,0xbe168317,0xbe187526,0xbe1a5828,0xbe1c2bee,0xbe1df04b,0xbe1fa512,0xbe214a17,0xbe22df31,0xbe246438,0xbe25d905,
0xbe273d75,0xbe289162,0xbe29d4ae,0xbe2b0736,0x3d5fc090,0x3d523045,0x3d448b3c,0x3d36d2cc,0x3d290852,0x3d1b2d29,0x3d0d42af,0x3cfe9488,
0x3ce28a93,0x3cc66a42,0x3caa365b,0x3c8df1a9,0x3c633de8,0x3c2a8210,0x3be36aca,0x3b637603,0xbe2c28dd,0xbe2d3986,0xbe2e3917,0xbe2f2776,
0xbe30048c,0xbe30d042,0xbe318a86,0xbe323343,0xbe32ca6b,0xbe334fed,0xbe33c3bc,0xbe3425ce,0xbe347619,0xbe34b494,0xbe34e139,0xbe34fc04,
0,0xbb637603,0xbbe36aca,0xbc2a8210,0xbc633de8,0xbc8df1a9,0xbcaa365b,0xbcc66a42,0xbce28a93,0xbcfe9488,0xbd0d42af,0xbd1b2d29,
0xbd290852,0xbd36d2cc,0xbd448b3c,0xbd523045,0xbe3504f3,0xbe34fc04,0xbe34e139,0xbe34b494,0xbe347619,0xbe3425ce,0xbe33c3bc,0xbe334fed,
0xbe32ca6b,0xbe323343,0xbe318a86,0xbe30d042,0xbe30048c,0xbe2f2776,0xbe2e3917,0xbe2d3986,0xbd5fc090,0xbd6d3ac6,0xbd7a9d93,0xbd83f3d2,
0xbd8a8bd4,0xbd91162a,0xbd97922e,0xbd9dff3d,0xbda45cb4,0xbdaaa9f2,0xbdb0e658,0xbdb71149,0xbdbd2a29,0xbdc3305d,0xbdc9234e,0xbdcf0265,
0xbe2c28dd,0xbe2b0736,0xbe29d4ae,0xbe289162,0xbe273d75,0xbe25d905,0xbe246438,0xbe22df31,0xbe214a17,0xbe1fa512,0xbe1df04b,0xbe1c2bee,
0xbe1a5828,0xbe187526,0xbe168317,0xbe14822f,0xbdd4cd0e,0xbdda82b6,0xbde022ce,0xbde5acc6,0xbdeb2014,0xbdf07c2e,0xbdf5c08b,0xbdfaeca7,
0xbe000000,0xbe027d0a,0xbe04ed34,0xbe07503f,0xbe09a5ef,0xbe0bee0a,0xbe0e2855,0xbe105499,0xbe12729e,0xbe105499,0xbe0e2855,0xbe0bee0a,
0xbe09a5ef,0xbe07503f,0xbe04ed34,0xbe027d0a,0xbe000000,0xbdfaeca7,0xbdf5c08b,0xbdf07c2e,0xbdeb2014,0xbde5acc6,0xbde022ce,0xbdda82b6,
0xbe12729e,0xbe14822f,0xbe168317,0xbe187526,0xbe1a5828,0xbe1c2bee,0xbe1df04b,0xbe1fa512,0xbe214a17,0xbe22df31,0xbe246438,0xbe25d905,
0xbe273d75,0xbe289162,0xbe29d4ae,0xbe2b0736,0xbdd4cd0e,0xbdcf0265,0xbdc9234e,0xbdc3305d,0xbdbd2a29,0xbdb71149,0xbdb0e658,0xbdaaa9f2,
0xbda45cb4,0xbd9dff3d,0xbd97922e,0xbd91162a,0xbd8a8bd4,0xbd83f3d2,0xbd7a9d93,0xbd6d3ac6,0xbe2c28dd,0xbe2d3986,0xbe2e3917,0xbe2f2776,
0xbe30048c,0xbe30d042,0xbe318a86,0xbe323343,0xbe32ca6b,0xbe334fed,0xbe33c3bc,0xbe3425ce,0xbe347619,0xbe34b494,0xbe34e139,0xbe34fc04,
0xbd5fc090,0xbd523045,0xbd448b3c,0xbd36d2cc,0xbd290852,0xbd1b2d29,0xbd0d42af,0xbcfe9488,0xbce28a93,0xbcc66a42,0xbcaa365b,0xbc8df1a9,
0xbc633de8,0xbc2a8210,0xbbe36aca,0xbb637603,0,0,0,0,0,0,0,0,
0,0,0,0,0,0,0,0,0,0,0,0,
0,0,0,0,0,0,0,0,0,0,0,0,
0,0,0,0,0,0,0,0,0,0,0,0,
0,0,0,0,0,0,0,0,0,0,0,0,
0,0,0,0,0,0,0,0,0x350f,0x350f,0x350f,0x350f,
0x350f,0x31f33418,0x34d02e41,0x34d0ae41,0x350f,0x34d02e41,0x31f3b418,0xb1f3b418,0x350f,0x34d0ae41,0xb1f3b418,0xb1f33418,
0x350f,0x31f3b418,0xb4d02e41,0x34d02e41,0x350f,0xb50f,0x350f,0xb50f,0x350f,0xb1f3b418,0x34d02e41,0xb4d02e41,
0x350f,0xb4d0ae41,0x31f3b418,0x31f33418,0x350f,0xb4d02e41,0xb1f3b418,0x31f3b418,0x350f,0xb1f33418,0xb4d02e41,0xb4d0ae41,
0,0,0,0,0,0,0,0,0,0,0,0,
0,0,0,0,0,0,0,0,0,0,0,0,
0x350f,0x350f,0x350f,0x350f,0x31f3b418,0xb50f,0xb1f3b418,0xb4d0ae41,0xb4d02e41,0x350f,0x34d02e41,0x31f3b418,
0x34d02e41,0xb50f,0xb4d02e41,0x31f33418,0xb1f3b418,0x350f,0x31f3b418,0xb4d02e41,0x350f,0xb50f,0x350f,0xb50f,
0x31f3b418,0x350f,0xb1f3b418,0x34d02e41,0xb4d02e41,0xb50f,0x34d02e41,0xb1f33418,0x34d02e41,0x350f,0xb4d02e41,0xb1f3b418,
0xb1f3b418,0xb50f,0x31f3b418,0x34d0ae41,0,0,0,0,0,0,0,0,
0,0,0,0,0,0,0,0,0,0,0,0,
0,0,0,0,0x350f,0x350f,0,0,0xb4d02e41,0xb1f33418,0,0,
0xb1f3b418,0xb4d02e41,0,0,0x31f3b418,0xb4d0ae41,0,0,0x34d02e41,0xb1f3b418,0,0,
0x350f,0xb50f,0,0,0xb4d02e41,0x31f3b418,0,0,0xb1f3b418,0x34d0ae41,0,0,
0x31f3b418,0x34d02e41,0,0,0x34d02e41,0x31f33418,0,0,0,0,0,0,
0,0,0,0,0,0,0,0,0,0,0,0,
0,0,0,0,0,0,0,0,0,0,0,0,
0,0,0,0,0,0,0,0,0,0,0,0,
0,0,0,0,0,0,0,0,0,0,0,0,
0,0,0,0,0,0,0,0,0,0,0,0,
0,0,0,0,0,0,0,0,0,0,0,0,
0,0,0,0,0,0,0,0,0,0,0,0,
0x350f0000,0x350f0000,0x350f0000,0x350f0000,0x350f0000,0x3418b1f3,0x2e41b4d0,0xae41b4d0,0x350f0000,0x2e41b4d0,0xb418b1f3,0xb41831f3,
0x350f0000,0xae41b4d0,0xb41831f3,0x341831f3,0x350f0000,0xb418b1f3,0x2e4134d0,0x2e41b4d0,0x350f0000,0xb50f0000,0x350f0000,0xb50f0000,
0x350f0000,0xb41831f3,0x2e41b4d0,0x2e4134d0,0x350f0000,0xae4134d0,0xb418b1f3,0x3418b1f3,0x350f0000,0x2e4134d0,0xb41831f3,0xb418b1f3,
0x350f0000,0x341831f3,0x2e4134d0,0xae4134d0,0,0,0,0,0,0,0,0,
0,0,0,0,0,0,0,0,0,0,0,0,
0,0,0,0,0x350f0000,0x350f0000,0x350f0000,0x350f0000,0xb418b1f3,0xb50f0000,0xb41831f3,0xae4134d0,
0x2e4134d0,0x350f0000,0x2e41b4d0,0xb418b1f3,0x2e41b4d0,0xb50f0000,0x2e4134d0,0x3418b1f3,0xb41831f3,0x350f0000,0xb418b1f3,0x2e4134d0,
0x350f0000,0xb50f0000,0x350f0000,0xb50f0000,0xb418b1f3,0x350f0000,0xb41831f3,0x2e41b4d0,0x2e4134d0,0xb50f0000,0x2e41b4d0,0x341831f3,
0x2e41b4d0,0x350f0000,0x2e4134d0,0xb41831f3,0xb41831f3,0xb50f0000,0xb418b1f3,0xae41b4d0,0,0,0,0,
0,0,0,0,0,0,0,0,0,0,0,0,
0,0,0,0,0,0,0,0,0x350f0000,0x350f0000,0,0,
0x2e4134d0,0x341831f3,0,0,0xb41831f3,0x2e4134d0,0,0,0xb418b1f3,0xae4134d0,0,0,
0x2e41b4d0,0xb41831f3,0,0,0x350f0000,0xb50f0000,0,0,0x2e4134d0,0xb418b1f3,0,0,
0xb41831f3,0xae41b4d0,0,0,0xb418b1f3,0x2e41b4d0,0,0,0x2e41b4d0,0x3418b1f3,0,0,
0,0,0,0,0,0,0,0,0,0,0,0,
0,0,0,0,0,0,0,0,0,0,0,0,
0,0,0,0,0,0,0,0,0,0,0,0,
0,0,0,0,0,0,0,0,0,0,0,0,
0,0,0,0,0,0,0,0,0,0,0,0,
0,0,0,0,0,0,0,0,0,0,0,0,
0,0,0,0,0,0,0,0,0,0,0,0,
0,0,0,0,0x3400,0x3400,0x3400,0x3400,0x3400,0x2e1f3364,0x31a831a8,0x33642e1f,
0x3400,0x31a831a8,0x34000000,0x31a8b1a8,0x3400,0x33642e1f,0x31a8b1a8,0xae1fb364,0x3400,0x34000000,0xb400,0xb4000000,
0x3400,0x3364ae1f,0xb1a8b1a8,0xae1f3364,0x3400,0x31a8b1a8,0xb4000000,0x31a831a8,0x3400,0x2e1fb364,0xb1a831a8,0x3364ae1f,
0x3400,0xb400,0x3400,0xb400,0x3400,0xae1fb364,0x31a831a8,0xb364ae1f,0x3400,0xb1a8b1a8,0x34000000,0xb1a831a8,
0x3400,0xb364ae1f,0x31a8b1a8,0x2e1f3364,0x3400,0xb4000000,0xb400,0x34000000,0x3400,0xb3642e1f,0xb1a8b1a8,0x2e1fb364,
0x3400,0xb1a831a8,0xb4000000,0xb1a8b1a8,0x3400,0xae1f3364,0xb1a831a8,0xb3642e1f,0x3400,0x3400,0x3400,0x3400,
0x34000000,0x3364ae1f,0x31a8b1a8,0x2e1fb364,0xb400,0xb1a8b1a8,0xb4000000,0xb1a831a8,0xb4000000,0xae1f3364,0x31a831a8,0x3364ae1f,
0x3400,0x34000000,0xb400,0xb4000000,0x34000000,0xae1fb364,0xb1a831a8,0x33642e1f,0xb400,0xb1a831a8,0x34000000,0xb1a8b1a8,
0xb4000000,0x33642e1f,0xb1a8b1a8,0x2e1f3364,0x3400,0xb400,0x3400,0xb400,0x34000000,0xb3642e1f,0x31a8b1a8,0xae1f3364,
0xb400,0x31a831a8,0xb4000000,0x31a8b1a8,0xb4000000,0x2e1fb364,0x31a831a8,0xb3642e1f,0x3400,0xb4000000,0xb400,0x34000000,
0x34000000,0x2e1f3364,0xb1a831a8,0xb364ae1f,0xb400,0x31a8b1a8,0x34000000,0x31a831a8,0xb4000000,0xb364ae1f,0xb1a8b1a8,0xae1fb364,
0x3400,0x3400,0x3400,0x3400,0xb400,0xae1fb364,0xb1a8b1a8,0xb364ae1f,0x3400,0x31a831a8,0x34000000,0x31a8b1a8,
0xb400,0xb364ae1f,0xb1a831a8,0x2e1f3364,0x3400,0x34000000,0xb400,0xb4000000,0xb400,0xb3642e1f,0x31a831a8,0x2e1fb364,
0x3400,0x31a8b1a8,0xb4000000,0x31a831a8,0xb400,0xae1f3364,0x31a8b1a8,0xb3642e1f,0x3400,0xb400,0x3400,0xb400,
0xb400,0x2e1f3364,0xb1a8b1a8,0x33642e1f,0x3400,0xb1a8b1a8,0x34000000,0xb1a831a8,0xb400,0x33642e1f,0xb1a831a8,0xae1fb364,
0x3400,0xb4000000,0xb400,0x34000000,0xb400,0x3364ae1f,0x31a831a8,0xae1f3364,0x3400,0xb1a831a8,0xb4000000,0xb1a8b1a8,
0xb400,0x2e1fb364,0x31a8b1a8,0x3364ae1f,0x3400,0x3400,0x3400,0x3400,0xb4000000,0xb3642e1f,0xb1a831a8,0xae1f3364,
0xb400,0xb1a8b1a8,0xb4000000,0xb1a831a8,0x34000000,0x2e1fb364,0xb1a8b1a8,0xb3642e1f,0x3400,0x34000000,0xb400,0xb4000000,
0xb4000000,0x2e1f3364,0x31a8b1a8,0xb364ae1f,0xb400,0xb1a831a8,0x34000000,0xb1a8b1a8,0x34000000,0xb364ae1f,0x31a831a8,0xae1fb364,
0x3400,0xb400,0x3400,0xb400,0xb4000000,0x3364ae1f,0xb1a831a8,0x2e1fb364,0xb400,0x31a831a8,0xb4000000,0x31a8b1a8,
0x34000000,0xae1f3364,0xb1a8b1a8,0x3364ae1f,0x3400,0xb4000000,0xb400,0x34000000,0xb4000000,0xae1fb364,0x31a8b1a8,0x33642e1f,
0xb400,0x31a8b1a8,0x34000000,0x31a831a8,0x34000000,0x33642e1f,0x31a831a8,0x2e1f3364,0x34000000,0x34000000,0x34000000,0x34000000,
0x34000000,0x3364ae1f,0x31a8b1a8,0x2e1fb364,0x34000000,0x31a8b1a8,0xb400,0xb1a8b1a8,0x34000000,0x2e1fb364,0xb1a8b1a8,0xb3642e1f,
0x34000000,0xb400,0xb4000000,0x3400,0x34000000,0xae1fb364,0xb1a831a8,0x33642e1f,0x34000000,0xb1a8b1a8,0x3400,0x31a8b1a8,
0x34000000,0xb364ae1f,0x31a831a8,0xae1fb364,0x34000000,0xb4000000,0x34000000,0xb4000000,0x34000000,0xb3642e1f,0x31a8b1a8,0xae1f3364,
0x34000000,0xb1a831a8,0xb400,0x31a831a8,0x34000000,0xae1f3364,0xb1a8b1a8,0x3364ae1f,0x34000000,0x3400,0xb4000000,0xb400,
0x34000000,0x2e1f3364,0xb1a831a8,0xb364ae1f,0x34000000,0x31a831a8,0x3400,0xb1a831a8,0x34000000,0x33642e1f,0x31a831a8,0x2e1f3364,
0x34000000,0x34000000,0x34000000,0x34000000,0xb400,0xae1fb364,0xb1a8b1a8,0xb364ae1f,0xb4000000,0xb1a831a8,0x3400,0x31a831a8,
0x3400,0x33642e1f,0x31a8b1a8,0xae1fb364,0x34000000,0xb400,0xb4000000,0x3400,0xb400,0xb3642e1f,0x31a831a8,0x2e1fb364,
0xb4000000,0x31a831a8,0xb400,0xb1a831a8,0x3400,0x2e1fb364,0xb1a831a8,0x3364ae1f,0x34000000,0xb4000000,0x34000000,0xb4000000,
0xb400,0x2e1f3364,0xb1a8b1a8,0x33642e1f,0xb4000000,0x31a8b1a8,0x3400,0xb1a8b1a8,0x3400,0xb364ae1f,0x31a8b1a8,0x2e1f3364,
0x34000000,0x3400,0xb4000000,0xb400,0xb400,0x3364ae1f,0x31a831a8,0xae1f3364,0xb4000000,0xb1a8b1a8,0xb400,0x31a8b1a8,
0x3400,0xae1f3364,0xb1a831a8,0xb3642e1f,0x34000000,0x34000000,0x34000000,0x34000000,0xb4000000,0xb3642e1f,0xb1a831a8,0xae1f3364,
0x34000000,0x31a8b1a8,0xb400,0xb1a8b1a8,0xb4000000,0xae1f3364,0x31a831a8,0x3364ae1f,0x34000000,0xb400,0xb4000000,0x3400,
0xb4000000,0x2e1f3364,0x31a8b1a8,0xb364ae1f,0x34000000,0xb1a8b1a8,0x3400,0x31a8b1a8,0xb4000000,0x33642e1f,0xb1a8b1a8,0x2e1f3364,
0x34000000,0xb4000000,0x34000000,0xb4000000,0xb4000000,0x3364ae1f,0xb1a831a8,0x2e1fb364,0x34000000,0xb1a831a8,0xb400,0x31a831a8,
0xb4000000,0x2e1fb364,0x31a831a8,0xb3642e1f,0x34000000,0x3400,0xb4000000,0xb400,0xb4000000,0xae1fb364,0x31a8b1a8,0x33642e1f,
0x34000000,0x31a831a8,0x3400,0xb1a831a8,0xb4000000,0xb364ae1f,0xb1a8b1a8,0xae1fb364,0x34000000,0x34000000,0x34000000,0x34000000,
0x3400,0x2e1f3364,0x31a831a8,0x33642e1f,0xb4000000,0xb1a831a8,0x3400,0x31a831a8,0xb400,0xb364ae1f,0xb1a831a8,0x2e1f3364,
0x34000000,0xb400,0xb4000000,0x3400,0x3400,0x3364ae1f,0xb1a8b1a8,0xae1f3364,0xb4000000,0x31a831a8,0xb400,0xb1a831a8,
0xb400,0xae1f3364,0x31a8b1a8,0xb3642e1f,0x34000000,0xb4000000,0x34000000,0xb4000000,0x3400,0xae1fb364,0x31a831a8,0xb364ae1f,
0xb4000000,0x31a8b1a8,0x3400,0xb1a8b1a8,0xb400,0x33642e1f,0xb1a831a8,0xae1fb364,0x34000000,0x3400,0xb4000000,0xb400,
0x3400,0xb3642e1f,0xb1a8b1a8,0x2e1fb364,0xb4000000,0xb1a8b1a8,0xb400,0x31a8b1a8,0xb400,0x2e1fb364,0x31a8b1a8,0x3364ae1f,
0x3f800000,0x3f800000,0x3f800000,0x3f800000,0x3f800000,0x3f7fcd79,0x3f7f35f9,0x3f7e39bc,0x3f800000,0x3f7f35f9,0x3f7cd925,0x3f78ed3c,
0x3f800000,0x3f7e39bc,0x3f78ed3c,0x3f702d4f,0x3f800000,0x3f7cd925,0x3f737871,0x3f641901,0x3f800000,0x3f7b14be,0x3f6c835e,0x3f54db31,
0x3f800000,0x3f78ed3c,0x3f641901,0x3f42a9f7,0x3f800000,0x3f766377,0x3f5a46a0,0x3f2dc5e3,0x3f800000,0x3f737871,0x3f4f1bbd,0x3f167918,
0x3f800000,0x3f702d4f,0x3f42a9f7,0x3efa2c90,0x3f800000,0x3f6c835e,0x3f3504f3,0x3ec3ef15,0x3f800000,0x3f687c12,0x3f26423a,0x3e8afa3e,
0x3f800000,0x3f641901,0x3f167918,0x3e20305b,0x3f800000,0x3f5f5be6,0x3f05c277,0x3d20cee7,0x3f800000,0x3f5a46a0,0x3ee87171,0xbda0af2a,
0x3f800000,0x3f54db31,0x3ec3ef15,0xbe47c5c2,0x3f800000,0x3f800000,0x3f800000,0x3f800000,0x3f7cd925,0x3f7b14be,0x3f78ed3c,0x3f766377,
0x3f737871,0x3f6c835e,0x3f641901,0x3f5a46a0,0x3f641901,0x3f54db31,0x3f42a9f7,0x3f2dc5e3,0x3f4f1bbd,0x3f3504f3,0x3f167918,0x3ee87171,
0x3f3504f3,0x3f0e39da,0x3ec3ef15,0x3e47c5c2,0x3f167918,0x3ec3ef15,0x3e20305b,0xbda0af2a,0x3ee87171,0x3e47c5c2,0xbda0af2a,0xbeb13641,
0x3e9e377a,0,0xbe9e377a,0xbf167918,0x3e20305b,0xbe47c5c2,0xbf05c277,0xbf490a88,0,0xbec3ef15,0xbf3504f3,0xbf6c835e,
0xbe20305b,0xbf0e39da,0xbf5a46a0,0xbf7e39bc,0xbe9e377a,0xbf3504f3,0xbf737871,0xbf7cd925,0xbee87171,0xbf54db31,0xbf7f35f9,0xbf687c12,
0xbf167918,0xbf6c835e,0xbf7cd925,0xbf42a9f7,0xbf3504f3,0xbf7b14be,0xbf6c835e,0xbf0e39da,0x3f800000,0x3f800000,0x3f800000,0x3f800000,
0x3f737871,0x3f702d4f,0x3f6c835e,0x3f687c12,0x3f4f1bbd,0x3f42a9f7,0x3f3504f3,0x3f26423a,0x3f167918,0x3efa2c90,0x3ec3ef15,0x3e8afa3e,
0x3e9e377a,0x3e20305b,0,0xbe20305b,0,0xbe47c5c2,0xbec3ef15,0xbf0e39da,0xbe9e377a,0xbf05c277,0xbf3504f3,0xbf5a46a0,
0xbf167918,0xbf490a88,0xbf6c835e,0xbf7e39bc,0xbf4f1bbd,0xbf737871,0xbf800000,0xbf737871,0xbf737871,0xbf7fcd79,0xbf6c835e,0xbf3bfc8f,
0xbf800000,0xbf6c835e,0xbf3504f3,0xbec3ef15,0xbf737871,0xbf3bfc8f,0xbec3ef15,0x3d20cee7,0xbf4f1bbd,0xbee87171,0,0x3ee87171,
0xbf167918,0xbdf0b772,0x3ec3ef15,0x3f490a88,0xbe9e377a,0x3e6f0c4d,0x3f3504f3,0x3f78ed3c,0,0x3f0e39da,0x3f6c835e,0x3f7b14be,
0x3f800000,0x3f800000,0x3f800000,0x3f800000,0x3f641901,0x3f5f5be6,0x3f5a46a0,0x3f54db31,0x3f167918,0x3f05c277,0x3ee87171,0x3ec3ef15,
0x3e20305b,0x3d20cee7,0xbda0af2a,0xbe47c5c2,0xbe9e377a,0xbee87171,0xbf167918,0xbf3504f3,0xbf3504f3,0xbf54db31,0xbf6c835e,0xbf7b14be,
0xbf737871,0xbf7f35f9,0xbf7cd925,0xbf6c835e,0xbf7cd925,0xbf687c12,0xbf42a9f7,0xbf0e39da,0xbf4f1bbd,0xbf167918,0xbe9e377a,0,
0xbee87171,0xbdf0b772,0x3e6f0c4d,0x3f0e39da,0,0x3ec3ef15,0x3f3504f3,0x3f6c835e,0x3ee87171,0x3f490a88,0x3f78ed3c,0x3f7b14be,
0x3f4f1bbd,0x3f7cd925,0x3f737871,0x3f3504f3,0x3f7cd925,0x3f702d4f,0x3f26423a,0x3e47c5c2,0x3f737871,0x3f26423a,0x3e20305b,0xbec3ef15,
0x3f3504f3,0x3e47c5c2,0xbec3ef15,0xbf54db31,0,0,0,0,0,0xbd20cee7,0xbda0af2a,0xbdf0b772,
0,0xbda0af2a,0xbe20305b,0xbe6f0c4d,0,0xbdf0b772,0xbe6f0c4d,0xbeb13641,0,0xbe20305b,0xbe9e377a,0xbee87171,
0,0xbe47c5c2,0xbec3ef15,0xbf0e39da,0,0xbe6f0c4d,0xbee87171,0xbf26423a,0,0xbe8afa3e,0xbf05c277,0xbf3bfc8f,
0,0xbe9e377a,0xbf167918,0xbf4f1bbd,0,0xbeb13641,0xbf26423a,0xbf5f5be6,0,0xbec3ef15,0xbf3504f3,0xbf6c835e,
0,0xbed65a92,0xbf42a9f7,0xbf766377,0,0xbee87171,0xbf4f1bbd,0xbf7cd925,0,0xbefa2c90,0xbf5a46a0,0xbf7fcd79,
0,0xbf05c277,0xbf641901,0xbf7f35f9,0,0xbf0e39da,0xbf6c835e,0xbf7b14be,0,0,0,0,
0xbe20305b,0xbe47c5c2,0xbe6f0c4d,0xbe8afa3e,0xbe9e377a,0xbec3ef15,0xbee87171,0xbf05c277,0xbee87171,0xbf0e39da,0xbf26423a,0xbf3bfc8f,
0xbf167918,0xbf3504f3,0xbf4f1bbd,0xbf641901,0xbf3504f3,0xbf54db31,0xbf6c835e,0xbf7b14be,0xbf4f1bbd,0xbf6c835e,0xbf7cd925,0xbf7f35f9,
0xbf641901,0xbf7b14be,0xbf7f35f9,0xbf702d4f,0xbf737871,0xbf800000,0xbf737871,0xbf4f1bbd,0xbf7cd925,0xbf7b14be,0xbf5a46a0,0xbf1e7cf1,
0xbf800000,0xbf6c835e,0xbf3504f3,0xbec3ef15,0xbf7cd925,0xbf54db31,0xbf05c277,0xbdf0b772,0xbf737871,0xbf3504f3,0xbe9e377a,0x3e20305b,
0xbf641901,0xbf0e39da,0xbda0af2a,0x3ed65a92,0xbf4f1bbd,0xbec3ef15,0x3e20305b,0x3f26423a,0xbf3504f3,0xbe47c5c2,0x3ec3ef15,0x3f54db31,
0,0,0,0,0xbe9e377a,0xbeb13641,0xbec3ef15,0xbed65a92,0xbf167918,0xbf26423a,0xbf3504f3,0xbf42a9f7,
0xbf4f1bbd,0xbf5f5be6,0xbf6c835e,0xbf766377,0xbf737871,0xbf7cd925,0xbf800000,0xbf7cd925,0xbf800000,0xbf7b14be,0xbf6c835e,0xbf54db31,
0xbf737871,0xbf5a46a0,0xbf3504f3,0xbf05c277,0xbf4f1bbd,0xbf1e7cf1,0xbec3ef15,0xbdf0b772,0xbf167918,0xbe9e377a,0,0x3e9e377a,
0xbe9e377a,0x3d20cee7,0x3ec3ef15,0x3f2dc5e3,0,0x3ec3ef15,0x3f3504f3,0x3f6c835e,0x3e9e377a,0x3f2dc5e3,0x3f6c835e,0x3f7fcd79,
0x3f167918,0x3f641901,0x3f800000,0x3f641901,0x3f4f1bbd,0x3f7e39bc,0x3f6c835e,0x3f1e7cf1,0x3f737871,0x3f78ed3c,0x3f3504f3,0x3e6f0c4d,
0x3f800000,0x3f54db31,0x3ec3ef15,0xbe47c5c2,0,0,0,0,0xbee87171,0xbefa2c90,0xbf05c277,0xbf0e39da,
0xbf4f1bbd,0xbf5a46a0,0xbf641901,0xbf6c835e,0xbf7cd925,0xbf7fcd79,0xbf7f35f9,0xbf7b14be,0xbf737871,0xbf641901,0xbf4f1bbd,0xbf3504f3,
0xbf3504f3,0xbf0e39da,0xbec3ef15,0xbe47c5c2,0xbe9e377a,0xbda0af2a,0x3e20305b,0x3ec3ef15,0x3e20305b,0x3ed65a92,0x3f26423a,0x3f54db31,
0x3f167918,0x3f4f1bbd,0x3f737871,0x3f800000,0x3f641901,0x3f7e39bc,0x3f78ed3c,0x3f54db31,0x3f800000,0x3f6c835e,0x3f3504f3,0x3ec3ef15,
0x3f641901,0x3f1e7cf1,0x3e6f0c4d,0xbe47c5c2,0x3f167918,0x3e20305b,0xbe9e377a,0xbf3504f3,0x3e20305b,0xbeb13641,0xbf42a9f7,0xbf7b14be,
0xbe9e377a,0xbf42a9f7,0xbf7cd925,0xbf6c835e,0xbf3504f3,0xbf7b14be,0xbf6c835e,0xbf0e39da,0x3400,0x3400,0x3400,0x3400,
0x3400,0xae1f3364,0xb1a831a8,0xb3642e1f,0x3400,0xb1a831a8,0xb4000000,0xb1a8b1a8,0x3400,0xb3642e1f,0xb1a8b1a8,0x2e1fb364,
0x3400,0xb4000000,0xb400,0x34000000,0x3400,0xb364ae1f,0x31a8b1a8,0x2e1f3364,0x3400,0xb1a8b1a8,0x34000000,0xb1a831a8,
0x3400,0xae1fb364,0x31a831a8,0xb364ae1f,0x3400,0xb400,0x3400,0xb400,0x3400,0x2e1fb364,0xb1a831a8,0x3364ae1f,
0x3400,0x31a8b1a8,0xb4000000,0x31a831a8,0x3400,0x3364ae1f,0xb1a8b1a8,0xae1f3364,0x3400,0x34000000,0xb400,0xb4000000,
0x3400,0x33642e1f,0x31a8b1a8,0xae1fb364,0x3400,0x31a831a8,0x34000000,0x31a8b1a8,0x3400,0x2e1f3364,0x31a831a8,0x33642e1f,
0x3400,0x3400,0x3400,0x3400,0xb4000000,0xb364ae1f,0xb1a8b1a8,0xae1fb364,0xb400,0x31a8b1a8,0x34000000,0x31a831a8,
0x34000000,0x2e1f3364,0xb1a831a8,0xb364ae1f,0x3400,0xb4000000,0xb400,0x34000000,0xb4000000,0x2e1fb364,0x31a831a8,0xb3642e1f,
0xb400,0x31a831a8,0xb4000000,0x31a8b1a8,0x34000000,0xb3642e1f,0x31a8b1a8,0xae1f3364,0x3400,0xb400,0x3400,0xb400,
0xb4000000,0x33642e1f,0xb1a8b1a8,0x2e1f3364,0xb400,0xb1a831a8,0x34000000,0xb1a8b1a8,0x34000000,0xae1fb364,0xb1a831a8,0x33642e1f,
0x3400,0x34000000,0xb400,0xb4000000,0xb4000000,0xae1f3364,0x31a831a8,0x3364ae1f,0xb400,0xb1a8b1a8,0xb4000000,0xb1a831a8,
0x34000000,0x3364ae1f,0x31a8b1a8,0x2e1fb364,0x3400,0x3400,0x3400,0x3400,0xb400,0x2e1fb364,0x31a8b1a8,0x3364ae1f,
0x3400,0xb1a831a8,0xb4000000,0xb1a8b1a8,0xb400,0x3364ae1f,0x31a831a8,0xae1f3364,0x3400,0xb4000000,0xb400,0x34000000,
0xb400,0x33642e1f,0xb1a831a8,0xae1fb364,0x3400,0xb1a8b1a8,0x34000000,0xb1a831a8,0xb400,0x2e1f3364,0xb1a8b1a8,0x33642e1f,
0x3400,0xb400,0x3400,0xb400,0xb400,0xae1f3364,0x31a8b1a8,0xb3642e1f,0x3400,0x31a8b1a8,0xb4000000,0x31a831a8,
0xb400,0xb3642e1f,0x31a831a8,0x2e1fb364,0x3400,0x34000000,0xb400,0xb4000000,0xb400,0xb364ae1f,0xb1a831a8,0x2e1f3364,
0x3400,0x31a831a8,0x34000000,0x31a8b1a8,0xb400,0xae1fb364,0xb1a8b1a8,0xb364ae1f,0x3400,0x3400,0x3400,0x3400,
0x34000000,0x33642e1f,0x31a831a8,0x2e1f3364,0xb400,0x31a8b1a8,0x34000000,0x31a831a8,0xb4000000,0xae1fb364,0x31a8b1a8,0x33642e1f,
0x3400,0xb4000000,0xb400,0x34000000,0x34000000,0xae1f3364,0xb1a8b1a8,0x3364ae1f,0xb400,0x31a831a8,0xb4000000,0x31a8b1a8,
0xb4000000,0x3364ae1f,0xb1a831a8,0x2e1fb364,0x3400,0xb400,0x3400,0xb400,0x34000000,0xb364ae1f,0x31a831a8,0xae1fb364,
0xb400,0xb1a831a8,0x34000000,0xb1a8b1a8,0xb4000000,0x2e1f3364,0x31a8b1a8,0xb364ae1f,0x3400,0x34000000,0xb400,0xb4000000,
0x34000000,0x2e1fb364,0xb1a8b1a8,0xb3642e1f,0xb400,0xb1a8b1a8,0xb4000000,0xb1a831a8,0xb4000000,0xb3642e1f,0xb1a831a8,0xae1f3364,
0x34000000,0x34000000,0x34000000,0x34000000,0x34000000,0x33642e1f,0x31a831a8,0x2e1f3364,0x34000000,0x31a831a8,0x3400,0xb1a831a8,
0x34000000,0x2e1f3364,0xb1a831a8,0xb364ae1f,0x34000000,0x3400,0xb4000000,0xb400,0x34000000,0xae1f3364,0xb1a8b1a8,0x3364ae1f,
0x34000000,0xb1a831a8,0xb400,0x31a831a8,0x34000000,0xb3642e1f,0x31a8b1a8,0xae1f3364,0x34000000,0xb4000000,0x34000000,0xb4000000,
0x34000000,0xb364ae1f,0x31a831a8,0xae1fb364,0x34000000,0xb1a8b1a8,0x3400,0x31a8b1a8,0x34000000,0xae1fb364,0xb1a831a8,0x33642e1f,
0x34000000,0xb400,0xb4000000,0x3400,0x34000000,0x2e1fb364,0xb1a8b1a8,0xb3642e1f,0x34000000,0x31a8b1a8,0xb400,0xb1a8b1a8,
0x34000000,0x3364ae1f,0x31a8b1a8,0x2e1fb364,0x34000000,0x34000000,0x34000000,0x34000000,0x3400,0xae1f3364,0xb1a831a8,0xb3642e1f,
0xb4000000,0xb1a8b1a8,0xb400,0x31a8b1a8,0xb400,0x3364ae1f,0x31a831a8,0xae1f3364,0x34000000,0x3400,0xb4000000,0xb400,
0x3400,0xb364ae1f,0x31a8b1a8,0x2e1f3364,0xb4000000,0x31a8b1a8,0x3400,0xb1a8b1a8,0xb400,0x2e1f3364,0xb1a8b1a8,0x33642e1f,
0x34000000,0xb4000000,0x34000000,0xb4000000,0x3400,0x2e1fb364,0xb1a831a8,0x3364ae1f,0xb4000000,0x31a831a8,0xb400,0xb1a831a8,
0xb400,0xb3642e1f,0x31a831a8,0x2e1fb364,0x34000000,0xb400,0xb4000000,0x3400,0x3400,0x33642e1f,0x31a8b1a8,0xae1fb364,
0xb4000000,0xb1a831a8,0x3400,0x31a831a8,0xb400,0xae1fb364,0xb1a8b1a8,0xb364ae1f,0x34000000,0x34000000,0x34000000,0x34000000,
0xb4000000,0xb364ae1f,0xb1a8b1a8,0xae1fb364,0x34000000,0x31a831a8,0x3400,0xb1a831a8,0xb4000000,0xae1fb364,0x31a8b1a8,0x33642e1f,
0x34000000,0x3400,0xb4000000,0xb400,0xb4000000,0x2e1fb364,0x31a831a8,0xb3642e1f,0x34000000,0xb1a831a8,0xb400,0x31a831a8,
0xb4000000,0x3364ae1f,0xb1a831a8,0x2e1fb364,0x34000000,0xb4000000,0x34000000,0xb4000000,0xb4000000,0x33642e1f,0xb1a8b1a8,0x2e1f3364,
0x34000000,0xb1a8b1a8,0x3400,0x31a8b1a8,0xb4000000,0x2e1f3364,0x31a8b1a8,0xb364ae1f,0x34000000,0xb400,0xb4000000,0x3400,
0xb4000000,0xae1f3364,0x31a831a8,0x3364ae1f,0x34000000,0x31a8b1a8,0xb400,0xb1a8b1a8,0xb4000000,0xb3642e1f,0xb1a831a8,0xae1f3364,
0x34000000,0x34000000,0x34000000,0x34000000,0xb400,0x2e1fb364,0x31a8b1a8,0x3364ae1f,0xb4000000,0xb1a8b1a8,0xb400,0x31a8b1a8,
0x3400,0xb3642e1f,0xb1a8b1a8,0x2e1fb364,0x34000000,0x3400,0xb4000000,0xb400,0xb400,0x33642e1f,0xb1a831a8,0xae1fb364,
0xb4000000,0x31a8b1a8,0x3400,0xb1a8b1a8,0x3400,0xae1fb364,0x31a831a8,0xb364ae1f,0x34000000,0xb4000000,0x34000000,0xb4000000,
0xb400,0xae1f3364,0x31a8b1a8,0xb3642e1f,0xb4000000,0x31a831a8,0xb400,0xb1a831a8,0x3400,0x3364ae1f,0xb1a8b1a8,0xae1f3364,
0x34000000,0xb400,0xb4000000,0x3400,0xb400,0xb364ae1f,0xb1a831a8,0x2e1f3364,0xb4000000,0xb1a831a8,0x3400,0x31a831a8,
0x3400,0x2e1f3364,0x31a831a8,0x33642e1f,0x350f,0x350f,0x350f,0x350f,0x350f,0xb1f33418,0xb4d02e41,0xb4d0ae41,
0x350f,0xb4d02e41,0xb1f3b418,0x31f3b418,0x350f,0xb4d0ae41,0x31f3b418,0x31f33418,0x350f,0xb1f3b418,0x34d02e41,0xb4d02e41,
0x350f,0xb50f,0x350f,0xb50f,0x350f,0x31f3b418,0xb4d02e41,0x34d02e41,0x350f,0x34d0ae41,0xb1f3b418,0xb1f33418,
0x350f,0x34d02e41,0x31f3b418,0xb1f3b418,0x350f,0x31f33418,0x34d02e41,0x34d0ae41,0,0,0,0,
0,0,0,0,0,0,0,0,0,0,0,0,
0,0,0,0,0,0,0,0,0x350f,0x350f,0x350f,0x350f,
0xb1f3b418,0xb50f,0x31f3b418,0x34d0ae41,0x34d02e41,0x350f,0xb4d02e41,0xb1f3b418,0xb4d02e41,0xb50f,0x34d02e41,0xb1f33418,
0x31f3b418,0x350f,0xb1f3b418,0x34d02e41,0x350f,0xb50f,0x350f,0xb50f,0xb1f3b418,0x350f,0x31f3b418,0xb4d02e41,
0x34d02e41,0xb50f,0xb4d02e41,0x31f33418,0xb4d02e41,0x350f,0x34d02e41,0x31f3b418,0x31f3b418,0xb50f,0xb1f3b418,0xb4d0ae41,
0,0,0,0,0,0,0,0,0,0,0,0,
0,0,0,0,0,0,0,0,0,0,0,0,
0x350f,0x350f,0,0,0x34d02e41,0x31f33418,0,0,0x31f3b418,0x34d02e41,0,0,
0xb1f3b418,0x34d0ae41,0,0,0xb4d02e41,0x31f3b418,0,0,0x350f,0xb50f,0,0,
0x34d02e41,0xb1f3b418,0,0,0x31f3b418,0xb4d0ae41,0,0,0xb1f3b418,0xb4d02e41,0,0,
0xb4d02e41,0xb1f33418,0,0,0,0,0,0,0,0,0,0,
0,0,0,0,0,0,0,0,0,0,0,0,
0,0,0,0,0,0,0,0,0,0,0,0,
0,0,0,0,0,0,0,0,0,0,0,0,
0,0,0,0,0,0,0,0,0,0,0,0,
0,0,0,0,0,0,0,0,0,0,0,0,
0,0,0,0,0,0,0,0,0,0,0,0,
0,0,0,0,0,0,0,0,0x350f0000,0x350f0000,0x350f0000,0x350f0000,
0x350f0000,0x341831f3,0x2e4134d0,0xae4134d0,0x350f0000,0x2e4134d0,0xb41831f3,0xb418b1f3,0x350f0000,0xae4134d0,0xb418b1f3,0x3418b1f3,
0x350f0000,0xb41831f3,0x2e41b4d0,0x2e4134d0,0x350f0000,0xb50f0000,0x350f0000,0xb50f0000,0x350f0000,0xb418b1f3,0x2e4134d0,0x2e41b4d0,
0x350f0000,0xae41b4d0,0xb41831f3,0x341831f3,0x350f0000,0x2e41b4d0,0xb418b1f3,0xb41831f3,0x350f0000,0x3418b1f3,0x2e41b4d0,0xae41b4d0,
0,0,0,0,0,0,0,0,0,0,0,0,
0,0,0,0,0,0,0,0,0,0,0,0,
0x350f0000,0x350f0000,0x350f0000,0x350f0000,0xb41831f3,0xb50f0000,0xb418b1f3,0xae41b4d0,0x2e41b4d0,0x350f0000,0x2e4134d0,0xb41831f3,
0x2e4134d0,0xb50f0000,0x2e41b4d0,0x341831f3,0xb418b1f3,0x350f0000,0xb41831f3,0x2e41b4d0,0x350f0000,0xb50f0000,0x350f0000,0xb50f0000,
0xb41831f3,0x350f0000,0xb418b1f3,0x2e4134d0,0x2e41b4d0,0xb50f0000,0x2e4134d0,0x3418b1f3,0x2e4134d0,0x350f0000,0x2e41b4d0,0xb418b1f3,
0xb418b1f3,0xb50f0000,0xb41831f3,0xae4134d0,0,0,0,0,0,0,0,0,
0,0,0,0,0,0,0,0,0,0,0,0,
0,0,0,0,0x350f0000,0x350f0000,0,0,0x2e41b4d0,0x3418b1f3,0,0,
0xb418b1f3,0x2e41b4d0,0,0,0xb41831f3,0xae41b4d0,0,0,0x2e4134d0,0xb418b1f3,0,0,
0x350f0000,0xb50f0000,0,0,0x2e41b4d0,0xb41831f3,0,0,0xb418b1f3,0xae4134d0,0,0,
0xb41831f3,0x2e4134d0,0,0,0x2e4134d0,0x341831f3,0,0,0,0,0,0,
0,0,0,0,0,0,0,0,0,0,0,0,
0,0,0,0,0,0,0,0,0,0,0,0,
0,0,0,0,0,0,0,0,0,0,0,0,
0,0,0,0,0,0,0,0,0,0,0,0,
0,0,0,0,0,0,0,0,0,0,0,0,
0,0,0,0,0,0,0,0,0,0,0,0,
0,0,0,0,0,0,0,0,0,0,0,0,
0x3c003c00,0x3c003c00,0x4000,0x4000,0xbc003c00,0xbc003c00,0xbda83da8,0xbda83da8,0x3c003c00,0x3c003c00,0xc0000000,0xc0000000,
0xbc003c00,0xbc003c00,0xbda8bda8,0xbda8bda8,0x3c003c00,0x3c003c00,0xc000,0xc000,0xbc003c00,0xbc003c00,0x3da8bda8,0x3da8bda8,
0x3c003c00,0x3c003c00,0x40000000,0x40000000,0xbc003c00,0xbc003c00,0x3da83da8,0x3da83da8,0x3c003c00,0xbc00bc00,0x4000,0xc000,
0xbc003c00,0x3c00bc00,0xbda83da8,0x3da8bda8,0x3c003c00,0xbc00bc00,0xc0000000,0x40000000,0xbc003c00,0x3c00bc00,0xbda8bda8,0x3da83da8,
0x3c003c00,0xbc00bc00,0xc000,0x4000,0xbc003c00,0x3c00bc00,0x3da8bda8,0xbda83da8,0x3c003c00,0xbc00bc00,0x40000000,0xc0000000,
0xbc003c00,0x3c00bc00,0x3da83da8,0xbda8bda8,0x4000,0x4000,0x4000,0x4000,0xc0000000,0xc0000000,0xbda8bda8,0xbda8bda8,
0xc000,0xc000,0x40000000,0x40000000,0x40000000,0x40000000,0xbda83da8,0xbda83da8,0x4000,0x4000,0xc000,0xc000,
0xc0000000,0xc0000000,0x3da83da8,0x3da83da8,0xc000,0xc000,0xc0000000,0xc0000000,0x40000000,0x40000000,0x3da8bda8,0x3da8bda8,
0x4000,0xc000,0x4000,0xc000,0xc0000000,0x40000000,0xbda8bda8,0x3da83da8,0xc000,0x4000,0x40000000,0xc0000000,
0x40000000,0xc0000000,0xbda83da8,0x3da8bda8,0x4000,0xc000,0xc000,0x4000,0xc0000000,0x40000000,0x3da83da8,0xbda8bda8,
0xc000,0x4000,0xc0000000,0x40000000,0x40000000,0xc0000000,0x3da8bda8,0xbda83da8,0x4000,0x4000,0x4000,0x4000,
0xbda83da8,0x3da83da8,0xc0000000,0x40000000,0xc0000000,0x40000000,0xc000,0xc000,0xbda8bda8,0x3da8bda8,0x40000000,0xc0000000,
0xc000,0xc000,0x4000,0x4000,0x3da8bda8,0xbda8bda8,0xc0000000,0x40000000,0x40000000,0xc0000000,0xc000,0xc000,
0x3da83da8,0xbda83da8,0x40000000,0xc0000000,0xc0000000,0xc0000000,0xc0000000,0xc0000000,0xbda8bda8,0xbda83da8,0xc000,0x4000,
0xc000,0x4000,0x40000000,0x40000000,0x3da8bda8,0x3da83da8,0x4000,0xc000,0x40000000,0x40000000,0xc0000000,0xc0000000,
0x3da83da8,0x3da8bda8,0xc000,0x4000,0x4000,0xc000,0x40000000,0x40000000,0xbda83da8,0xbda8bda8,0x4000,0xc000,
0x4000,0x4000,0x4000,0x4000,0xbda8bda8,0x3da8bda8,0x4000,0xc000,0x40000000,0xc0000000,0x4000,0x4000,
0xbda83da8,0x3da83da8,0x4000,0xc000,0xc000,0xc000,0x4000,0x4000,0x3da83da8,0xbda83da8,0x4000,0xc000,
0xc0000000,0x40000000,0x4000,0x4000,0x3da8bda8,0xbda8bda8,0x4000,0xc000,0xc0000000,0xc0000000,0xc0000000,0xc0000000,
0x3da8bda8,0x3da83da8,0xc0000000,0x40000000,0x4000,0xc000,0xc0000000,0xc0000000,0xbda8bda8,0xbda83da8,0xc0000000,0x40000000,
0x40000000,0x40000000,0xc0000000,0xc0000000,0xbda83da8,0xbda8bda8,0xc0000000,0x40000000,0xc000,0x4000,0xc0000000,0xc0000000,
0x3da83da8,0x3da8bda8,0xc0000000,0x40000000,0x3c003c00,0x3c003c00,0x4000,0x4000,0xbc003c00,0xbc003c00,0xbda83da8,0xbda83da8,
0x3c003c00,0x3c003c00,0xc0000000,0xc0000000,0xbc003c00,0xbc003c00,0xbda8bda8,0xbda8bda8,0x3c003c00,0x3c003c00,0xc000,0xc000,
0xbc003c00,0xbc003c00,0x3da8bda8,0x3da8bda8,0x3c003c00,0x3c003c00,0x40000000,0x40000000,0xbc003c00,0xbc003c00,0x3da83da8,0x3da83da8,
0x3c003c00,0xbc00bc00,0x4000,0xc000,0xbc003c00,0x3c00bc00,0xbda83da8,0x3da8bda8,0x3c003c00,0xbc00bc00,0xc0000000,0x40000000,
0xbc003c00,0x3c00bc00,0xbda8bda8,0x3da83da8,0x3c003c00,0xbc00bc00,0xc000,0x4000,0xbc003c00,0x3c00bc00,0x3da8bda8,0xbda83da8,
0x3c003c00,0xbc00bc00,0x40000000,0xc0000000,0xbc003c00,0x3c00bc00,0x3da83da8,0xbda8bda8,0x4000,0x4000,0x4000,0x4000,
0xc0000000,0xc0000000,0xbda8bda8,0xbda8bda8,0xc000,0xc000,0x40000000,0x40000000,0x40000000,0x40000000,0xbda83da8,0xbda83da8,
0x4000,0x4000,0xc000,0xc000,0xc0000000,0xc0000000,0x3da83da8,0x3da83da8,0xc000,0xc000,0xc0000000,0xc0000000,
0x40000000,0x40000000,0x3da8bda8,0x3da8bda8,0x4000,0xc000,0x4000,0xc000,0xc0000000,0x40000000,0xbda8bda8,0x3da83da8,
0xc000,0x4000,0x40000000,0xc0000000,0x40000000,0xc0000000,0xbda83da8,0x3da8bda8,0x4000,0xc000,0xc000,0x4000,
0xc0000000,0x40000000,0x3da83da8,0xbda8bda8,0xc000,0x4000,0xc0000000,0x40000000,0x40000000,0xc0000000,0x3da8bda8,0xbda83da8,
0xc000,0xc000,0xc000,0xc000,0x3da8bda8,0xbda8bda8,0x40000000,0xc0000000,0x40000000,0xc0000000,0x4000,0x4000,
0x3da83da8,0xbda83da8,0xc0000000,0x40000000,0x4000,0x4000,0xc000,0xc000,0xbda83da8,0x3da83da8,0x40000000,0xc0000000,
0xc0000000,0x40000000,0x4000,0x4000,0xbda8bda8,0x3da8bda8,0xc0000000,0x40000000,0x40000000,0x40000000,0x40000000,0x40000000,
0x3da83da8,0x3da8bda8,0x4000,0xc000,0x4000,0xc000,0xc0000000,0xc0000000,0xbda83da8,0xbda8bda8,0xc000,0x4000,
0xc0000000,0xc0000000,0x40000000,0x40000000,0xbda8bda8,0xbda83da8,0x4000,0xc000,0xc000,0x4000,0xc0000000,0xc0000000,
0x3da8bda8,0x3da83da8,0xc000,0x4000,0xc000,0xc000,0xc000,0xc000,0x3da83da8,0xbda83da8,0xc000,0x4000,
0xc0000000,0x40000000,0xc000,0xc000,0x3da8bda8,0xbda8bda8,0xc000,0x4000,0x4000,0x4000,0xc000,0xc000,
0xbda8bda8,0x3da8bda8,0xc000,0x4000,0x40000000,0xc0000000,0xc000,0xc000,0xbda83da8,0x3da83da8,0xc000,0x4000,
0x40000000,0x40000000,0x40000000,0x40000000,0xbda83da8,0xbda8bda8,0x40000000,0xc0000000,0xc000,0x4000,0x40000000,0x40000000,
0x3da83da8,0x3da8bda8,0x40000000,0xc0000000,0xc0000000,0xc0000000,0x40000000,0x40000000,0x3da8bda8,0x3da83da8,0x40000000,0xc0000000,
0x4000,0xc000,0x40000000,0x40000000,0xbda8bda8,0xbda83da8,0x40000000,0xc0000000,0x3f800000,0x3f800000,0x3f800000,0x3f800000,
0x3f800000,0x3f800000,0x3f800000,0x3f800000,0x3f800000,0x3f800000,0x3f800000,0x3f800000,0x3f800000,0x3f800000,0x3f800000,0x3f800000,
0x3f800000,0x3f800000,0x3f800000,0x3f800000,0x3f800000,0x3f800000,0x3f800000,0x3f800000,0x3f800000,0x3f800000,0x3f800000,0x3f800000,
0x3f800000,0x3f800000,0x3f800000,0x3f800000,0x3f800000,0x3f800000,0x3f800000,0x3f800000,0x3f800000,0x3f800000,0x3f800000,0x3f800000,
0x3f800000,0x3f800000,0x3f800000,0x3f800000,0x3f800000,0x3f800000,0x3f800000,0x3f800000,0x3f800000,0x3f800000,0x3f800000,0x3f800000,
0x3f800000,0x3f800000,0x3f800000,0x3f800000,0x3f800000,0x3f800000,0x3f800000,0x3f800000,0x3f800000,0x3f800000,0x3f800000,0x3f800000,
0x3f800000,0x3f800000,0x3f800000,0x3f800000,0x3f800000,0x3f800000,0x3f800000,0x3f800000,0x3f800000,0x3f800000,0x3f800000,0x3f800000,
0x3f800000,0x3f800000,0x3f800000,0x3f800000,0x3f800000,0x3f800000,0x3f800000,0x3f800000,0x3f800000,0x3f800000,0x3f800000,0x3f800000,
0x3f800000,0x3f800000,0x3f800000,0x3f800000,0x3f800000,0x3f800000,0x3f800000,0x3f800000,0x3f800000,0x3f800000,0x3f800000,0x3f800000,
0x3f800000,0x3f800000,0x3f800000,0x3f800000,0x3f800000,0x3f800000,0x3f800000,0x3f800000,0x3f800000,0x3f800000,0x3f800000,0x3f800000,
0x3f800000,0x3f800000,0x3f800000,0x3f800000,0x3f800000,0x3f800000,0x3f800000,0x3f800000,0x3f800000,0x3f800000,0x3f800000,0x3f800000,
0x3f800000,0x3f800000,0x3f800000,0x3f800000,0x3f800000,0x3f800000,0x3f800000,0x3f800000,0x3f800000,0x3f800000,0x3f800000,0x3f800000,
0x3f800000,0x3f800000,0x3f800000,0x3f800000,0x3f800000,0x3f800000,0x3f800000,0x3f800000,0x3f800000,0x3f800000,0x3f800000,0x3f800000,
0x3f800000,0x3f800000,0x3f800000,0x3f800000,0x3f800000,0x3f800000,0x3f800000,0x3f800000,0x3f800000,0x3f800000,0x3f800000,0x3f800000,
0x3f800000,0x3f800000,0x3f800000,0x3f800000,0x3f800000,0x3f800000,0x3f800000,0x3f800000,0x3f800000,0x3f800000,0x3f800000,0x3f800000,
0x3f800000,0x3f800000,0x3f800000,0x3f800000,0x3f800000,0x3f800000,0x3f800000,0x3f800000,0x3f800000,0x3f800000,0x3f800000,0x3f800000,
0x3f800000,0x3f800000,0x3f800000,0x3f800000,0x3f800000,0x3f800000,0x3f800000,0x3f800000,0x3f800000,0x3f800000,0x3f800000,0x3f800000,
0x3f800000,0x3f800000,0x3f800000,0x3f800000,0x3f800000,0x3f800000,0x3f800000,0x3f800000,0x3f800000,0x3f800000,0x3f800000,0x3f800000,
0x3f800000,0x3f800000,0x3f800000,0x3f800000,0x3f800000,0x3f800000,0x3f800000,0x3f800000,0x3f800000,0x3f800000,0x3f800000,0x3f800000,
0x3f800000,0x3f800000,0x3f800000,0x3f800000,0x3f800000,0x3f800000,0x3f800000,0x3f800000,0x3f800000,0x3f800000,0x3f800000,0x3f800000,
0x3f800000,0x3f800000,0x3f800000,0x3f800000,0x3f800000,0x3f800000,0x3f800000,0x3f800000,0x3f800000,0x3f800000,0x3f800000,0x3f800000,
0x3f800000,0x3f800000,0x3f800000,0x3f800000,0x3f800000,0x3f800000,0x3f800000,0x3f800000,0x3f800000,0x3f800000,0x3f800000,0x3f800000,
0,0,0,0,0,0,0,0,0,0,0,0,
0,0,0,0,0,0,0,0,0,0,0,0,
0,0,0,0,0,0,0,0,0,0,0,0,
0,0,0,0,0,0,0,0,0,0,0,0,
0,0,0,0,0,0,0,0,0,0,0,0,
0,0,0,0,0,0,0,0,0,0,0,0,
0,0,0,0,0,0,0,0,0,0,0,0,
0,0,0,0,0,0,0,0,0,0,0,0,
0,0,0,0,0,0,0,0,0,0,0,0,
0,0,0,0,0,0,0,0,0,0,0,0,
0,0,0,0,0,0,0,0,0,0,0,0,
0,0,0,0,0,0,0,0,0,0,0,0,
0,0,0,0,0,0,0,0,0,0,0,0,
0,0,0,0,0,0,0,0,0,0,0,0,
0,0,0,0,0,0,0,0,0,0,0,0,
0,0,0,0,0,0,0,0,0,0,0,0,
0,0,0,0,0,0,0,0,0,0,0,0,
0,0,0,0,0,0,0,0,0,0,0,0,
0,0,0,0,0,0,0,0,0,0,0,0,
0,0,0,0,0,0,0,0,0,0,0,0,
0,0,0,0,0,0,0,0,0,0,0,0,
0,0,0,0,0x3f800000,0x3f7cd925,0x3f737871,0x3f641901,0x3f7ffcd7,0x3f7c7160,0x3f72ae9e,0x3f62f219,
0x3f7ff35e,0x3f7c0361,0x3f71decf,0x3f61c598,0x3f7fe394,0x3f7b8f2a,0x3f710908,0x3f609384,0x3f7fcd79,0x3f7b14be,0x3f702d4f,0x3f5f5be6,
0x3f7fb10f,0x3f7a9421,0x3f6f4ba8,0x3f5e1ec5,0x3f7f8e56,0x3f7a0d54,0x3f6e641a,0x3f5cdc29,0x3f7f654e,0x3f79805c,0x3f6d76aa,0x3f5b941a,
0x3f7f35f9,0x3f78ed3c,0x3f6c835e,0x3f5a46a0,0x3f7f0059,0x3f7853f8,0x3f6b8a3d,0x3f58f3c4,0x3f7ec46d,0x3f77b493,0x3f6a8b4b,0x3f579b8c,
0x3f7e8238,0x3f770f11,0x3f698690,0x3f563e03,0x3f7e39bc,0x3f766377,0x3f687c12,0x3f54db31,0x3f7deafa,0x3f75b1c9,0x3f676bd8,0x3f53731f,
0x3f7d95f4,0x3f74fa0b,0x3f6655e7,0x3f5205d4,0x3f7d3aac,0x3f743c41,0x3f653a48,0x3f50935b,0x3f4f1bbd,0x3f3504f3,0x3f167918,0x3ee87171,
0x3f4d9f02,0x3f333bc6,0x3f146eba,0x3ee3f40d,0x3f4c1d35,0x3f316e2c,0x3f1260b2,0x3edf7108,0x3f4a965e,0x3f2f9c32,0x3f104f0e,0x3edae880,
0x3f490a88,0x3f2dc5e3,0x3f0e39da,0x3ed65a92,0x3f4779bc,0x3f2beb4a,0x3f0c2124,0x3ed1c759,0x3f45e403,0x3f2a0c73,0x3f0a04f8,0x3ecd2ef3,
0x3f444969,0x3f28296a,0x3f07e565,0x3ec8917e,0x3f42a9f7,0x3f26423a,0x3f05c277,0x3ec3ef15,0x3f4105b8,0x3f2456f1,0x3f039c3d,0x3ebf47d7,
0x3f3f5cb5,0x3f226799,0x3f0172c3,0x3eba9be1,0x3f3daef9,0x3f207440,0x3efe8c2f,0x3eb5eb4f,0x3f3bfc8f,0x3f1e7cf1,0x3efa2c90,0x3eb13641,
0x3f3a4582,0x3f1c81b9,0x3ef5c6c5,0x3eac7cd4,0x3f3889dc,0x3f1a82a4,0x3ef15aea,0x3ea7bf25,0x3f36c9a9,0x3f187fc0,0x3eece91a,0x3ea2fd52,
0x3e9e377a,0x3e20305b,0,0xbe20305b,0x3e996dba,0x3e164083,0xbc20d8d2,0xbe2a1c3f,0x3e94a031,0x3e0c4cf7,0xbca0d6d6,0xbe3403f1,
0x3e8fcefe,0x3e0255f4,0xbcf13d4c,0xbe3de731,0x3e8afa3e,0x3df0b772,0xbd20cee7,0xbe47c5c2,0x3e862210,0x3ddcbd0d,0xbd48fb30,0xbe519f65,
0x3e814693,0x3dc8bd36,0xbd712283,0xbe5b73dc,0x3e78cfcc,0x3db4b86a,0xbd8ca1f2,0xbe6542e9,0x3e6f0c4d,0x3da0af2a,0xbda0af2a,0xbe6f0c4d,
0x3e6542e9,0x3d8ca1f2,0xbdb4b86a,0xbe78cfcc,0x3e5b73dc,0x3d712283,0xbdc8bd36,0xbe814693,0x3e519f65,0x3d48fb30,0xbddcbd0d,0xbe862210,
0x3e47c5c2,0x3d20cee7,0xbdf0b772,0xbe8afa3e,0x3e3de731,0x3cf13d4c,0xbe0255f4,0xbe8fcefe,0x3e3403f1,0x3ca0d6d6,0xbe0c4cf7,0xbe94a031,
0x3e2a1c3f,0x3c20d8d2,0xbe164083,0xbe996dba,0xbe9e377a,0xbee87171,0xbf167918,0xbf3504f3,0xbea2fd52,0xbeece91a,0xbf187fc0,0xbf36c9a9,
0xbea7bf25,0xbef15aea,0xbf1a82a4,0xbf3889dc,0xbeac7cd4,0xbef5c6c5,0xbf1c81b9,0xbf3a4582,0xbeb13641,0xbefa2c90,0xbf1e7cf1,0xbf3bfc8f,
0xbeb5eb4f,0xbefe8c2f,0xbf207440,0xbf3daef9,0xbeba9be1,0xbf0172c3,0xbf226799,0xbf3f5cb5,0xbebf47d7,0xbf039c3d,0xbf2456f1,0xbf4105b8,
0xbec3ef15,0xbf05c277,0xbf26423a,0xbf42a9f7,0xbec8917e,0xbf07e565,0xbf28296a,0xbf444969,0xbecd2ef3,0xbf0a04f8,0xbf2a0c73,0xbf45e403,
0xbed1c759,0xbf0c2124,0xbf2beb4a,0xbf4779bc,0xbed65a92,0xbf0e39da,0xbf2dc5e3,0xbf490a88,0xbedae880,0xbf104f0e,0xbf2f9c32,0xbf4a965e,
0xbedf7108,0xbf1260b2,0xbf316e2c,0xbf4c1d35,0xbee3f40d,0xbf146eba,0xbf333bc6,0xbf4d9f02,0,0x3e20305b,0x3e9e377a,0x3ee87171,
0x3c20d8d2,0x3e2a1c3f,0x3ea2fd52,0x3eece91a,0x3ca0d6d6,0x3e3403f1,0x3ea7bf25,0x3ef15aea,0x3cf13d4c,0x3e3de731,0x3eac7cd4,0x3ef5c6c5,
0x3d20cee7,0x3e47c5c2,0x3eb13641,0x3efa2c90,0x3d48fb30,0x3e519f65,0x3eb5eb4f,0x3efe8c2f,0x3d712283,0x3e5b73dc,0x3eba9be1,0x3f0172c3,
0x3d8ca1f2,0x3e6542e9,0x3ebf47d7,0x3f039c3d,0x3da0af2a,0x3e6f0c4d,0x3ec3ef15,0x3f05c277,0x3db4b86a,0x3e78cfcc,0x3ec8917e,0x3f07e565,
0x3dc8bd36,0x3e814693,0x3ecd2ef3,0x3f0a04f8,0x3ddcbd0d,0x3e862210,0x3ed1c759,0x3f0c2124,0x3df0b772,0x3e8afa3e,0x3ed65a92,0x3f0e39da,
0x3e0255f4,0x3e8fcefe,0x3edae880,0x3f104f0e,0x3e0c4cf7,0x3e94a031,0x3edf7108,0x3f1260b2,0x3e164083,0x3e996dba,0x3ee3f40d,0x3f146eba,
0x3f167918,0x3f3504f3,0x3f4f1bbd,0x3f641901,0x3f187fc0,0x3f36c9a9,0x3f50935b,0x3f653a48,0x3f1a82a4,0x3f3889dc,0x3f5205d4,0x3f6655e7,
0x3f1c81b9,0x3f3a4582,0x3f53731f,0x3f676bd8,0x3f1e7cf1,0x3f3bfc8f,0x3f54db31,0x3f687c12,0x3f207440,0x3f3daef9,0x3f563e03,0x3f698690,
0x3f226799,0x3f3f5cb5,0x3f579b8c,0x3f6a8b4b,0x3f2456f1,0x3f4105b8,0x3f58f3c4,0x3f6b8a3d,0x3f26423a,0x3f42a9f7,0x3f5a46a0,0x3f6c835e,
0x3f28296a,0x3f444969,0x3f5b941a,0x3f6d76aa,0x3f2a0c73,0x3f45e403,0x3f5cdc29,0x3f6e641a,0x3f2beb4a,0x3f4779bc,0x3f5e1ec5,0x3f6f4ba8,
0x3f2dc5e3,0x3f490a88,0x3f5f5be6,0x3f702d4f,0x3f2f9c32,0x3f4a965e,0x3f609384,0x3f710908,0x3f316e2c,0x3f4c1d35,0x3f61c598,0x3f71decf,
0x3f333bc6,0x3f4d9f02,0x3f62f219,0x3f72ae9e,0x3f737871,0x3f7cd925,0x3f800000,0x3f7cd925,0x3f743c41,0x3f7d3aac,0x3f7ffcd7,0x3f7c7160,
0x3f74fa0b,0x3f7d95f4,0x3f7ff35e,0x3f7c0361,0x3f75b1c9,0x3f7deafa,0x3f7fe394,0x3f7b8f2a,0x3f766377,0x3f7e39bc,0x3f7fcd79,0x3f7b14be,
0x3f770f11,0x3f7e8238,0x3f7fb10f,0x3f7a9421,0x3f77b493,0x3f7ec46d,0x3f7f8e56,0x3f7a0d54,0x3f7853f8,0x3f7f0059,0x3f7f654e,0x3f79805c,
0x3f78ed3c,0x3f7f35f9,0x3f7f35f9,0x3f78ed3c,0x3f79805c,0x3f7f654e,0x3f7f0059,0x3f7853f8,0x3f7a0d54,0x3f7f8e56,0x3f7ec46d,0x3f77b493,
0x3f7a9421,0x3f7fb10f,0x3f7e8238,0x3f770f11,0x3f7b14be,0x3f7fcd79,0x3f7e39bc,0x3f766377,0x3f7b8f2a,0x3f7fe394,0x3f7deafa,0x3f75b1c9,
0x3f7c0361,0x3f7ff35e,0x3f7d95f4,0x3f74fa0b,0x3f7c7160,0x3f7ffcd7,0x3f7d3aac,0x3f743c41,0x3f737871,0x3f641901,0x3f4f1bbd,0x3f3504f3,
0x3f72ae9e,0x3f62f219,0x3f4d9f02,0x3f333bc6,0x3f71decf,0x3f61c598,0x3f4c1d35,0x3f316e2c,0x3f710908,0x3f609384,0x3f4a965e,0x3f2f9c32,
0x3f702d4f,0x3f5f5be6,0x3f490a88,0x3f2dc5e3,0x3f6f4ba8,0x3f5e1ec5,0x3f4779bc,0x3f2beb4a,0x3f6e641a,0x3f5cdc29,0x3f45e403,0x3f2a0c73,
0x3f6d76aa,0x3f5b941a,0x3f444969,0x3f28296a,0x3f6c835e,0x3f5a46a0,0x3f42a9f7,0x3f26423a,0x3f6b8a3d,0x3f58f3c4,0x3f4105b8,0x3f2456f1,
0x3f6a8b4b,0x3f579b8c,0x3f3f5cb5,0x3f226799,0x3f698690,0x3f563e03,0x3f3daef9,0x3f207440,0x3f687c12,0x3f54db31,0x3f3bfc8f,0x3f1e7cf1,
0x3f676bd8,0x3f53731f,0x3f3a4582,0x3f1c81b9,0x3f6655e7,0x3f5205d4,0x3f3889dc,0x3f1a82a4,0x3f653a48,0x3f50935b,0x3f36c9a9,0x3f187fc0,
0x3f800000,0x3f737871,0x3f4f1bbd,0x3f167918,0x3f7ff35e,0x3f71decf,0x3f4c1d35,0x3f1260b2,0x3f7fcd79,0x3f702d4f,0x3f490a88,0x3f0e39da,
0x3f7f8e56,0x3f6e641a,0x3f45e403,0x3f0a04f8,0x3f7f35f9,0x3f6c835e,0x3f42a9f7,0x3f05c277,0x3f7ec46d,0x3f6a8b4b,0x3f3f5cb5,0x3f0172c3,
0x3f7e39bc,0x3f687c12,0x3f3bfc8f,0x3efa2c90,0x3f7d95f4,0x3f6655e7,0x3f3889dc,0x3ef15aea,0x3f7cd925,0x3f641901,0x3f3504f3,0x3ee87171,
0x3f7c0361,0x3f61c598,0x3f316e2c,0x3edf7108,0x3f7b14be,0x3f5f5be6,0x3f2dc5e3,0x3ed65a92,0x3f7a0d54,0x3f5cdc29,0x3f2a0c73,0x3ecd2ef3,
0x3f78ed3c,0x3f5a46a0,0x3f26423a,0x3ec3ef15,0x3f77b493,0x3f579b8c,0x3f226799,0x3eba9be1,0x3f766377,0x3f54db31,0x3f1e7cf1,0x3eb13641,
0x3f74fa0b,0x3f5205d4,0x3f1a82a4,0x3ea7bf25,0x3e9e377a,0,0xbe9e377a,0xbf167918,0x3e94a031,0xbca0d6d6,0xbea7bf25,0xbf1a82a4,
0x3e8afa3e,0xbd20cee7,0xbeb13641,0xbf1e7cf1,0x3e814693,0xbd712283,0xbeba9be1,0xbf226799,0x3e6f0c4d,0xbda0af2a,0xbec3ef15,0xbf26423a,
0x3e5b73dc,0xbdc8bd36,0xbecd2ef3,0xbf2a0c73,0x3e47c5c2,0xbdf0b772,0xbed65a92,0xbf2dc5e3,0x3e3403f1,0xbe0c4cf7,0xbedf7108,0xbf316e2c,
0x3e20305b,0xbe20305b,0xbee87171,0xbf3504f3,0x3e0c4cf7,0xbe3403f1,0xbef15aea,0xbf3889dc,0x3df0b772,0xbe47c5c2,0xbefa2c90,0xbf3bfc8f,
0x3dc8bd36,0xbe5b73dc,0xbf0172c3,0xbf3f5cb5,0x3da0af2a,0xbe6f0c4d,0xbf05c277,0xbf42a9f7,0x3d712283,0xbe814693,0xbf0a04f8,0xbf45e403,
0x3d20cee7,0xbe8afa3e,0xbf0e39da,0xbf490a88,0x3ca0d6d6,0xbe94a031,0xbf1260b2,0xbf4c1d35,0xbf4f1bbd,0xbf737871,0xbf800000,0xbf737871,
0xbf5205d4,0xbf74fa0b,0xbf7ff35e,0xbf71decf,0xbf54db31,0xbf766377,0xbf7fcd79,0xbf702d4f,0xbf579b8c,0xbf77b493,0xbf7f8e56,0xbf6e641a,
0xbf5a46a0,0xbf78ed3c,0xbf7f35f9,0xbf6c835e,0xbf5cdc29,0xbf7a0d54,0xbf7ec46d,0xbf6a8b4b,0xbf5f5be6,0xbf7b14be,0xbf7e39bc,0xbf687c12,
0xbf61c598,0xbf7c0361,0xbf7d95f4,0xbf6655e7,0xbf641901,0xbf7cd925,0xbf7cd925,0xbf641901,0xbf6655e7,0xbf7d95f4,0xbf7c0361,0xbf61c598,
0xbf687c12,0xbf7e39bc,0xbf7b14be,0xbf5f5be6,0xbf6a8b4b,0xbf7ec46d,0xbf7a0d54,0xbf5cdc29,0xbf6c835e,0xbf7f35f9,0xbf78ed3c,0xbf5a46a0,
0xbf6e641a,0xbf7f8e56,0xbf77b493,0xbf579b8c,0xbf702d4f,0xbf7fcd79,0xbf766377,0xbf54db31,0xbf71decf,0xbf7ff35e,0xbf74fa0b,0xbf5205d4,
0xbf4f1bbd,0xbf167918,0xbe9e377a,0,0xbf4c1d35,0xbf1260b2,0xbe94a031,0x3ca0d6d6,0xbf490a88,0xbf0e39da,0xbe8afa3e,0x3d20cee7,
0xbf45e403,0xbf0a04f8,0xbe814693,0x3d712283,0xbf42a9f7,0xbf05c277,0xbe6f0c4d,0x3da0af2a,0xbf3f5cb5,0xbf0172c3,0xbe5b73dc,0x3dc8bd36,
0xbf3bfc8f,0xbefa2c90,0xbe47c5c2,0x3df0b772,0xbf3889dc,0xbef15aea,0xbe3403f1,0x3e0c4cf7,0xbf3504f3,0xbee87171,0xbe20305b,0x3e20305b,
0xbf316e2c,0xbedf7108,0xbe0c4cf7,0x3e3403f1,0xbf2dc5e3,0xbed65a92,0xbdf0b772,0x3e47c5c2,0xbf2a0c73,0xbecd2ef3,0xbdc8bd36,0x3e5b73dc,
0xbf26423a,0xbec3ef15,0xbda0af2a,0x3e6f0c4d,0xbf226799,0xbeba9be1,0xbd712283,0x3e814693,0xbf1e7cf1,0xbeb13641,0xbd20cee7,0x3e8afa3e,
0xbf1a82a4,0xbea7bf25,0xbca0d6d6,0x3e94a031,0,0x3e9e377a,0x3f167918,0x3f4f1bbd,0x3ca0d6d6,0x3ea7bf25,0x3f1a82a4,0x3f5205d4,
0x3d20cee7,0x3eb13641,0x3f1e7cf1,0x3f54db31,0x3d712283,0x3eba9be1,0x3f226799,0x3f579b8c,0x3da0af2a,0x3ec3ef15,0x3f26423a,0x3f5a46a0,
0x3dc8bd36,0x3ecd2ef3,0x3f2a0c73,0x3f5cdc29,0x3df0b772,0x3ed65a92,0x3f2dc5e3,0x3f5f5be6,0x3e0c4cf7,0x3edf7108,0x3f316e2c,0x3f61c598,
0x3e20305b,0x3ee87171,0x3f3504f3,0x3f641901,0x3e3403f1,0x3ef15aea,0x3f3889dc,0x3f6655e7,0x3e47c5c2,0x3efa2c90,0x3f3bfc8f,0x3f687c12,
0x3e5b73dc,0x3f0172c3,0x3f3f5cb5,0x3f6a8b4b,0x3e6f0c4d,0x3f05c277,0x3f42a9f7,0x3f6c835e,0x3e814693,0x3f0a04f8,0x3f45e403,0x3f6e641a,
0x3e8afa3e,0x3f0e39da,0x3f490a88,0x3f702d4f,0x3e94a031,0x3f1260b2,0x3f4c1d35,0x3f71decf,0x3f737871,0x3f800000,0x3f737871,0x3f4f1bbd,
0x3f74fa0b,0x3f7ff35e,0x3f71decf,0x3f4c1d35,0x3f766377,0x3f7fcd79,0x3f702d4f,0x3f490a88,0x3f77b493,0x3f7f8e56,0x3f6e641a,0x3f45e403,
0x3f78ed3c,0x3f7f35f9,0x3f6c835e,0x3f42a9f7,0x3f7a0d54,0x3f7ec46d,0x3f6a8b4b,0x3f3f5cb5,0x3f7b14be,0x3f7e39bc,0x3f687c12,0x3f3bfc8f,
0x3f7c0361,0x3f7d95f4,0x3f6655e7,0x3f3889dc,0x3f7cd925,0x3f7cd925,0x3f641901,0x3f3504f3,0x3f7d95f4,0x3f7c0361,0x3f61c598,0x3f316e2c,
0x3f7e39bc,0x3f7b14be,0x3f5f5be6,0x3f2dc5e3,0x3f7ec46d,0x3f7a0d54,0x3f5cdc29,0x3f2a0c73,0x3f7f35f9,0x3f78ed3c,0x3f5a46a0,0x3f26423a,
0x3f7f8e56,0x3f77b493,0x3f579b8c,0x3f226799,0x3f7fcd79,0x3f766377,0x3f54db31,0x3f1e7cf1,0x3f7ff35e,0x3f74fa0b,0x3f5205d4,0x3f1a82a4,
0x3f167918,0x3e9e377a,0,0xbe9e377a,0x3f1260b2,0x3e94a031,0xbca0d6d6,0xbea7bf25,0x3f0e39da,0x3e8afa3e,0xbd20cee7,0xbeb13641,
0x3f0a04f8,0x3e814693,0xbd712283,0xbeba9be1,0x3f05c277,0x3e6f0c4d,0xbda0af2a,0xbec3ef15,0x3f0172c3,0x3e5b73dc,0xbdc8bd36,0xbecd2ef3,
0x3efa2c90,0x3e47c5c2,0xbdf0b772,0xbed65a92,0x3ef15aea,0x3e3403f1,0xbe0c4cf7,0xbedf7108,0x3ee87171,0x3e20305b,0xbe20305b,0xbee87171,
0x3edf7108,0x3e0c4cf7,0xbe3403f1,0xbef15aea,0x3ed65a92,0x3df0b772,0xbe47c5c2,0xbefa2c90,0x3ecd2ef3,0x3dc8bd36,0xbe5b73dc,0xbf0172c3,
0x3ec3ef15,0x3da0af2a,0xbe6f0c4d,0xbf05c277,0x3eba9be1,0x3d712283,0xbe814693,0xbf0a04f8,0x3eb13641,0x3d20cee7,0xbe8afa3e,0xbf0e39da,
0x3ea7bf25,0x3ca0d6d6,0xbe94a031,0xbf1260b2,0xbf167918,0xbf4f1bbd,0xbf737871,0xbf800000,0xbf1a82a4,0xbf5205d4,0xbf74fa0b,0xbf7ff35e,
0xbf1e7cf1,0xbf54db31,0xbf766377,0xbf7fcd79,0xbf226799,0xbf579b8c,0xbf77b493,0xbf7f8e56,0xbf26423a,0xbf5a46a0,0xbf78ed3c,0xbf7f35f9,
0xbf2a0c73,0xbf5cdc29,0xbf7a0d54,0xbf7ec46d,0xbf2dc5e3,0xbf5f5be6,0xbf7b14be,0xbf7e39bc,0xbf316e2c,0xbf61c598,0xbf7c0361,0xbf7d95f4,
0xbf3504f3,0xbf641901,0xbf7cd925,0xbf7cd925,0xbf3889dc,0xbf6655e7,0xbf7d95f4,0xbf7c0361,0xbf3bfc8f,0xbf687c12,0xbf7e39bc,0xbf7b14be,
0xbf3f5cb5,0xbf6a8b4b,0xbf7ec46d,0xbf7a0d54,0xbf42a9f7,0xbf6c835e,0xbf7f35f9,0xbf78ed3c,0xbf45e403,0xbf6e641a,0xbf7f8e56,0xbf77b493,
0xbf490a88,0xbf702d4f,0xbf7fcd79,0xbf766377,0xbf4c1d35,0xbf71decf,0xbf7ff35e,0xbf74fa0b,0x3400,0x3400,0x3400,0x3400,
0x3400,0x2e1f3364,0x31a831a8,0x33642e1f,0x3400,0x31a831a8,0x34000000,0x31a8b1a8,0x3400,0x33642e1f,0x31a8b1a8,0xae1fb364,
0x3400,0x34000000,0xb400,0xb4000000,0x3400,0x3364ae1f,0xb1a8b1a8,0xae1f3364,0x3400,0x31a8b1a8,0xb4000000,0x31a831a8,
0x3400,0x2e1fb364,0xb1a831a8,0x3364ae1f,0x3400,0xb400,0x3400,0xb400,0x3400,0xae1fb364,0x31a831a8,0xb364ae1f,
0x3400,0xb1a8b1a8,0x34000000,0xb1a831a8,0x3400,0xb364ae1f,0x31a8b1a8,0x2e1f3364,0x3400,0xb4000000,0xb400,0x34000000,
0x3400,0xb3642e1f,0xb1a8b1a8,0x2e1fb364,0x3400,0xb1a831a8,0xb4000000,0xb1a8b1a8,0x3400,0xae1f3364,0xb1a831a8,0xb3642e1f,
0x3400,0x3400,0x3400,0x3400,0x34000000,0x3364ae1f,0x31a8b1a8,0x2e1fb364,0xb400,0xb1a8b1a8,0xb4000000,0xb1a831a8,
0xb4000000,0xae1f3364,0x31a831a8,0x3364ae1f,0x3400,0x34000000,0xb400,0xb4000000,0x34000000,0xae1fb364,0xb1a831a8,0x33642e1f,
0xb400,0xb1a831a8,0x34000000,0xb1a8b1a8,0xb4000000,0x33642e1f,0xb1a8b1a8,0x2e1f3364,0x3400,0xb400,0x3400,0xb400,
0x34000000,0xb3642e1f,0x31a8b1a8,0xae1f3364,0xb400,0x31a831a8,0xb4000000,0x31a8b1a8,0xb4000000,0x2e1fb364,0x31a831a8,0xb3642e1f,
0x3400,0xb4000000,0xb400,0x34000000,0x34000000,0x2e1f3364,0xb1a831a8,0xb364ae1f,0xb400,0x31a8b1a8,0x34000000,0x31a831a8,
0xb4000000,0xb364ae1f,0xb1a8b1a8,0xae1fb364,0x3400,0x3400,0x3400,0x3400,0xb400,0xae1fb364,0xb1a8b1a8,0xb364ae1f,
0x3400,0x31a831a8,0x34000000,0x31a8b1a8,0xb400,0xb364ae1f,0xb1a831a8,0x2e1f3364,0x3400,0x34000000,0xb400,0xb4000000,
0xb400,0xb3642e1f,0x31a831a8,0x2e1fb364,0x3400,0x31a8b1a8,0xb4000000,0x31a831a8,0xb400,0xae1f3364,0x31a8b1a8,0xb3642e1f,
0x3400,0xb400,0x3400,0xb400,0xb400,0x2e1f3364,0xb1a8b1a8,0x33642e1f,0x3400,0xb1a8b1a8,0x34000000,0xb1a831a8,
0xb400,0x33642e1f,0xb1a831a8,0xae1fb364,0x3400,0xb4000000,0xb400,0x34000000,0xb400,0x3364ae1f,0x31a831a8,0xae1f3364,
0x3400,0xb1a831a8,0xb4000000,0xb1a8b1a8,0xb400,0x2e1fb364,0x31a8b1a8,0x3364ae1f,0x3400,0x3400,0x3400,0x3400,
0xb4000000,0xb3642e1f,0xb1a831a8,0xae1f3364,0xb400,0xb1a8b1a8,0xb4000000,0xb1a831a8,0x34000000,0x2e1fb364,0xb1a8b1a8,0xb3642e1f,
0x3400,0x34000000,0xb400,0xb4000000,0xb4000000,0x2e1f3364,0x31a8b1a8,0xb364ae1f,0xb400,0xb1a831a8,0x34000000,0xb1a8b1a8,
0x34000000,0xb364ae1f,0x31a831a8,0xae1fb364,0x3400,0xb400,0x3400,0xb400,0xb4000000,0x3364ae1f,0xb1a831a8,0x2e1fb364,
0xb400,0x31a831a8,0xb4000000,0x31a8b1a8,0x34000000,0xae1f3364,0xb1a8b1a8,0x3364ae1f,0x3400,0xb4000000,0xb400,0x34000000,
0xb4000000,0xae1fb364,0x31a8b1a8,0x33642e1f,0xb400,0x31a8b1a8,0x34000000,0x31a831a8,0x34000000,0x33642e1f,0x31a831a8,0x2e1f3364,
0x34000000,0x34000000,0x34000000,0x34000000,0x34000000,0x3364ae1f,0x31a8b1a8,0x2e1fb364,0x34000000,0x31a8b1a8,0xb400,0xb1a8b1a8,
0x34000000,0x2e1fb364,0xb1a8b1a8,0xb3642e1f,0x34000000,0xb400,0xb4000000,0x3400,0x34000000,0xae1fb364,0xb1a831a8,0x33642e1f,
0x34000000,0xb1a8b1a8,0x3400,0x31a8b1a8,0x34000000,0xb364ae1f,0x31a831a8,0xae1fb364,0x34000000,0xb4000000,0x34000000,0xb4000000,
0x34000000,0xb3642e1f,0x31a8b1a8,0xae1f3364,0x34000000,0xb1a831a8,0xb400,0x31a831a8,0x34000000,0xae1f3364,0xb1a8b1a8,0x3364ae1f,
0x34000000,0x3400,0xb4000000,0xb400,0x34000000,0x2e1f3364,0xb1a831a8,0xb364ae1f,0x34000000,0x31a831a8,0x3400,0xb1a831a8,
0x34000000,0x33642e1f,0x31a831a8,0x2e1f3364,0x34000000,0x34000000,0x34000000,0x34000000,0xb400,0xae1fb364,0xb1a8b1a8,0xb364ae1f,
0xb4000000,0xb1a831a8,0x3400,0x31a831a8,0x3400,0x33642e1f,0x31a8b1a8,0xae1fb364,0x34000000,0xb400,0xb4000000,0x3400,
0xb400,0xb3642e1f,0x31a831a8,0x2e1fb364,0xb4000000,0x31a831a8,0xb400,0xb1a831a8,0x3400,0x2e1fb364,0xb1a831a8,0x3364ae1f,
0x34000000,0xb4000000,0x34000000,0xb4000000,0xb400,0x2e1f3364,0xb1a8b1a8,0x33642e1f,0xb4000000,0x31a8b1a8,0x3400,0xb1a8b1a8,
0x3400,0xb364ae1f,0x31a8b1a8,0x2e1f3364,0x34000000,0x3400,0xb4000000,0xb400,0xb400,0x3364ae1f,0x31a831a8,0xae1f3364,
0xb4000000,0xb1a8b1a8,0xb400,0x31a8b1a8,0x3400,0xae1f3364,0xb1a831a8,0xb3642e1f,0x34000000,0x34000000,0x34000000,0x34000000,
0xb4000000,0xb3642e1f,0xb1a831a8,0xae1f3364,0x34000000,0x31a8b1a8,0xb400,0xb1a8b1a8,0xb4000000,0xae1f3364,0x31a831a8,0x3364ae1f,
0x34000000,0xb400,0xb4000000,0x3400,0xb4000000,0x2e1f3364,0x31a8b1a8,0xb364ae1f,0x34000000,0xb1a8b1a8,0x3400,0x31a8b1a8,
0xb4000000,0x33642e1f,0xb1a8b1a8,0x2e1f3364,0x34000000,0xb4000000,0x34000000,0xb4000000,0xb4000000,0x3364ae1f,0xb1a831a8,0x2e1fb364,
0x34000000,0xb1a831a8,0xb400,0x31a831a8,0xb4000000,0x2e1fb364,0x31a831a8,0xb3642e1f,0x34000000,0x3400,0xb4000000,0xb400,
0xb4000000,0xae1fb364,0x31a8b1a8,0x33642e1f,0x34000000,0x31a831a8,0x3400,0xb1a831a8,0xb4000000,0xb364ae1f,0xb1a8b1a8,0xae1fb364,
0x34000000,0x34000000,0x34000000,0x34000000,0x3400,0x2e1f3364,0x31a831a8,0x33642e1f,0xb4000000,0xb1a831a8,0x3400,0x31a831a8,
0xb400,0xb364ae1f,0xb1a831a8,0x2e1f3364,0x34000000,0xb400,0xb4000000,0x3400,0x3400,0x3364ae1f,0xb1a8b1a8,0xae1f3364,
0xb4000000,0x31a831a8,0xb400,0xb1a831a8,0xb400,0xae1f3364,0x31a8b1a8,0xb3642e1f,0x34000000,0xb4000000,0x34000000,0xb4000000,
0x3400,0xae1fb364,0x31a831a8,0xb364ae1f,0xb4000000,0x31a8b1a8,0x3400,0xb1a8b1a8,0xb400,0x33642e1f,0xb1a831a8,0xae1fb364,
0x34000000,0x3400,0xb4000000,0xb400,0x3400,0xb3642e1f,0xb1a8b1a8,0x2e1fb364,0xb4000000,0xb1a8b1a8,0xb400,0x31a8b1a8,
0xb400,0x2e1fb364,0x31a8b1a8,0x3364ae1f,0x3f800000,0x3f800000,0x3f800000,0x3f800000,0x3f800000,0x3f7fec43,0x3f7fb10f,0x3f7f4e6d,
0x3f800000,0x3f7fb10f,0x3f7ec46d,0x3f7d3aac,0x3f800000,0x3f7f4e6d,0x3f7d3aac,0x3f79c79d,0x3f800000,0x3f7ec46d,0x3f7b14be,0x3f74fa0b,
0x3f800000,0x3f7e1324,0x3f7853f8,0x3f6ed89e,0x3f800000,0x3f7d3aac,0x3f74fa0b,0x3f676bd8,0x3f800000,0x3f7c3b28,0x3f710908,0x3f5ebe05,
0x3f800000,0x3f7b14be,0x3f6c835e,0x3f54db31,0x3f800000,0x3f79c79d,0x3f676bd8,0x3f49d112,0x3f800000,0x3f7853f8,0x3f61c598,0x3f3daef9,
0x3f800000,0x3f76ba07,0x3f5b941a,0x3f3085bb,0x3f800000,0x3f74fa0b,0x3f54db31,0x3f226799,0x3f800000,0x3f731447,0x3f4d9f02,0x3f13682a,
0x3f800000,0x3f710908,0x3f45e403,0x3f039c3d,0x3f800000,0x3f6ed89e,0x3f3daef9,0x3ee63375,0x3f800000,0x3f800000,0x3f800000,0x3f800000,
0x3f7ec46d,0x3f7e1324,0x3f7d3aac,0x3f7c3b28,0x3f7b14be,0x3f7853f8,0x3f74fa0b,0x3f710908,0x3f74fa0b,0x3f6ed89e,0x3f676bd8,0x3f5ebe05,
0x3f6c835e,0x3f61c598,0x3f54db31,0x3f45e403,0x3f61c598,0x3f514d3d,0x3f3daef9,0x3f273656,0x3f54db31,0x3f3daef9,0x3f226799,0x3f039c3d,
0x3f45e403,0x3f273656,0x3f039c3d,0x3eb8442a,0x3f3504f3,0x3f0e39da,0x3ec3ef15,0x3e47c5c2,0x3f226799,0x3ee63375,0x3e78cfcc,0x3cc90ab0,
0x3f0e39da,0x3eac7cd4,0x3dc8bd36,0xbe164083,0x3ef15aea,0x3e605c13,0xbd48fb30,0xbea09ae5,0x3ec3ef15,0x3dc8bd36,0xbe47c5c2,0xbef15aea,
0x3e94a031,0xbcc90ab0,0xbeac7cd4,0xbf1d7fd1,0x3e47c5c2,0xbe164083,0xbef15aea,0xbf3daef9,0x3dc8bd36,0xbe888e93,0xbf187fc0,0xbf584853,
0x3f800000,0x3f800000,0x3f800000,0x3f800000,0x3f7b14be,0x3f79c79d,0x3f7853f8,0x3f76ba07,0x3f6c835e,0x3f676bd8,0x3f61c598,0x3f5b941a,
0x3f54db31,0x3f49d112,0x3f3daef9,0x3f3085bb,0x3f3504f3,0x3f226799,0x3f0e39da,0x3ef15aea,0x3f0e39da,0x3ee63375,0x3eac7cd4,0x3e605c13,
0x3ec3ef15,0x3e78cfcc,0x3dc8bd36,0xbd48fb30,0x3e47c5c2,0x3cc90ab0,0xbe164083,0xbea09ae5,0,0xbe47c5c2,0xbec3ef15,0xbf0e39da,
0xbe47c5c2,0xbecf7bca,0xbf187fc0,0xbf41d870,0xbec3ef15,0xbf187fc0,0xbf45e403,0xbf676bd8,0xbf0e39da,0xbf41d870,0xbf676bd8,0xbf7c3b28,
0xbf3504f3,0xbf61c598,0xbf7b14be,0xbf7ec46d,0xbf54db31,0xbf76ba07,0xbf7fb10f,0xbf6ed89e,0xbf6c835e,0xbf7fb10f,0xbf74fa0b,0xbf4d9f02,
0xbf7b14be,0xbf7c3b28,0xbf5b941a,0xbf1d7fd1,0x3f800000,0x3f800000,0x3f800000,0x3f800000,0x3f74fa0b,0x3f731447,0x3f710908,0x3f6ed89e,
0x3f54db31,0x3f4d9f02,0x3f45e403,0x3f3daef9,0x3f226799,0x3f13682a,0x3f039c3d,0x3ee63375,0x3ec3ef15,0x3e94a031,0x3e47c5c2,0x3dc8bd36,
0x3dc8bd36,0xbcc90ab0,0xbe164083,0xbe888e93,0xbe47c5c2,0xbeac7cd4,0xbef15aea,0xbf187fc0,0xbef15aea,0xbf1d7fd1,0xbf3daef9,0xbf584853,
0xbf3504f3,0xbf54db31,0xbf6c835e,0xbf7b14be,0xbf61c598,0xbf76ba07,0xbf7fb10f,0xbf7c3b28,0xbf7b14be,0xbf7fb10f,0xbf74fa0b,0xbf5b941a,
0xbf7ec46d,0xbf6ed89e,0xbf4d9f02,0xbf1d7fd1,0xbf6c835e,0xbf45e403,0xbf0e39da,0xbe94a031,0xbf45e403,0xbf08f59b,0xbe78cfcc,0x3d96a905,
0xbf0e39da,0xbe78cfcc,0x3dc8bd36,0x3edae880,0xbe94a031,0x3d96a905,0x3edae880,0x3f396842,0,0,0,0,
0,0xbcc90ab0,0xbd48fb30,0xbd96a905,0,0xbd48fb30,0xbdc8bd36,0xbe164083,0,0xbd96a905,0xbe164083,0xbe605c13,
0,0xbdc8bd36,0xbe47c5c2,0xbe94a031,0,0xbdfab273,0xbe78cfcc,0xbeb8442a,0,0xbe164083,0xbe94a031,0xbedae880,
0,0xbe2f10a2,0xbeac7cd4,0xbefc5d27,0,0xbe47c5c2,0xbec3ef15,0xbf0e39da,0,0xbe605c13,0xbedae880,0xbf1d7fd1,
0,0xbe78cfcc,0xbef15aea,0xbf2beb4a,0,0xbe888e93,0xbf039c3d,0xbf396842,0,0xbe94a031,0xbf0e39da,0xbf45e403,
0,0xbea09ae5,0xbf187fc0,0xbf514d3d,0,0xbeac7cd4,0xbf226799,0xbf5b941a,0,0xbeb8442a,0xbf2beb4a,0xbf64aa59,
0,0,0,0,0xbdc8bd36,0xbdfab273,0xbe164083,0xbe2f10a2,0xbe47c5c2,0xbe78cfcc,0xbe94a031,0xbeac7cd4,
0xbe94a031,0xbeb8442a,0xbedae880,0xbefc5d27,0xbec3ef15,0xbef15aea,0xbf0e39da,0xbf226799,0xbef15aea,0xbf13682a,0xbf2beb4a,0xbf41d870,
0xbf0e39da,0xbf2beb4a,0xbf45e403,0xbf5b941a,0xbf226799,0xbf41d870,0xbf5b941a,0xbf6ed89e,0xbf3504f3,0xbf54db31,0xbf6c835e,0xbf7b14be,
0xbf45e403,0xbf64aa59,0xbf7853f8,0xbf7fec43,0xbf54db31,0xbf710908,0xbf7ec46d,0xbf7d3aac,0xbf61c598,0xbf79c79d,0xbf7fb10f,0xbf731447,
0xbf6c835e,0xbf7ec46d,0xbf7b14be,0xbf61c598,0xbf74fa0b,0xbf7fec43,0xbf710908,0xbf49d112,0xbf7b14be,0xbf7d3aac,0xbf61c598,0xbf2beb4a,
0xbf7ec46d,0xbf76ba07,0xbf4d9f02,0xbf08f59b,0,0,0,0,0xbe47c5c2,0xbe605c13,0xbe78cfcc,0xbe888e93,
0xbec3ef15,0xbedae880,0xbef15aea,0xbf039c3d,0xbf0e39da,0xbf1d7fd1,0xbf2beb4a,0xbf396842,0xbf3504f3,0xbf45e403,0xbf54db31,0xbf61c598,
0xbf54db31,0xbf64aa59,0xbf710908,0xbf79c79d,0xbf6c835e,0xbf7853f8,0xbf7ec46d,0xbf7fb10f,0xbf7b14be,0xbf7fec43,0xbf7d3aac,0xbf731447,
0xbf800000,0xbf7b14be,0xbf6c835e,0xbf54db31,0xbf7b14be,0xbf6a09a7,0xbf4d9f02,0xbf273656,0xbf6c835e,0xbf4d9f02,0xbf226799,0xbedae880,
0xbf54db31,0xbf273656,0xbedae880,0xbe2f10a2,0xbf3504f3,0xbef15aea,0xbe47c5c2,0x3dc8bd36,0xbf0e39da,0xbe888e93,0x3d48fb30,0x3eb8442a,
0xbec3ef15,0xbd48fb30,0x3e94a031,0x3f187fc0,0xbe47c5c2,0x3e2f10a2,0x3f039c3d,0x3f49d112,0,0,0,0,
0xbe94a031,0xbea09ae5,0xbeac7cd4,0xbeb8442a,0xbf0e39da,0xbf187fc0,0xbf226799,0xbf2beb4a,0xbf45e403,0xbf514d3d,0xbf5b941a,0xbf64aa59,
0xbf6c835e,0xbf74fa0b,0xbf7b14be,0xbf7ec46d,0xbf7ec46d,0xbf7fec43,0xbf7d3aac,0xbf76ba07,0xbf7b14be,0xbf710908,0xbf61c598,0xbf4d9f02,
0xbf61c598,0xbf49d112,0xbf2beb4a,0xbf08f59b,0xbf3504f3,0xbf0e39da,0xbec3ef15,0xbe47c5c2,0xbef15aea,0xbe888e93,0xbd48fb30,0x3e2f10a2,
0xbe47c5c2,0x3d48fb30,0x3e94a031,0x3f039c3d,0x3dc8bd36,0x3eb8442a,0x3f187fc0,0x3f49d112,0x3ec3ef15,0x3f226799,0x3f54db31,0x3f74fa0b,
0x3f226799,0x3f584853,0x3f7853f8,0x3f7f4e6d,0x3f54db31,0x3f7853f8,0x3f7ec46d,0x3f676bd8,0x3f74fa0b,0x3f7f4e6d,0x3f676bd8,0x3f3085bb,
0x3f800000,0x3f800000,0x3f800000,0x3f800000,0x3f800000,0x3f7fec43,0x3f7fb10f,0x3f7f4e6d,0x3f800000,0x3f7fb10f,0x3f7ec46d,0x3f7d3aac,
0x3f800000,0x3f7f4e6d,0x3f7d3aac,0x3f79c79d,0x3f800000,0x3f7ec46d,0x3f7b14be,0x3f74fa0b,0x3f800000,0x3f7e1324,0x3f7853f8,0x3f6ed89e,
0x3f800000,0x3f7d3aac,0x3f74fa0b,0x3f676bd8,0x3f800000,0x3f7c3b28,0x3f710908,0x3f5ebe05,0x3f800000,0x3f7b14be,0x3f6c835e,0x3f54db31,
0x3f800000,0x3f79c79d,0x3f676bd8,0x3f49d112,0x3f800000,0x3f7853f8,0x3f61c598,0x3f3daef9,0x3f800000,0x3f76ba07,0x3f5b941a,0x3f3085bb,
0x3f800000,0x3f74fa0b,0x3f54db31,0x3f226799,0x3f800000,0x3f731447,0x3f4d9f02,0x3f13682a,0x3f800000,0x3f710908,0x3f45e403,0x3f039c3d,
0x3f800000,0x3f6ed89e,0x3f3daef9,0x3ee63375,0x3f800000,0x3f800000,0x3f800000,0x3f800000,0x3f7ec46d,0x3f7e1324,0x3f7d3aac,0x3f7c3b28,
0x3f7b14be,0x3f7853f8,0x3f74fa0b,0x3f710908,0x3f74fa0b,0x3f6ed89e,0x3f676bd8,0x3f5ebe05,0x3f6c835e,0x3f61c598,0x3f54db31,0x3f45e403,
0x3f61c598,0x3f514d3d,0x3f3daef9,0x3f273656,0x3f54db31,0x3f3daef9,0x3f226799,0x3f039c3d,0x3f45e403,0x3f273656,0x3f039c3d,0x3eb8442a,
0x3f3504f3,0x3f0e39da,0x3ec3ef15,0x3e47c5c2,0x3f226799,0x3ee63375,0x3e78cfcc,0x3cc90ab0,0x3f0e39da,0x3eac7cd4,0x3dc8bd36,0xbe164083,
0x3ef15aea,0x3e605c13,0xbd48fb30,0xbea09ae5,0x3ec3ef15,0x3dc8bd36,0xbe47c5c2,0xbef15aea,0x3e94a031,0xbcc90ab0,0xbeac7cd4,0xbf1d7fd1,
0x3e47c5c2,0xbe164083,0xbef15aea,0xbf3daef9,0x3dc8bd36,0xbe888e93,0xbf187fc0,0xbf584853,0x3f800000,0x3f800000,0x3f800000,0x3f800000,
0x3f7b14be,0x3f79c79d,0x3f7853f8,0x3f76ba07,0x3f6c835e,0x3f676bd8,0x3f61c598,0x3f5b941a,0x3f54db31,0x3f49d112,0x3f3daef9,0x3f3085bb,
0x3f3504f3,0x3f226799,0x3f0e39da,0x3ef15aea,0x3f0e39da,0x3ee63375,0x3eac7cd4,0x3e605c13,0x3ec3ef15,0x3e78cfcc,0x3dc8bd36,0xbd48fb30,
0x3e47c5c2,0x3cc90ab0,0xbe164083,0xbea09ae5,0,0xbe47c5c2,0xbec3ef15,0xbf0e39da,0xbe47c5c2,0xbecf7bca,0xbf187fc0,0xbf41d870,
0xbec3ef15,0xbf187fc0,0xbf45e403,0xbf676bd8,0xbf0e39da,0xbf41d870,0xbf676bd8,0xbf7c3b28,0xbf3504f3,0xbf61c598,0xbf7b14be,0xbf7ec46d,
0xbf54db31,0xbf76ba07,0xbf7fb10f,0xbf6ed89e,0xbf6c835e,0xbf7fb10f,0xbf74fa0b,0xbf4d9f02,0xbf7b14be,0xbf7c3b28,0xbf5b941a,0xbf1d7fd1,
0x3f800000,0x3f800000,0x3f800000,0x3f800000,0x3f74fa0b,0x3f731447,0x3f710908,0x3f6ed89e,0x3f54db31,0x3f4d9f02,0x3f45e403,0x3f3daef9,
0x3f226799,0x3f13682a,0x3f039c3d,0x3ee63375,0x3ec3ef15,0x3e94a031,0x3e47c5c2,0x3dc8bd36,0x3dc8bd36,0xbcc90ab0,0xbe164083,0xbe888e93,
0xbe47c5c2,0xbeac7cd4,0xbef15aea,0xbf187fc0,0xbef15aea,0xbf1d7fd1,0xbf3daef9,0xbf584853,0xbf3504f3,0xbf54db31,0xbf6c835e,0xbf7b14be,
0xbf61c598,0xbf76ba07,0xbf7fb10f,0xbf7c3b28,0xbf7b14be,0xbf7fb10f,0xbf74fa0b,0xbf5b941a,0xbf7ec46d,0xbf6ed89e,0xbf4d9f02,0xbf1d7fd1,
0xbf6c835e,0xbf45e403,0xbf0e39da,0xbe94a031,0xbf45e403,0xbf08f59b,0xbe78cfcc,0x3d96a905,0xbf0e39da,0xbe78cfcc,0x3dc8bd36,0x3edae880,
0xbe94a031,0x3d96a905,0x3edae880,0x3f396842,0,0,0,0,0,0x3cc90ab0,0x3d48fb30,0x3d96a905,
0,0x3d48fb30,0x3dc8bd36,0x3e164083,0,0x3d96a905,0x3e164083,0x3e605c13,0,0x3dc8bd36,0x3e47c5c2,0x3e94a031,
0,0x3dfab273,0x3e78cfcc,0x3eb8442a,0,0x3e164083,0x3e94a031,0x3edae880,0,0x3e2f10a2,0x3eac7cd4,0x3efc5d27,
0,0x3e47c5c2,0x3ec3ef15,0x3f0e39da,0,0x3e605c13,0x3edae880,0x3f1d7fd1,0,0x3e78cfcc,0x3ef15aea,0x3f2beb4a,
0,0x3e888e93,0x3f039c3d,0x3f396842,0,0x3e94a031,0x3f0e39da,0x3f45e403,0,0x3ea09ae5,0x3f187fc0,0x3f514d3d,
0,0x3eac7cd4,0x3f226799,0x3f5b941a,0,0x3eb8442a,0x3f2beb4a,0x3f64aa59,0,0,0,0,
0x3dc8bd36,0x3dfab273,0x3e164083,0x3e2f10a2,0x3e47c5c2,0x3e78cfcc,0x3e94a031,0x3eac7cd4,0x3e94a031,0x3eb8442a,0x3edae880,0x3efc5d27,
0x3ec3ef15,0x3ef15aea,0x3f0e39da,0x3f226799,0x3ef15aea,0x3f13682a,0x3f2beb4a,0x3f41d870,0x3f0e39da,0x3f2beb4a,0x3f45e403,0x3f5b941a,
0x3f226799,0x3f41d870,0x3f5b941a,0x3f6ed89e,0x3f3504f3,0x3f54db31,0x3f6c835e,0x3f7b14be,0x3f45e403,0x3f64aa59,0x3f7853f8,0x3f7fec43,
0x3f54db31,0x3f710908,0x3f7ec46d,0x3f7d3aac,0x3f61c598,0x3f79c79d,0x3f7fb10f,0x3f731447,0x3f6c835e,0x3f7ec46d,0x3f7b14be,0x3f61c598,
0x3f74fa0b,0x3f7fec43,0x3f710908,0x3f49d112,0x3f7b14be,0x3f7d3aac,0x3f61c598,0x3f2beb4a,0x3f7ec46d,0x3f76ba07,0x3f4d9f02,0x3f08f59b,
0,0,0,0,0x3e47c5c2,0x3e605c13,0x3e78cfcc,0x3e888e93,0x3ec3ef15,0x3edae880,0x3ef15aea,0x3f039c3d,
0x3f0e39da,0x3f1d7fd1,0x3f2beb4a,0x3f396842,0x3f3504f3,0x3f45e403,0x3f54db31,0x3f61c598,0x3f54db31,0x3f64aa59,0x3f710908,0x3f79c79d,
0x3f6c835e,0x3f7853f8,0x3f7ec46d,0x3f7fb10f,0x3f7b14be,0x3f7fec43,0x3f7d3aac,0x3f731447,0x3f800000,0x3f7b14be,0x3f6c835e,0x3f54db31,
0x3f7b14be,0x3f6a09a7,0x3f4d9f02,0x3f273656,0x3f6c835e,0x3f4d9f02,0x3f226799,0x3edae880,0x3f54db31,0x3f273656,0x3edae880,0x3e2f10a2,
0x3f3504f3,0x3ef15aea,0x3e47c5c2,0xbdc8bd36,0x3f0e39da,0x3e888e93,0xbd48fb30,0xbeb8442a,0x3ec3ef15,0x3d48fb30,0xbe94a031,0xbf187fc0,
0x3e47c5c2,0xbe2f10a2,0xbf039c3d,0xbf49d112,0,0,0,0,0x3e94a031,0x3ea09ae5,0x3eac7cd4,0x3eb8442a,
0x3f0e39da,0x3f187fc0,0x3f226799,0x3f2beb4a,0x3f45e403,0x3f514d3d,0x3f5b941a,0x3f64aa59,0x3f6c835e,0x3f74fa0b,0x3f7b14be,0x3f7ec46d,
0x3f7ec46d,0x3f7fec43,0x3f7d3aac,0x3f76ba07,0x3f7b14be,0x3f710908,0x3f61c598,0x3f4d9f02,0x3f61c598,0x3f49d112,0x3f2beb4a,0x3f08f59b,
0x3f3504f3,0x3f0e39da,0x3ec3ef15,0x3e47c5c2,0x3ef15aea,0x3e888e93,0x3d48fb30,0xbe2f10a2,0x3e47c5c2,0xbd48fb30,0xbe94a031,0xbf039c3d,
0xbdc8bd36,0xbeb8442a,0xbf187fc0,0xbf49d112,0xbec3ef15,0xbf226799,0xbf54db31,0xbf74fa0b,0xbf226799,0xbf584853,0xbf7853f8,0xbf7f4e6d,
0xbf54db31,0xbf7853f8,0xbf7ec46d,0xbf676bd8,0xbf74fa0b,0xbf7f4e6d,0xbf676bd8,0xbf3085bb,0x3400,0x3400,0x3400,0x3400,
0x3400,0xae1f3364,0xb1a831a8,0xb3642e1f,0x3400,0xb1a831a8,0xb4000000,0xb1a8b1a8,0x3400,0xb3642e1f,0xb1a8b1a8,0x2e1fb364,
0x3400,0xb4000000,0xb400,0x34000000,0x3400,0xb364ae1f,0x31a8b1a8,0x2e1f3364,0x3400,0xb1a8b1a8,0x34000000,0xb1a831a8,
0x3400,0xae1fb364,0x31a831a8,0xb364ae1f,0x3400,0xb400,0x3400,0xb400,0x3400,0x2e1fb364,0xb1a831a8,0x3364ae1f,
0x3400,0x31a8b1a8,0xb4000000,0x31a831a8,0x3400,0x3364ae1f,0xb1a8b1a8,0xae1f3364,0x3400,0x34000000,0xb400,0xb4000000,
0x3400,0x33642e1f,0x31a8b1a8,0xae1fb364,0x3400,0x31a831a8,0x34000000,0x31a8b1a8,0x3400,0x2e1f3364,0x31a831a8,0x33642e1f,
0x3400,0x3400,0x3400,0x3400,0xb4000000,0xb364ae1f,0xb1a8b1a8,0xae1fb364,0xb400,0x31a8b1a8,0x34000000,0x31a831a8,
0x34000000,0x2e1f3364,0xb1a831a8,0xb364ae1f,0x3400,0xb4000000,0xb400,0x34000000,0xb4000000,0x2e1fb364,0x31a831a8,0xb3642e1f,
0xb400,0x31a831a8,0xb4000000,0x31a8b1a8,0x34000000,0xb3642e1f,0x31a8b1a8,0xae1f3364,0x3400,0xb400,0x3400,0xb400,
0xb4000000,0x33642e1f,0xb1a8b1a8,0x2e1f3364,0xb400,0xb1a831a8,0x34000000,0xb1a8b1a8,0x34000000,0xae1fb364,0xb1a831a8,0x33642e1f,
0x3400,0x34000000,0xb400,0xb4000000,0xb4000000,0xae1f3364,0x31a831a8,0x3364ae1f,0xb400,0xb1a8b1a8,0xb4000000,0xb1a831a8,
0x34000000,0x3364ae1f,0x31a8b1a8,0x2e1fb364,0x3400,0x3400,0x3400,0x3400,0xb400,0x2e1fb364,0x31a8b1a8,0x3364ae1f,
0x3400,0xb1a831a8,0xb4000000,0xb1a8b1a8,0xb400,0x3364ae1f,0x31a831a8,0xae1f3364,0x3400,0xb4000000,0xb400,0x34000000,
0xb400,0x33642e1f,0xb1a831a8,0xae1fb364,0x3400,0xb1a8b1a8,0x34000000,0xb1a831a8,0xb400,0x2e1f3364,0xb1a8b1a8,0x33642e1f,
0x3400,0xb400,0x3400,0xb400,0xb400,0xae1f3364,0x31a8b1a8,0xb3642e1f,0x3400,0x31a8b1a8,0xb4000000,0x31a831a8,
0xb400,0xb3642e1f,0x31a831a8,0x2e1fb364,0x3400,0x34000000,0xb400,0xb4000000,0xb400,0xb364ae1f,0xb1a831a8,0x2e1f3364,
0x3400,0x31a831a8,0x34000000,0x31a8b1a8,0xb400,0xae1fb364,0xb1a8b1a8,0xb364ae1f,0x3400,0x3400,0x3400,0x3400,
0x34000000,0x33642e1f,0x31a831a8,0x2e1f3364,0xb400,0x31a8b1a8,0x34000000,0x31a831a8,0xb4000000,0xae1fb364,0x31a8b1a8,0x33642e1f,
0x3400,0xb4000000,0xb400,0x34000000,0x34000000,0xae1f3364,0xb1a8b1a8,0x3364ae1f,0xb400,0x31a831a8,0xb4000000,0x31a8b1a8,
0xb4000000,0x3364ae1f,0xb1a831a8,0x2e1fb364,0x3400,0xb400,0x3400,0xb400,0x34000000,0xb364ae1f,0x31a831a8,0xae1fb364,
0xb400,0xb1a831a8,0x34000000,0xb1a8b1a8,0xb4000000,0x2e1f3364,0x31a8b1a8,0xb364ae1f,0x3400,0x34000000,0xb400,0xb4000000,
0x34000000,0x2e1fb364,0xb1a8b1a8,0xb3642e1f,0xb400,0xb1a8b1a8,0xb4000000,0xb1a831a8,0xb4000000,0xb3642e1f,0xb1a831a8,0xae1f3364,
0x34000000,0x34000000,0x34000000,0x34000000,0x34000000,0x33642e1f,0x31a831a8,0x2e1f3364,0x34000000,0x31a831a8,0x3400,0xb1a831a8,
0x34000000,0x2e1f3364,0xb1a831a8,0xb364ae1f,0x34000000,0x3400,0xb4000000,0xb400,0x34000000,0xae1f3364,0xb1a8b1a8,0x3364ae1f,
0x34000000,0xb1a831a8,0xb400,0x31a831a8,0x34000000,0xb3642e1f,0x31a8b1a8,0xae1f3364,0x34000000,0xb4000000,0x34000000,0xb4000000,
0x34000000,0xb364ae1f,0x31a831a8,0xae1fb364,0x34000000,0xb1a8b1a8,0x3400,0x31a8b1a8,0x34000000,0xae1fb364,0xb1a831a8,0x33642e1f,
0x34000000,0xb400,0xb4000000,0x3400,0x34000000,0x2e1fb364,0xb1a8b1a8,0xb3642e1f,0x34000000,0x31a8b1a8,0xb400,0xb1a8b1a8,
0x34000000,0x3364ae1f,0x31a8b1a8,0x2e1fb364,0x34000000,0x34000000,0x34000000,0x34000000,0x3400,0xae1f3364,0xb1a831a8,0xb3642e1f,
0xb4000000,0xb1a8b1a8,0xb400,0x31a8b1a8,0xb400,0x3364ae1f,0x31a831a8,0xae1f3364,0x34000000,0x3400,0xb4000000,0xb400,
0x3400,0xb364ae1f,0x31a8b1a8,0x2e1f3364,0xb4000000,0x31a8b1a8,0x3400,0xb1a8b1a8,0xb400,0x2e1f3364,0xb1a8b1a8,0x33642e1f,
0x34000000,0xb4000000,0x34000000,0xb4000000,0x3400,0x2e1fb364,0xb1a831a8,0x3364ae1f,0xb4000000,0x31a831a8,0xb400,0xb1a831a8,
0xb400,0xb3642e1f,0x31a831a8,0x2e1fb364,0x34000000,0xb400,0xb4000000,0x3400,0x3400,0x33642e1f,0x31a8b1a8,0xae1fb364,
0xb4000000,0xb1a831a8,0x3400,0x31a831a8,0xb400,0xae1fb364,0xb1a8b1a8,0xb364ae1f,0x34000000,0x34000000,0x34000000,0x34000000,
0xb4000000,0xb364ae1f,0xb1a8b1a8,0xae1fb364,0x34000000,0x31a831a8,0x3400,0xb1a831a8,0xb4000000,0xae1fb364,0x31a8b1a8,0x33642e1f,
0x34000000,0x3400,0xb4000000,0xb400,0xb4000000,0x2e1fb364,0x31a831a8,0xb3642e1f,0x34000000,0xb1a831a8,0xb400,0x31a831a8,
0xb4000000,0x3364ae1f,0xb1a831a8,0x2e1fb364,0x34000000,0xb4000000,0x34000000,0xb4000000,0xb4000000,0x33642e1f,0xb1a8b1a8,0x2e1f3364,
0x34000000,0xb1a8b1a8,0x3400,0x31a8b1a8,0xb4000000,0x2e1f3364,0x31a8b1a8,0xb364ae1f,0x34000000,0xb400,0xb4000000,0x3400,
0xb4000000,0xae1f3364,0x31a831a8,0x3364ae1f,0x34000000,0x31a8b1a8,0xb400,0xb1a8b1a8,0xb4000000,0xb3642e1f,0xb1a831a8,0xae1f3364,
0x34000000,0x34000000,0x34000000,0x34000000,0xb400,0x2e1fb364,0x31a8b1a8,0x3364ae1f,0xb4000000,0xb1a8b1a8,0xb400,0x31a8b1a8,
0x3400,0xb3642e1f,0xb1a8b1a8,0x2e1fb364,0x34000000,0x3400,0xb4000000,0xb400,0xb400,0x33642e1f,0xb1a831a8,0xae1fb364,
0xb4000000,0x31a8b1a8,0x3400,0xb1a8b1a8,0x3400,0xae1fb364,0x31a831a8,0xb364ae1f,0x34000000,0xb4000000,0x34000000,0xb4000000,
0xb400,0xae1f3364,0x31a8b1a8,0xb3642e1f,0xb4000000,0x31a831a8,0xb400,0xb1a831a8,0x3400,0x3364ae1f,0xb1a8b1a8,0xae1f3364,
0x34000000,0xb400,0xb4000000,0x3400,0xb400,0xb364ae1f,0xb1a831a8,0x2e1f3364,0xb4000000,0xb1a831a8,0x3400,0x31a831a8,
0x3400,0x2e1f3364,0x31a831a8,0x33642e1f,0x3400,0x224833fe,0x264633f6,0x28b233ea,0x3400,0x2ed7333b,0x322f3113,0x33c32bc6,
0x3400,0x31ed315f,0x33f6a646,0x30c4b26d,0x3400,0x33882d64,0x3113b22f,0xb01db2dd,0x3400,0x33fea248,0xa646b3f6,0xb3ea28b2,
0x3400,0x333baed7,0xb22fb113,0xabc633c3,0x3400,0x315fb1ed,0xb3f62646,0x326d30c4,0x3400,0x2d64b388,0xb113322f,0x32ddb01d,
0x3400,0xa248b3fe,0x264633f6,0xa8b2b3ea,0x3400,0xaed7b33b,0x322f3113,0xb3c3abc6,0x3400,0xb1edb15f,0x33f6a646,0xb0c4326d,
0x3400,0xb388ad64,0x3113b22f,0x301d32dd,0x3400,0xb3fe2248,0xa646b3f6,0x33eaa8b2,0x3400,0xb33b2ed7,0xb22fb113,0x2bc6b3c3,
0x3400,0xb15f31ed,0xb3f62646,0xb26db0c4,0x3400,0xad643388,0xb113322f,0xb2dd301d,0x2a3e33d9,0x2bc633c3,0x2ca533a8,0x2d643388,
0x33d9aa3e,0x326db0c4,0x2f8bb30e,0x2248b3fe,0xaa3eb3d9,0xb2ddb01d,0xb3a82ca5,0xaed7333b,0xb3d92a3e,0xa8b233ea,0x330e2f8b,0x31edb15f,
0x2a3e33d9,0x33c3abc6,0xaca5b3a8,0xb3882d64,0x33d9aa3e,0xb0c4b26d,0xaf8b330e,0x33fe2248,0xaa3eb3d9,0xb01d32dd,0x33a8aca5,0xb33baed7,
0xb3d92a3e,0x33ea28b2,0xb30eaf8b,0x315f31ed,0x2a3e33d9,0xabc6b3c3,0x2ca533a8,0xad64b388,0x33d9aa3e,0xb26d30c4,0x2f8bb30e,0xa24833fe,
0xaa3eb3d9,0x32dd301d,0xb3a82ca5,0x2ed7b33b,0xb3d92a3e,0x28b2b3ea,0x330e2f8b,0xb1ed315f,0x2a3e33d9,0xb3c32bc6,0xaca5b3a8,0x3388ad64,
0x33d9aa3e,0x30c4326d,0xaf8b330e,0xb3fea248,0xaa3eb3d9,0x301db2dd,0x33a8aca5,0x333b2ed7,0xb3d92a3e,0xb3eaa8b2,0xb30eaf8b,0xb15fb1ed,
0x2e1f3364,0x2ed7333b,0x2f8b330e,0x301d32dd,0xae1fb364,0xb1edb15f,0xb3a8aca5,0xb3ea28b2,0x2e1f3364,0x33882d64,0x330eaf8b,0x2bc6b3c3,
0xae1fb364,0xb3fe2248,0xaca533a8,0x326d30c4,0x2e1f3364,0x333baed7,0xaf8bb30e,0xb2dd301d,0xae1fb364,0xb15f31ed,0x33a82ca5,0xa8b2b3ea,
0x2e1f3364,0x2d64b388,0xb30e2f8b,0x33c32bc6,0xae1fb364,0x224833fe,0x2ca5b3a8,0xb0c4326d,0x2e1f3364,0xaed7b33b,0x2f8b330e,0xb01db2dd,
0xae1fb364,0x31ed315f,0xb3a8aca5,0x33eaa8b2,0x2e1f3364,0xb388ad64,0x330eaf8b,0xabc633c3,0xae1fb364,0x33fea248,0xaca533a8,0xb26db0c4,
0x2e1f3364,0xb33b2ed7,0xaf8bb30e,0x32ddb01d,0xae1fb364,0x315fb1ed,0x33a82ca5,0x28b233ea,0x2e1f3364,0xad643388,0xb30e2f8b,0xb3c3abc6,
0xae1fb364,0xa248b3fe,0x2ca5b3a8,0x30c4b26d,0x307232a7,0x30c4326d,0x3113322f,0x315f31ed,0xb2a73072,0xb01d32dd,0xa64633f6,0x2d643388,
0xb072b2a7,0xb3eaa8b2,0xb22f3113,0xa24833fe,0x32a7b072,0xabc6b3c3,0xb3f6a646,0xaed7333b,0x307232a7,0x326db0c4,0xb113b22f,0xb1ed315f,
0xb2a73072,0x32dd301d,0x2646b3f6,0xb3882d64,0xb072b2a7,0xa8b233ea,0x322fb113,0xb3fea248,0x32a7b072,0xb3c32bc6,0x33f62646,0xb33baed7,
0x307232a7,0xb0c4b26d,0x3113322f,0xb15fb1ed,0xb2a73072,0x301db2dd,0xa64633f6,0xad64b388,0xb072b2a7,0x33ea28b2,0xb22f3113,0x2248b3fe,
0x32a7b072,0x2bc633c3,0xb3f6a646,0x2ed7b33b,0x307232a7,0xb26d30c4,0xb113b22f,0x31edb15f,0xb2a73072,0xb2ddb01d,0x2646b3f6,0x3388ad64,
0xb072b2a7,0x28b2b3ea,0x322fb113,0x33fe2248,0x32a7b072,0x33c3abc6,0x33f62646,0x333b2ed7,0x34000000,0x33fea248,0x33f6a646,0x33eaa8b2,
0x34000000,0x333baed7,0x3113b22f,0x2bc6b3c3,0x34000000,0x315fb1ed,0xa646b3f6,0xb26db0c4,0x34000000,0x2d64b388,0xb22fb113,0xb2dd301d,
0x34000000,0xa248b3fe,0xb3f62646,0x28b233ea,0x34000000,0xaed7b33b,0xb113322f,0x33c32bc6,0x34000000,0xb1edb15f,0x264633f6,0x30c4b26d,
0x34000000,0xb388ad64,0x322f3113,0xb01db2dd,0x34000000,0xb3fe2248,0x33f6a646,0xb3ea28b2,0x34000000,0xb33b2ed7,0x3113b22f,0xabc633c3,
0x34000000,0xb15f31ed,0xa646b3f6,0x326d30c4,0x34000000,0xad643388,0xb22fb113,0x32ddb01d,0x34000000,0x224833fe,0xb3f62646,0xa8b2b3ea,
0x34000000,0x2ed7333b,0xb113322f,0xb3c3abc6,0x34000000,0x31ed315f,0x264633f6,0xb0c4326d,0x34000000,0x33882d64,0x322f3113,0x301d32dd,
0x33d9aa3e,0x33c3abc6,0x33a8aca5,0x3388ad64,0xaa3eb3d9,0xb0c4b26d,0xb30eaf8b,0xb3fea248,0xb3d92a3e,0xb01d32dd,0x2ca533a8,0x333b2ed7,
0x2a3e33d9,0x33ea28b2,0x2f8bb30e,0xb15fb1ed,0x33d9aa3e,0xabc6b3c3,0xb3a82ca5,0x2d643388,0xaa3eb3d9,0xb26d30c4,0x330e2f8b,0x2248b3fe,
0xb3d92a3e,0x32dd301d,0xaca5b3a8,0xaed7333b,0x2a3e33d9,0x28b2b3ea,0xaf8b330e,0x31edb15f,0x33d9aa3e,0xb3c32bc6,0x33a8aca5,0xb3882d64,
0xaa3eb3d9,0x30c4326d,0xb30eaf8b,0x33fe2248,0xb3d92a3e,0x301db2dd,0x2ca533a8,0xb33baed7,0x2a3e33d9,0xb3eaa8b2,0x2f8bb30e,0x315f31ed,
0x33d9aa3e,0x2bc633c3,0xb3a82ca5,0xad64b388,0xaa3eb3d9,0x326db0c4,0x330e2f8b,0xa24833fe,0xb3d92a3e,0xb2ddb01d,0xaca5b3a8,0x2ed7b33b,
0x2a3e33d9,0xa8b233ea,0xaf8b330e,0xb1ed315f,0x3364ae1f,0x333baed7,0x330eaf8b,0x32ddb01d,0xb3642e1f,0xb15f31ed,0xaca533a8,0x28b233ea,
0x3364ae1f,0x2d64b388,0xaf8bb30e,0xb3c3abc6,0xb3642e1f,0x224833fe,0x33a82ca5,0x30c4b26d,0x3364ae1f,0xaed7b33b,0xb30e2f8b,0x301d32dd,
0xb3642e1f,0x31ed315f,0x2ca5b3a8,0xb3ea28b2,0x3364ae1f,0xb388ad64,0x2f8b330e,0x2bc6b3c3,0xb3642e1f,0x33fea248,0xb3a8aca5,0x326d30c4,
0x3364ae1f,0xb33b2ed7,0x330eaf8b,0xb2dd301d,0xb3642e1f,0x315fb1ed,0xaca533a8,0xa8b2b3ea,0x3364ae1f,0xad643388,0xaf8bb30e,0x33c32bc6,
0xb3642e1f,0xa248b3fe,0x33a82ca5,0xb0c4326d,0x3364ae1f,0x2ed7333b,0xb30e2f8b,0xb01db2dd,0xb3642e1f,0xb1edb15f,0x2ca5b3a8,0x33eaa8b2,
0x3364ae1f,0x33882d64,0x2f8b330e,0xabc633c3,0xb3642e1f,0xb3fe2248,0xb3a8aca5,0xb26db0c4,0x32a7b072,0x326db0c4,0x322fb113,0x31edb15f,
0x307232a7,0x32dd301d,0x33f62646,0x3388ad64,0xb2a73072,0xa8b233ea,0x3113322f,0x33fe2248,0xb072b2a7,0xb3c32bc6,0xa64633f6,0x333b2ed7,
0x32a7b072,0xb0c4b26d,0xb22f3113,0x315f31ed,0x307232a7,0x301db2dd,0xb3f6a646,0x2d643388,0xb2a73072,0x33ea28b2,0xb113b22f,0xa24833fe,
0xb072b2a7,0x2bc633c3,0x2646b3f6,0xaed7333b,0x32a7b072,0xb26d30c4,0x322fb113,0xb1ed315f,0x307232a7,0xb2ddb01d,0x33f62646,0xb3882d64,
0xb2a73072,0x28b2b3ea,0x3113322f,0xb3fea248,0xb072b2a7,0x33c3abc6,0xa64633f6,0xb33baed7,0x32a7b072,0x30c4326d,0xb22f3113,0xb15fb1ed,
0x307232a7,0xb01d32dd,0xb3f6a646,0xad64b388,0xb2a73072,0xb3eaa8b2,0xb113b22f,0x2248b3fe,0xb072b2a7,0xabc6b3c3,0x2646b3f6,0x2ed7b33b,
0x3f800000,0x3f7fffb1,0x3f7ffec4,0x3f7ffd39,0x3f800000,0x3f7fe705,0x3f7f9c18,0x3f7f1f49,0x3f800000,0x3f7fa6e3,0x3f7e9bc9,0x3f7cdf6c,
0x3f800000,0x3f7f3f54,0x3f7cfe73,0x3f7940c0,0x3f800000,0x3f7eb069,0x3f7ac516,0x3f74484b,0x3f800000,0x3f7dfa38,0x3f77f110,0x3f6dfcf2,
0x3f800000,0x3f7d1cdd,0x3f748422,0x3f666771,0x3f800000,0x3f7c187a,0x3f708066,0x3f5d924d,0x3f800000,0x3f7aed37,0x3f6be858,0x3f5389c7,
0x3f800000,0x3f799b43,0x3f66becc,0x3f485bca,0x3f800000,0x3f7822d1,0x3f6106f2,0x3f3c17d9,0x3f800000,0x3f76841b,0x3f5ac450,0x3f2ecef7,
0x3f800000,0x3f74bf62,0x3f53fac3,0x3f209393,0x3f800000,0x3f72d4eb,0x3f4cae79,0x3f11796b,0x3f800000,0x3f70c501,0x3f44e3f5,0x3f019573,
0x3f800000,0x3f6e8ff8,0x3f3ca003,0x3ee1fb6a,0x3f7ffb11,0x3f7ff84a,0x3f7ff4e6,0x3f7ff0e3,0x3f7e70b0,0x3f7d906e,0x3f7c7eb0,0x3f7b3bab,
0x3f7a7302,0x3f77583a,0x3f73913f,0x3f6f20b0,0x3f740bdd,0x3f6d67a1,0x3f655e0b,0x3f5bfb34,0x3f6b4b0c,0x3f5fe4e9,0x3f5233c6,0x3f425b89,
0x3f604621,0x3f4f0417,0x3f3a7ca4,0x3f2302d3,0x3f531849,0x3f3b0629,0x3f1ebc12,0x3efdbabb,0x3f43e200,0x3f24381a,0x3eff17b2,0x3eadf73c,
0x3f32c8c9,0x3f0af1b7,0x3ebb31a0,0x3e3228d4,0x3f1ff6cb,0x3edf28a9,0x3e667c66,0x3b490fc6,0x3f0b9a6b,0x3ea5129f,0x3da3308c,0xbe2bf804,
0x3eebcbbb,0x3e5101f1,0xbd8a200a,0xbeab0201,0x3ebe1d4a,0x3da973ba,0xbe5a3997,0xbefafef7,0x3e8e9a22,0xbd2351cb,0xbeb554ec,0xbf21cbfb,
0x3e3b6ecf,0xbe25c58c,0xbef9a02d,0xbf4154e1,0x3dafb680,0xbe901c2c,0xbf1c420c,0xbf5b2c79,0x3f7fec43,0x3f7fe705,0x3f7fe129,0x3f7fdaaf,
0x3f79c79d,0x3f7822d1,0x3f764d97,0x3f74484b,0x3f6a09a7,0x3f644fac,0x3f5df6be,0x3f570348,0x3f514d3d,0x3f456439,0x3f385216,0x3f2a2a80,
0x3f3085bb,0x3f1ce11f,0x3f07a136,0x3ee1fb6a,0x3f08f59b,0x3ed97cb9,0x3e9d9e78,0x3e3e853e,0x3eb8442a,0x3e5d4b1a,0x3d8a200a,0xbda973ba,
0x3e2f10a2,0xbb490fc6,0xbe354098,0xbeb0eacb,0xbcc90ab0,0xbe636c83,0xbed25a09,0xbf1553b7,0xbe605c13,0xbedc53c1,0xbf1ebc12,0xbf476085,
0xbecf7bca,0xbf1e1e22,0xbf4ac77f,0xbf6afb8c,0xbf13682a,0xbf466354,0xbf6aab7b,0xbf7d906e,0xbf396842,0xbf650479,0xbf7c7eb0,0xbf7dc68c,
0xbf584853,0xbf788486,0xbf7f2f9d,0xbf6b99fb,0xbf6ed89e,0xbf7ff0e3,0xbf7294f8,0xbf485bca,0xbf7c3b28,0xbf7aed37,0xbf577026,0xbf16999f,
0x3f7fd397,0x3f7fcbe2,0x3f7fc38f,0x3f7fba9e,0x3f721352,0x3f6faf1b,0x3f6d1c1d,0x3f6a5ad9,0x3f4f7a1f,0x3f476085,0x3f3ebc1b,0x3f3592e7,
0x3f1b02c6,0x3f0af1b7,0x3ef41f07,0x3ed0eb2a,0x3eb263ef,0x3e80f8c0,0x3e1c76de,0x3d55880e,0x3d7b2b74,0xbd83db0a,0xbe419b37,0xbe9f1cdf,
0xbe6c9a7f,0xbebf926f,0xbf0242b1,0xbf21cbfb,0xbf00e7e4,0xbf256bcb,0xbf44e3f5,0xbf5e5aa6,0xbf3b8f3b,0xbf5a5ba0,0xbf708066,0xbf7d1cdd,
0xbf660f88,0xbf7940c0,0xbf7ffec4,0xbf79f35e,0xbf7cbfc9,0xbf7efcc9,0xbf718f57,0xbf554aa4,0xbf7dabcc,0xbf6afb8c,0xbf46e22a,0xbf140c5f,
0xbf68bf3c,0xbf3f41fc,0xbf04f484,0xbe7bdba4,0xbf3fc767,0xbf003a06,0xbe4dee60,0x3df475c0,0xbf064b82,0xbe5101f1,0x3e1008b7,0x3eeff7fb,
0xbe827dc0,0x3de7fa9a,0x3eee9479,0x3f4154e1,0,0xbb490fc6,0xbbc90f88,0xbc16cb58,0,0xbce22a7a,0xbd621469,0xbda973ba,
0,0xbd55880e,0xbdd53db9,0xbe1f917b,0,0xbd9cecf9,0xbe1c76de,0xbe698bba,0,0xbdcefdb7,0xbe4dee60,0xbe9920fe,
0,0xbe007745,0xbe7ee6e1,0xbebca7af,0,0xbe195be0,0xbe97a117,0xbedf28a9,0,0xbe3228d4,0xbeaf713a,0xbf003a06,
0,0xbe4ada4f,0xbec6d529,0xbf102dd5,0,0xbe636c83,0xbeddbe79,0xbf1f599f,0,0xbe7bdba4,0xbef41f07,0xbf2da859,
0,0xbe8a11f7,0xbf04f484,0xbf3b0629,0,0xbe9620d2,0xbf0f8784,0xbf476085,0,0xbea21887,0xbf19c200,0xbf52a649,
0,0xbeadf73c,0xbf239da9,0xbf5cc7d1,0,0xbeb9bb1e,0xbf2d1469,0xbf65b710,0xbc490e90,0xbc7b514b,0xbc96c9b6,0xbcafea69,
0xbde1bc2e,0xbe0cec4a,0xbe28defc,0xbe44b0b9,0xbe541501,0xbe840270,0xbe9d9e78,0xbeb6ccc3,0xbe9aa086,0xbebf926f,0xbee363fa,0xbf02ef9f,
0xbec9b953,0xbef840c8,0xbf121eb0,0xbf269de3,0xbef6e0cb,0xbf16999f,0xbf2f61a5,0xbf456439,0xbf10d3cd,0xbf2ecef7,0xbf48d8b3,0xbf5e5aa6,
0xbf24d225,0xbf446337,0xbf5df6be,0xbf70c501,0xbf373a23,0xbf570348,0xbf6e46be,0xbf7c187a,0xbf47de65,0xbf666771,0xbf796e4e,0xbf7fffb1,
0xbf5695e5,0xbf725470,0xbf7f2f9d,0xbf7c5d3a,0xbf633c5a,0xbf7a9c59,0xbf7f6ac7,0xbf714c7a,0xbf6db293,0xbf7f1f49,0xbf7a1e84,0xbf5f20db,
0xbf75dec6,0xbf7fcbe2,0xbf6f6830,0xbf466354,0xbf7baccd,0xbf7c9f8a,0xbf5f8327,0xbf27ce61,0xbf7f0e58,0xbf75a67b,0xbf4ac77f,0xbf044889,
0xbcc90ab0,0xbce22a7a,0xbcfb49ba,0xbd0a342f,0xbe605c13,0xbe7bdba4,0xbe8b9507,0xbe9920fe,0xbecf7bca,0xbee79a5d,0xbeff17b2,0xbf0af1b7,
0xbf13682a,0xbf2302d3,0xbf31a81d,0xbf3f41fc,0xbf396842,0xbf4a4c87,0xbf591e6a,0xbf65b710,0xbf584853,0xbf67c187,0xbf73913f,0xbf7b87b2,
0xbf6ed89e,0xbf79f35e,0xbf7f6ac7,0xbf7f1f49,0xbf7c3b28,0xbf7fffb1,0xbf7bf531,0xbf703b37,0xbf7fec43,0xbf799b43,0xbf696591,0xbf4fefa8,
0xbf79c79d,0xbf671599,0xbf48d8b3,0xbf209393,0xbf6a09a7,0xbf495521,0xbf1c420c,0xbecb2aae,0xbf514d3d,0xbf21cbfb,0xbecc9b8b,0xbe0cec4a,
0xbf3085bb,0xbee4cbfe,0xbe28defc,0x3e06b26e,0xbf08f59b,0xbe75c35a,0x3da3308c,0x3ec8477c,0xbeb8442a,0xbcafea69,0x3ea395c5,0x3f1f599f,
0xbe2f10a2,0x3e4ada4f,0x3f0a48ad,0x3f4f0417,0xbd16c32c,0xbd2351cb,0xbd2fe007,0xbd3c6dd5,0xbea68f12,0xbeb3dca5,0xbec1071e,0xbece0bea,
0xbf15f6d9,0xbf209393,0xbf2ac082,0xbf34768f,0xbf4bbbf8,0xbf570348,0xbf6106f2,0xbf69b7e4,0xbf6ff573,0xbf77beb7,0xbf7cfe73,0xbf7fa6e3,
0xbf7f84ab,0xbf7f7808,0xbf7b61fc,0xbf73530e,0xbf791298,0xbf6d67a1,0xbf5c61c7,0xbf466354,0xbf5d2d53,0xbf436051,0xbf239da9,0xbefdbabb,
0xbf2e3bde,0xbf05a02c,0xbeaf713a,0xbe195be0,0xbee0924f,0xbe698bba,0xbbc90f88,0x3e5d4b1a,0xbe22abb6,0x3db5f8da,0x3ea986c4,0x3f0d9281,
0x3e09cf86,0x3ecb2aae,0x3f212ff9,0x3f50d937,0x3ed53641,0x3f2a2a80,0x3f5ac450,0x3f7822d1,0x3f299415,0x3f5d924d,0x3f7ac516,0x3f7e2b72,
0x3f59f26a,0x3f7a9c59,0x3f7d7474,0x3f62241a,0x3f778bc5,0x3f7e5a38,0x3f628210,0x3f27ce61,0x3f800000,0x3f800000,0x3f800000,0x3f800000,
0x3f7fffb1,0x3f7fe705,0x3f7fa6e3,0x3f7f3f54,0x3f7ffec4,0x3f7f9c18,0x3f7e9bc9,0x3f7cfe73,0x3f7ffd39,0x3f7f1f49,0x3f7cdf6c,0x3f7940c0,
0x3f7ffb11,0x3f7e70b0,0x3f7a7302,0x3f740bdd,0x3f7ff84a,0x3f7d906e,0x3f77583a,0x3f6d67a1,0x3f7ff4e6,0x3f7c7eb0,0x3f73913f,0x3f655e0b,
0x3f7ff0e3,0x3f7b3bab,0x3f6f20b0,0x3f5bfb34,0x3f7fec43,0x3f79c79d,0x3f6a09a7,0x3f514d3d,0x3f7fe705,0x3f7822d1,0x3f644fac,0x3f456439,
0x3f7fe129,0x3f764d97,0x3f5df6be,0x3f385216,0x3f7fdaaf,0x3f74484b,0x3f570348,0x3f2a2a80,0x3f7fd397,0x3f721352,0x3f4f7a1f,0x3f1b02c6,
0x3f7fcbe2,0x3f6faf1b,0x3f476085,0x3f0af1b7,0x3f7fc38f,0x3f6d1c1d,0x3f3ebc1b,0x3ef41f07,0x3f7fba9e,0x3f6a5ad9,0x3f3592e7,0x3ed0eb2a,
0x3f800000,0x3f800000,0x3f800000,0x3f800000,0x3f7eb069,0x3f7dfa38,0x3f7d1cdd,0x3f7c187a,0x3f7ac516,0x3f77f110,0x3f748422,0x3f708066,
0x3f74484b,0x3f6dfcf2,0x3f666771,0x3f5d924d,0x3f6b4b0c,0x3f604621,0x3f531849,0x3f43e200,0x3f5fe4e9,0x3f4f0417,0x3f3b0629,0x3f24381a,
0x3f5233c6,0x3f3a7ca4,0x3f1ebc12,0x3eff17b2,0x3f425b89,0x3f2302d3,0x3efdbabb,0x3eadf73c,0x3f3085bb,0x3f08f59b,0x3eb8442a,0x3e2f10a2,
0x3f1ce11f,0x3ed97cb9,0x3e5d4b1a,0xbb490fc6,0x3f07a136,0x3e9d9e78,0x3d8a200a,0xbe354098,0x3ee1fb6a,0x3e3e853e,0xbda973ba,0xbeb0eacb,
0x3eb263ef,0x3d7b2b74,0xbe6c9a7f,0xbf00e7e4,0x3e80f8c0,0xbd83db0a,0xbebf926f,0xbf256bcb,0x3e1c76de,0xbe419b37,0xbf0242b1,0xbf44e3f5,
0x3d55880e,0xbe9f1cdf,0xbf21cbfb,0xbf5e5aa6,0x3f800000,0x3f800000,0x3f800000,0x3f800000,0x3f7aed37,0x3f799b43,0x3f7822d1,0x3f76841b,
0x3f6be858,0x3f66becc,0x3f6106f2,0x3f5ac450,0x3f5389c7,0x3f485bca,0x3f3c17d9,0x3f2ecef7,0x3f32c8c9,0x3f1ff6cb,0x3f0b9a6b,0x3eebcbbb,
0x3f0af1b7,0x3edf28a9,0x3ea5129f,0x3e5101f1,0x3ebb31a0,0x3e667c66,0x3da3308c,0xbd8a200a,0x3e3228d4,0x3b490fc6,0xbe2bf804,0xbeab0201,
0xbcc90ab0,0xbe605c13,0xbecf7bca,0xbf13682a,0xbe636c83,0xbedc53c1,0xbf1e1e22,0xbf466354,0xbed25a09,0xbf1ebc12,0xbf4ac77f,0xbf6aab7b,
0xbf1553b7,0xbf476085,0xbf6afb8c,0xbf7d906e,0xbf3b8f3b,0xbf660f88,0xbf7cbfc9,0xbf7dabcc,0xbf5a5ba0,0xbf7940c0,0xbf7efcc9,0xbf6afb8c,
0xbf708066,0xbf7ffec4,0xbf718f57,0xbf46e22a,0xbf7d1cdd,0xbf79f35e,0xbf554aa4,0xbf140c5f,0x3f800000,0x3f800000,0x3f800000,0x3f800000,
0x3f74bf62,0x3f72d4eb,0x3f70c501,0x3f6e8ff8,0x3f53fac3,0x3f4cae79,0x3f44e3f5,0x3f3ca003,0x3f209393,0x3f11796b,0x3f019573,0x3ee1fb6a,
0x3ebe1d4a,0x3e8e9a22,0x3e3b6ecf,0x3dafb680,0x3da973ba,0xbd2351cb,0xbe25c58c,0xbe901c2c,0xbe5a3997,0xbeb554ec,0xbef9a02d,0xbf1c420c,
0xbefafef7,0xbf21cbfb,0xbf4154e1,0xbf5b2c79,0xbf396842,0xbf584853,0xbf6ed89e,0xbf7c3b28,0xbf650479,0xbf788486,0xbf7ff0e3,0xbf7aed37,
0xbf7c7eb0,0xbf7f2f9d,0xbf7294f8,0xbf577026,0xbf7dc68c,0xbf6b99fb,0xbf485bca,0xbf16999f,0xbf68bf3c,0xbf3fc767,0xbf064b82,0xbe827dc0,
0xbf3f41fc,0xbf003a06,0xbe5101f1,0x3de7fa9a,0xbf04f484,0xbe4dee60,0x3e1008b7,0x3eee9479,0xbe7bdba4,0x3df475c0,0x3eeff7fb,0x3f4154e1,
0,0,0,0,0x3b490fc6,0x3ce22a7a,0x3d55880e,0x3d9cecf9,0x3bc90f88,0x3d621469,0x3dd53db9,0x3e1c76de,
0x3c16cb58,0x3da973ba,0x3e1f917b,0x3e698bba,0x3c490e90,0x3de1bc2e,0x3e541501,0x3e9aa086,0x3c7b514b,0x3e0cec4a,0x3e840270,0x3ebf926f,
0x3c96c9b6,0x3e28defc,0x3e9d9e78,0x3ee363fa,0x3cafea69,0x3e44b0b9,0x3eb6ccc3,0x3f02ef9f,0x3cc90ab0,0x3e605c13,0x3ecf7bca,0x3f13682a,
0x3ce22a7a,0x3e7bdba4,0x3ee79a5d,0x3f2302d3,0x3cfb49ba,0x3e8b9507,0x3eff17b2,0x3f31a81d,0x3d0a342f,0x3e9920fe,0x3f0af1b7,0x3f3f41fc,
0x3d16c32c,0x3ea68f12,0x3f15f6d9,0x3f4bbbf8,0x3d2351cb,0x3eb3dca5,0x3f209393,0x3f570348,0x3d2fe007,0x3ec1071e,0x3f2ac082,0x3f6106f2,
0x3d3c6dd5,0x3ece0bea,0x3f34768f,0x3f69b7e4,0,0,0,0,0x3dcefdb7,0x3e007745,0x3e195be0,0x3e3228d4,
0x3e4dee60,0x3e7ee6e1,0x3e97a117,0x3eaf713a,0x3e9920fe,0x3ebca7af,0x3edf28a9,0x3f003a06,0x3ec9b953,0x3ef6e0cb,0x3f10d3cd,0x3f24d225,
0x3ef840c8,0x3f16999f,0x3f2ecef7,0x3f446337,0x3f121eb0,0x3f2f61a5,0x3f48d8b3,0x3f5df6be,0x3f269de3,0x3f456439,0x3f5e5aa6,0x3f70c501,
0x3f396842,0x3f584853,0x3f6ed89e,0x3f7c3b28,0x3f4a4c87,0x3f67c187,0x3f79f35e,0x3f7fffb1,0x3f591e6a,0x3f73913f,0x3f7f6ac7,0x3f7bf531,
0x3f65b710,0x3f7b87b2,0x3f7f1f49,0x3f703b37,0x3f6ff573,0x3f7f84ab,0x3f791298,0x3f5d2d53,0x3f77beb7,0x3f7f7808,0x3f6d67a1,0x3f436051,
0x3f7cfe73,0x3f7b61fc,0x3f5c61c7,0x3f239da9,0x3f7fa6e3,0x3f73530e,0x3f466354,0x3efdbabb,0,0,0,0,
0x3e4ada4f,0x3e636c83,0x3e7bdba4,0x3e8a11f7,0x3ec6d529,0x3eddbe79,0x3ef41f07,0x3f04f484,0x3f102dd5,0x3f1f599f,0x3f2da859,0x3f3b0629,
0x3f373a23,0x3f47de65,0x3f5695e5,0x3f633c5a,0x3f570348,0x3f666771,0x3f725470,0x3f7a9c59,0x3f6e46be,0x3f796e4e,0x3f7f2f9d,0x3f7f6ac7,
0x3f7c187a,0x3f7fffb1,0x3f7c5d3a,0x3f714c7a,0x3f7fec43,0x3f79c79d,0x3f6a09a7,0x3f514d3d,0x3f799b43,0x3f671599,0x3f495521,0x3f21cbfb,
0x3f696591,0x3f48d8b3,0x3f1c420c,0x3ecc9b8b,0x3f4fefa8,0x3f209393,0x3ecb2aae,0x3e0cec4a,0x3f2e3bde,0x3ee0924f,0x3e22abb6,0xbe09cf86,
0x3f05a02c,0x3e698bba,0xbdb5f8da,0xbecb2aae,0x3eaf713a,0x3bc90f88,0xbea986c4,0xbf212ff9,0x3e195be0,0xbe5d4b1a,0xbf0d9281,0xbf50d937,
0,0,0,0,0x3e9620d2,0x3ea21887,0x3eadf73c,0x3eb9bb1e,0x3f0f8784,0x3f19c200,0x3f239da9,0x3f2d1469,
0x3f476085,0x3f52a649,0x3f5cc7d1,0x3f65b710,0x3f6db293,0x3f75dec6,0x3f7baccd,0x3f7f0e58,0x3f7f1f49,0x3f7fcbe2,0x3f7c9f8a,0x3f75a67b,
0x3f7a1e84,0x3f6f6830,0x3f5f8327,0x3f4ac77f,0x3f5f20db,0x3f466354,0x3f27ce61,0x3f044889,0x3f3085bb,0x3f08f59b,0x3eb8442a,0x3e2f10a2,
0x3ee4cbfe,0x3e75c35a,0x3cafea69,0xbe4ada4f,0x3e28defc,0xbda3308c,0xbea395c5,0xbf0a48ad,0xbe06b26e,0xbec8477c,0xbf1f599f,0xbf4f0417,
0xbed53641,0xbf299415,0xbf59f26a,0xbf778bc5,0xbf2a2a80,0xbf5d924d,0xbf7a9c59,0xbf7e5a38,0xbf5ac450,0xbf7ac516,0xbf7d7474,0xbf628210,
0xbf7822d1,0xbf7e2b72,0xbf62241a,0xbf27ce61,0x3400,0x3400,0x3400,0x3400,0xa24833fe,0xaed7333b,0xb1ed315f,0xb3882d64,
0xa64633f6,0xb22f3113,0xb3f6a646,0xb113b22f,0xa8b233ea,0xb3c32bc6,0xb0c4b26d,0x301db2dd,0xaa3e33d9,0xb3d9aa3e,0x2a3eb3d9,0x33d92a3e,
0xabc633c3,0xb26db0c4,0x32ddb01d,0x28b233ea,0xaca533a8,0xaf8bb30e,0x33a82ca5,0xb30e2f8b,0xad643388,0xa248b3fe,0x2ed7333b,0xb1edb15f,
0xae1f3364,0x2e1fb364,0xae1f3364,0x2e1fb364,0xaed7333b,0x31edb15f,0xb3882d64,0x33fe2248,0xaf8b330e,0x33a8aca5,0xb30eaf8b,0x2ca533a8,
0xb01d32dd,0x33ea28b2,0xabc6b3c3,0xb26d30c4,0xb07232a7,0x32a73072,0x3072b2a7,0xb2a7b072,0xb0c4326d,0x301d32dd,0x33eaa8b2,0x2bc6b3c3,
0xb113322f,0x264633f6,0x322f3113,0x33f6a646,0xb15f31ed,0xad643388,0x224833fe,0x2ed7333b,0x3400,0x3400,0x3400,0x3400,
0xb3fea248,0xb33baed7,0xb15fb1ed,0xad64b388,0x2646b3f6,0x322fb113,0x33f62646,0x3113322f,0x33ea28b2,0x2bc633c3,0xb26d30c4,0xb2ddb01d,
0xaa3e33d9,0xb3d9aa3e,0x2a3eb3d9,0x33d92a3e,0xb3c3abc6,0x30c4b26d,0x301d32dd,0xb3ea28b2,0x2ca5b3a8,0x2f8b330e,0xb3a8aca5,0x330eaf8b,
0x33882d64,0xb3fe2248,0x333baed7,0xb15f31ed,0xae1f3364,0x2e1fb364,0xae1f3364,0x2e1fb364,0xb33baed7,0x315f31ed,0xad64b388,0xa24833fe,
0x2f8bb30e,0xb3a82ca5,0x330e2f8b,0xaca5b3a8,0x32dd301d,0x28b2b3ea,0xb3c32bc6,0x30c4326d,0xb07232a7,0x32a73072,0x3072b2a7,0xb2a7b072,
0xb26db0c4,0xb2dd301d,0x28b233ea,0x33c32bc6,0x3113b22f,0xa646b3f6,0xb22fb113,0xb3f62646,0x31ed315f,0x33882d64,0x33fea248,0x333baed7,
0x3400,0x3400,0x3400,0x3400,0x2248b3fe,0x2ed7b33b,0x31edb15f,0x3388ad64,0xa64633f6,0xb22f3113,0xb3f6a646,0xb113b22f,
0x28b2b3ea,0x33c3abc6,0x30c4326d,0xb01d32dd,0xaa3e33d9,0xb3d9aa3e,0x2a3eb3d9,0x33d92a3e,0x2bc6b3c3,0x326d30c4,0xb2dd301d,0xa8b2b3ea,
0xaca533a8,0xaf8bb30e,0x33a82ca5,0xb30e2f8b,0x2d64b388,0x224833fe,0xaed7b33b,0x31ed315f,0xae1f3364,0x2e1fb364,0xae1f3364,0x2e1fb364,
0x2ed7b33b,0xb1ed315f,0x3388ad64,0xb3fea248,0xaf8b330e,0x33a8aca5,0xb30eaf8b,0x2ca533a8,0x301db2dd,0xb3eaa8b2,0x2bc633c3,0x326db0c4,
0xb07232a7,0x32a73072,0x3072b2a7,0xb2a7b072,0x30c4b26d,0xb01db2dd,0xb3ea28b2,0xabc633c3,0xb113322f,0x264633f6,0x322f3113,0x33f6a646,
0x315fb1ed,0x2d64b388,0xa248b3fe,0xaed7b33b,0x3400,0x3400,0x3400,0x3400,0x33fe2248,0x333b2ed7,0x315f31ed,0x2d643388,
0x2646b3f6,0x322fb113,0x33f62646,0x3113322f,0xb3eaa8b2,0xabc6b3c3,0x326db0c4,0x32dd301d,0xaa3e33d9,0xb3d9aa3e,0x2a3eb3d9,0x33d92a3e,
0x33c32bc6,0xb0c4326d,0xb01db2dd,0x33eaa8b2,0x2ca5b3a8,0x2f8b330e,0xb3a8aca5,0x330eaf8b,0xb388ad64,0x33fea248,0xb33b2ed7,0x315fb1ed,
0xae1f3364,0x2e1fb364,0xae1f3364,0x2e1fb364,0x333b2ed7,0xb15fb1ed,0x2d643388,0x2248b3fe,0x2f8bb30e,0xb3a82ca5,0x330e2f8b,0xaca5b3a8,
0xb2ddb01d,0xa8b233ea,0x33c3abc6,0xb0c4b26d,0xb07232a7,0x32a73072,0x3072b2a7,0xb2a7b072,0x326d30c4,0x32ddb01d,0xa8b2b3ea,0xb3c3abc6,
0x3113b22f,0xa646b3f6,0xb22fb113,0xb3f62646,0xb1edb15f,0xb388ad64,0xb3fe2248,0xb33b2ed7,0x34000000,0x34000000,0x34000000,0x34000000,
0x33fe2248,0x333b2ed7,0x315f31ed,0x2d643388,0x33f62646,0x3113322f,0xa64633f6,0xb22f3113,0x33ea28b2,0x2bc633c3,0xb26d30c4,0xb2ddb01d,
0x33d92a3e,0xaa3e33d9,0xb3d9aa3e,0x2a3eb3d9,0x33c32bc6,0xb0c4326d,0xb01db2dd,0x33eaa8b2,0x33a82ca5,0xb30e2f8b,0x2ca5b3a8,0x2f8b330e,
0x33882d64,0xb3fe2248,0x333baed7,0xb15f31ed,0x33642e1f,0xb364ae1f,0x33642e1f,0xb364ae1f,0x333b2ed7,0xb15fb1ed,0x2d643388,0x2248b3fe,
0x330e2f8b,0xaca5b3a8,0xaf8b330e,0x33a8aca5,0x32dd301d,0x28b2b3ea,0xb3c32bc6,0x30c4326d,0x32a73072,0x3072b2a7,0xb2a7b072,0xb07232a7,
0x326d30c4,0x32ddb01d,0xa8b2b3ea,0xb3c3abc6,0x322f3113,0x33f6a646,0x3113b22f,0xa646b3f6,0x31ed315f,0x33882d64,0x33fea248,0x333baed7,
0x34000000,0x34000000,0x34000000,0x34000000,0xa24833fe,0xaed7333b,0xb1ed315f,0xb3882d64,0xb3f6a646,0xb113b22f,0x2646b3f6,0x322fb113,
0x28b2b3ea,0x33c3abc6,0x30c4326d,0xb01d32dd,0x33d92a3e,0xaa3e33d9,0xb3d9aa3e,0x2a3eb3d9,0xabc633c3,0xb26db0c4,0x32ddb01d,0x28b233ea,
0xb3a8aca5,0x330eaf8b,0xaca533a8,0xaf8bb30e,0x2d64b388,0x224833fe,0xaed7b33b,0x31ed315f,0x33642e1f,0xb364ae1f,0x33642e1f,0xb364ae1f,
0xaed7333b,0x31edb15f,0xb3882d64,0x33fe2248,0xb30eaf8b,0x2ca533a8,0x2f8bb30e,0xb3a82ca5,0x301db2dd,0xb3eaa8b2,0x2bc633c3,0x326db0c4,
0x32a73072,0x3072b2a7,0xb2a7b072,0xb07232a7,0xb0c4326d,0x301d32dd,0x33eaa8b2,0x2bc6b3c3,0xb22fb113,0xb3f62646,0xb113322f,0x264633f6,
0x315fb1ed,0x2d64b388,0xa248b3fe,0xaed7b33b,0x34000000,0x34000000,0x34000000,0x34000000,0xb3fea248,0xb33baed7,0xb15fb1ed,0xad64b388,
0x33f62646,0x3113322f,0xa64633f6,0xb22f3113,0xb3eaa8b2,0xabc6b3c3,0x326db0c4,0x32dd301d,0x33d92a3e,0xaa3e33d9,0xb3d9aa3e,0x2a3eb3d9,
0xb3c3abc6,0x30c4b26d,0x301d32dd,0xb3ea28b2,0x33a82ca5,0xb30e2f8b,0x2ca5b3a8,0x2f8b330e,0xb388ad64,0x33fea248,0xb33b2ed7,0x315fb1ed,
0x33642e1f,0xb364ae1f,0x33642e1f,0xb364ae1f,0xb33baed7,0x315f31ed,0xad64b388,0xa24833fe,0x330e2f8b,0xaca5b3a8,0xaf8b330e,0x33a8aca5,
0xb2ddb01d,0xa8b233ea,0x33c3abc6,0xb0c4b26d,0x32a73072,0x3072b2a7,0xb2a7b072,0xb07232a7,0xb26db0c4,0xb2dd301d,0x28b233ea,0x33c32bc6,
0x322f3113,0x33f6a646,0x3113b22f,0xa646b3f6,0xb1edb15f,0xb388ad64,0xb3fe2248,0xb33b2ed7,0x34000000,0x34000000,0x34000000,0x34000000,
0x2248b3fe,0x2ed7b33b,0x31edb15f,0x3388ad64,0xb3f6a646,0xb113b22f,0x2646b3f6,0x322fb113,0xa8b233ea,0xb3c32bc6,0xb0c4b26d,0x301db2dd,
0x33d92a3e,0xaa3e33d9,0xb3d9aa3e,0x2a3eb3d9,0x2bc6b3c3,0x326d30c4,0xb2dd301d,0xa8b2b3ea,0xb3a8aca5,0x330eaf8b,0xaca533a8,0xaf8bb30e,
0xad643388,0xa248b3fe,0x2ed7333b,0xb1edb15f,0x33642e1f,0xb364ae1f,0x33642e1f,0xb364ae1f,0x2ed7b33b,0xb1ed315f,0x3388ad64,0xb3fea248,
0xb30eaf8b,0x2ca533a8,0x2f8bb30e,0xb3a82ca5,0xb01d32dd,0x33ea28b2,0xabc6b3c3,0xb26d30c4,0x32a73072,0x3072b2a7,0xb2a7b072,0xb07232a7,
0x30c4b26d,0xb01db2dd,0xb3ea28b2,0xabc633c3,0xb22fb113,0xb3f62646,0xb113322f,0x264633f6,0xb15f31ed,0xad643388,0x224833fe,0x2ed7333b,
0x3400,0x264633f6,0x2a3e33d9,0x2ca533a8,0x3400,0x2f8b330e,0x32a73072,0x33f62646,0x3400,0x322f3113,0x33d9aa3e,0x2f8bb30e,
0x3400,0x33a82ca5,0x3072b2a7,0xb113b22f,0x3400,0x33f6a646,0xaa3eb3d9,0xb3a82ca5,0x3400,0x330eaf8b,0xb2a7b072,0xa64633f6,
0x3400,0x3113b22f,0xb3d92a3e,0x330e2f8b,0x3400,0x2ca5b3a8,0xb07232a7,0x322fb113,0x3400,0xa646b3f6,0x2a3e33d9,0xaca5b3a8,
0x3400,0xaf8bb30e,0x32a73072,0xb3f6a646,0x3400,0xb22fb113,0x33d9aa3e,0xaf8b330e,0x3400,0xb3a8aca5,0x3072b2a7,0x3113322f,
0x3400,0xb3f62646,0xaa3eb3d9,0x33a8aca5,0x3400,0xb30e2f8b,0xb2a7b072,0x2646b3f6,0x3400,0xb113322f,0xb3d92a3e,0xb30eaf8b,
0x3400,0xaca533a8,0xb07232a7,0xb22f3113,0x2e1f3364,0x2f8b330e,0x307232a7,0x3113322f,0x3364ae1f,0x3113b22f,0x2a3eb3d9,0xaca5b3a8,
0xae1fb364,0xb3a8aca5,0xb2a73072,0xa64633f6,0xb3642e1f,0x264633f6,0x33d92a3e,0x2f8bb30e,0x2e1f3364,0x330eaf8b,0xb072b2a7,0xb22f3113,
0x3364ae1f,0xb22fb113,0xaa3e33d9,0x33a8aca5,0xae1fb364,0xaca533a8,0x32a7b072,0xb3f6a646,0xb3642e1f,0x33f6a646,0xb3d9aa3e,0x330e2f8b,
0x2e1f3364,0xaf8bb30e,0x307232a7,0xb113b22f,0x3364ae1f,0xb113322f,0x2a3eb3d9,0x2ca533a8,0xae1fb364,0x33a82ca5,0xb2a73072,0x2646b3f6,
0xb3642e1f,0xa646b3f6,0x33d92a3e,0xaf8b330e,0x2e1f3364,0xb30e2f8b,0xb072b2a7,0x322fb113,0x3364ae1f,0x322f3113,0xaa3e33d9,0xb3a82ca5,
0xae1fb364,0x2ca5b3a8,0x32a7b072,0x33f62646,0xb3642e1f,0xb3f62646,0xb3d9aa3e,0xb30eaf8b,0x31a831a8,0x322f3113,0x32a73072,0x330e2f8b,
0xb1a8b1a8,0xb3a8aca5,0xb3d92a3e,0xb22f3113,0x31a831a8,0x33f6a646,0x3072b2a7,0xaca5b3a8,0xb1a8b1a8,0xb30e2f8b,0x2a3e33d9,0x33f62646,
0x31a831a8,0x3113b22f,0xb2a7b072,0xaf8b330e,0xb1a8b1a8,0xaca533a8,0x33d9aa3e,0xb113b22f,0x31a831a8,0xa646b3f6,0xb07232a7,0x33a8aca5,
0xb1a8b1a8,0x2f8b330e,0xaa3eb3d9,0xa64633f6,0x31a831a8,0xb22fb113,0x32a73072,0xb30eaf8b,0xb1a8b1a8,0x33a82ca5,0xb3d92a3e,0x322fb113,
0x31a831a8,0xb3f62646,0x3072b2a7,0x2ca533a8,0xb1a8b1a8,0x330eaf8b,0x2a3e33d9,0xb3f6a646,0x31a831a8,0xb113322f,0xb2a7b072,0x2f8bb30e,
0xb1a8b1a8,0x2ca5b3a8,0x33d9aa3e,0x3113322f,0x31a831a8,0x264633f6,0xb07232a7,0xb3a82ca5,0xb1a8b1a8,0xaf8bb30e,0xaa3eb3d9,0x2646b3f6,
0x33642e1f,0x33a82ca5,0x33d92a3e,0x33f62646,0xae1f3364,0x264633f6,0x307232a7,0x330e2f8b,0xb364ae1f,0xb30e2f8b,0xaa3e33d9,0x3113322f,
0x2e1fb364,0xb22fb113,0xb2a73072,0x2ca533a8,0x33642e1f,0x2ca5b3a8,0xb3d9aa3e,0xa64633f6,0xae1f3364,0x33f6a646,0xb072b2a7,0xaf8b330e,
0xb364ae1f,0x2f8b330e,0x2a3eb3d9,0xb22f3113,0x2e1fb364,0xb113322f,0x32a7b072,0xb3a82ca5,0x33642e1f,0xb3a8aca5,0x33d92a3e,0xb3f6a646,
0xae1f3364,0xa646b3f6,0x307232a7,0xb30eaf8b,0xb364ae1f,0x330eaf8b,0xaa3e33d9,0xb113b22f,0x2e1fb364,0x322f3113,0xb2a73072,0xaca5b3a8,
0x33642e1f,0xaca533a8,0xb3d9aa3e,0x2646b3f6,0xae1f3364,0xb3f62646,0xb072b2a7,0x2f8bb30e,0xb364ae1f,0xaf8bb30e,0x2a3eb3d9,0x322fb113,
0x2e1fb364,0x3113b22f,0x32a7b072,0x33a8aca5,0x34000000,0x33f6a646,0x33d9aa3e,0x33a8aca5,0x34000000,0x330eaf8b,0x3072b2a7,0x2646b3f6,
0x34000000,0x3113b22f,0xaa3eb3d9,0xb30eaf8b,0x34000000,0x2ca5b3a8,0xb2a7b072,0xb22f3113,0x34000000,0xa646b3f6,0xb3d92a3e,0x2ca533a8,
0x34000000,0xaf8bb30e,0xb07232a7,0x33f62646,0x34000000,0xb22fb113,0x2a3e33d9,0x2f8bb30e,0x34000000,0xb3a8aca5,0x32a73072,0xb113b22f,
0x34000000,0xb3f62646,0x33d9aa3e,0xb3a82ca5,0x34000000,0xb30e2f8b,0x3072b2a7,0xa64633f6,0x34000000,0xb113322f,0xaa3eb3d9,0x330e2f8b,
0x34000000,0xaca533a8,0xb2a7b072,0x322fb113,0x34000000,0x264633f6,0xb3d92a3e,0xaca5b3a8,0x34000000,0x2f8b330e,0xb07232a7,0xb3f6a646,
0x34000000,0x322f3113,0x2a3e33d9,0xaf8b330e,0x34000000,0x33a82ca5,0x32a73072,0x3113322f,0x3364ae1f,0x330eaf8b,0x32a7b072,0x322fb113,
0xae1fb364,0xb22fb113,0xb3d9aa3e,0xb3a82ca5,0xb3642e1f,0xaca533a8,0x307232a7,0x33f62646,0x2e1f3364,0x33f6a646,0x2a3eb3d9,0xb30eaf8b,
0x3364ae1f,0xaf8bb30e,0xb2a73072,0x3113322f,0xae1fb364,0xb113322f,0x33d92a3e,0xaca5b3a8,0xb3642e1f,0x33a82ca5,0xb072b2a7,0xa64633f6,
0x2e1f3364,0xa646b3f6,0xaa3e33d9,0x2f8bb30e,0x3364ae1f,0xb30e2f8b,0x32a7b072,0xb22f3113,0xae1fb364,0x322f3113,0xb3d9aa3e,0x33a8aca5,
0xb3642e1f,0x2ca5b3a8,0x307232a7,0xb3f6a646,0x2e1f3364,0xb3f62646,0x2a3eb3d9,0x330e2f8b,0x3364ae1f,0x2f8b330e,0xb2a73072,0xb113b22f,
0xae1fb364,0x3113b22f,0x33d92a3e,0x2ca533a8,0xb3642e1f,0xb3a8aca5,0xb072b2a7,0x2646b3f6,0x2e1f3364,0x264633f6,0xaa3e33d9,0xaf8b330e,
0x31a8b1a8,0x3113b22f,0x3072b2a7,0x2f8bb30e,0xb1a831a8,0xaca533a8,0x2a3e33d9,0x3113322f,0x31a8b1a8,0xa646b3f6,0xb2a7b072,0xb3a82ca5,
0xb1a831a8,0x2f8b330e,0x33d9aa3e,0x2646b3f6,0x31a8b1a8,0xb22fb113,0xb07232a7,0x330e2f8b,0xb1a831a8,0x33a82ca5,0xaa3eb3d9,0xb22f3113,
0x31a8b1a8,0xb3f62646,0x32a73072,0xaca5b3a8,0xb1a831a8,0x330eaf8b,0xb3d92a3e,0x33f62646,0x31a8b1a8,0xb113322f,0x3072b2a7,0xaf8b330e,
0xb1a831a8,0x2ca5b3a8,0x2a3e33d9,0xb113b22f,0x31a8b1a8,0x264633f6,0xb2a7b072,0x33a8aca5,0xb1a831a8,0xaf8bb30e,0x33d9aa3e,0xa64633f6,
0x31a8b1a8,0x322f3113,0xb07232a7,0xb30eaf8b,0xb1a831a8,0xb3a8aca5,0xaa3eb3d9,0x322fb113,0x31a8b1a8,0x33f6a646,0x32a73072,0x2ca533a8,
0xb1a831a8,0xb30e2f8b,0xb3d92a3e,0xb3f6a646,0x2e1fb364,0x2ca5b3a8,0x2a3eb3d9,0x2646b3f6,0x33642e1f,0x33f6a646,0x32a7b072,0x2f8bb30e,
0xae1f3364,0x2f8b330e,0x33d92a3e,0x322fb113,0xb364ae1f,0xb113322f,0x307232a7,0x33a8aca5,0x2e1fb364,0xb3a8aca5,0xaa3e33d9,0x33f62646,
0x33642e1f,0xa646b3f6,0xb2a73072,0x330e2f8b,0xae1f3364,0x330eaf8b,0xb3d9aa3e,0x3113322f,0xb364ae1f,0x322f3113,0xb072b2a7,0x2ca533a8,
0x2e1fb364,0xaca533a8,0x2a3eb3d9,0xa64633f6,0x33642e1f,0xb3f62646,0x32a7b072,0xaf8b330e,0xae1f3364,0xaf8bb30e,0x33d92a3e,0xb22f3113,
0xb364ae1f,0x3113b22f,0x307232a7,0xb3a82ca5,0x2e1fb364,0x33a82ca5,0xaa3e33d9,0xb3f6a646,0x33642e1f,0x264633f6,0xb2a73072,0xb30eaf8b,
0xae1f3364,0xb30e2f8b,0xb3d9aa3e,0xb113b22f,0xb364ae1f,0xb22fb113,0xb072b2a7,0xaca5b3a8,0x3f800000,0x3f7ffec4,0x3f7ffb11,0x3f7ff4e6,
0x3f800000,0x3f7fe129,0x3f7f84ab,0x3f7eea9d,0x3f800000,0x3f7f9c18,0x3f7e70b0,0x3f7c7eb0,0x3f800000,0x3f7f2f9d,0x3f7cbfc9,0x3f78b47b,
0x3f800000,0x3f7e9bc9,0x3f7a7302,0x3f73913f,0x3f800000,0x3f7de0b1,0x3f778bc5,0x3f6d1c1d,0x3f800000,0x3f7cfe73,0x3f740bdd,0x3f655e0b,
0x3f800000,0x3f7bf531,0x3f6ff573,0x3f5c61c7,0x3f800000,0x3f7ac516,0x3f6b4b0c,0x3f5233c6,0x3f800000,0x3f796e4e,0x3f660f88,0x3f46e22a,
0x3f800000,0x3f77f110,0x3f604621,0x3f3a7ca4,0x3f800000,0x3f764d97,0x3f59f26a,0x3f2d1469,0x3f800000,0x3f748422,0x3f531849,0x3f1ebc12,
0x3f800000,0x3f7294f8,0x3f4bbbf8,0x3f0f8784,0x3f800000,0x3f708066,0x3f43e200,0x3eff17b2,0x3f800000,0x3f6e46be,0x3f3b8f3b,0x3eddbe79,
0x3f7fec43,0x3f7fe129,0x3f7fd397,0x3f7fc38f,0x3f7e1324,0x3f7cfe73,0x3f7baccd,0x3f7a1e84,0x3f79c79d,0x3f764d97,0x3f721352,0x3f6d1c1d,
0x3f731447,0x3f6be858,0x3f633c5a,0x3f591e6a,0x3f6a09a7,0x3f5df6be,0x3f4f7a1f,0x3f3ebc1b,0x3f5ebe05,0x3f4cae79,0x3f373a23,0x3f1ebc12,
0x3f514d3d,0x3f385216,0x3f1b02c6,0x3ef41f07,0x3f41d870,0x3f212ff9,0x3ef6e0cb,0x3ea395c5,0x3f3085bb,0x3f07a136,0x3eb263ef,0x3e1c76de,
0x3f1d7fd1,0x3ed8106b,0x3e541501,0xbc96c9b6,0x3f08f59b,0x3e9d9e78,0x3d7b2b74,0xbe419b37,0x3ee63375,0x3e419b37,0xbdafb680,0xbeb554ec,
0x3eb8442a,0x3d8a200a,0xbe6c9a7f,0xbf0242b1,0x3e888e93,0xbd621469,0xbebe1d4a,0xbf26050a,0x3e2f10a2,0xbe354098,0xbf00e7e4,0xbf44e3f5,
0x3d96a905,0xbe97a117,0xbf1ff6cb,0xbf5df6be,0x3f7fb10f,0x3f7f9c18,0x3f7f84ab,0x3f7f6ac7,0x3f7853f8,0x3f764d97,0x3f740bdd,0x3f718f57,
0x3f676bd8,0x3f6106f2,0x3f59f26a,0x3f5233c6,0x3f4d9f02,0x3f40d0da,0x3f32c8c9,0x3f239da9,0x3f2beb4a,0x3f173c07,0x3f00e7e4,0x3ed25a09,
0x3f039c3d,0x3ecc9b8b,0x3e8e9a22,0x3e1c76de,0x3eac7cd4,0x3e419b37,0x3d16c32c,0xbdee3876,0x3e164083,0xbcfb49ba,0xbe541501,0xbec1071e,
0xbd48fb30,0xbe7ee6e1,0xbee0924f,0xbf1c420c,0xbe78cfcc,0xbee900b7,0xbf24d225,0xbf4cae79,0xbedae880,0xbf239da9,0xbf4f7a1f,0xbf6e46be,
0xbf187fc0,0xbf4ac77f,0xbf6db293,0xbf7e9bc9,0xbf3daef9,0xbf6816a8,0xbf7dabcc,0xbf7c7eb0,0xbf5b941a,0xbf7a1e84,0xbf7e70b0,0xbf6816a8,
0xbf710908,0xbf7ffec4,0xbf6ff573,0xbf42de29,0xbf7d3aac,0xbf796e4e,0xbf531849,0xbf0f8784,0x3f7f4e6d,0x3f7f2f9d,0x3f7f0e58,0x3f7eea9d,
0x3f6ed89e,0x3f6be858,0x3f68bf3c,0x3f655e0b,0x3f49d112,0x3f40d0da,0x3f373a23,0x3f2d1469,0x3f13682a,0x3f0242b1,0x3ee0924f,0x3ebb31a0,
0x3ea09ae5,0x3e5a3997,0x3de1bc2e,0x3bc90f88,0x3cc90ab0,0xbdd53db9,0xbe6c9a7f,0xbeb554ec,0xbe888e93,0xbed25a09,0xbf0b9a6b,0xbf2ac082,
0xbf08f59b,0xbf2d1469,0xbf4bbbf8,0xbf63f473,0xbf41d870,0xbf5f8327,0xbf740bdd,0xbf7e9bc9,0xbf6a09a7,0xbf7b61fc,0xbf7fd397,0xbf772417,
0xbf7e1324,0xbf7de0b1,0xbf6db293,0xbf4e8d90,0xbf7c3b28,0xbf66becc,0xbf3fc767,0xbf0a48ad,0xbf64aa59,0xbf385216,0xbef6e0cb,0xbe4dee60,
0xbf396842,0xbeee9479,0xbe22abb6,0x3e28defc,0xbefc5d27,0xbe28defc,0x3e3b6ecf,0x3f0242b1,0xbe605c13,0x3e1c76de,0x3f00e7e4,0x3f48d8b3,
0,0xbbc90f88,0xbc490e90,0xbc96c9b6,0,0xbcfb49ba,0xbd7b2b74,0xbdbc3ac3,0,0xbd621469,0xbde1bc2e,0xbe28defc,
0,0xbda3308c,0xbe22abb6,0xbe72b651,0,0xbdd53db9,0xbe541501,0xbe9d9e78,0,0xbe039502,0xbe827dc0,0xbec1071e,
0,0xbe1c76de,0xbe9aa086,0xbee363fa,0,0xbe354098,0xbeb263ef,0xbf0242b1,0,0xbe4dee60,0xbec9b953,0xbf121eb0,
0,0xbe667c66,0xbee0924f,0xbf212ff9,0,0xbe7ee6e1,0xbef6e0cb,0xbf2f61a5,0,0xbe8b9507,0xbf064b82,0xbf3ca003,
0,0xbe97a117,0xbf10d3cd,0xbf48d8b3,0,0xbea395c5,0xbf1b02c6,0xbf53fac3,0,0xbeaf713a,0xbf24d225,0xbf5df6be,
0,0xbebb31a0,0xbf2e3bde,0xbf66becc,0xbcc90ab0,0xbcfb49ba,0xbd16c32c,0xbd2fe007,0xbdfab273,0xbe1c76de,0xbe3b6ecf,0xbe5a3997,
0xbe605c13,0xbe8b9507,0xbea68f12,0xbec1071e,0xbea09ae5,0xbec6d529,0xbeebcbbb,0xbf07a136,0xbecf7bca,0xbeff17b2,0xbf15f6d9,0xbf2ac082,
0xbefc5d27,0xbf19c200,0xbf32c8c9,0xbf48d8b3,0xbf13682a,0xbf31a81d,0xbf4bbbf8,0xbf6106f2,0xbf273656,0xbf46e22a,0xbf604621,0xbf7294f8,
0xbf396842,0xbf591e6a,0xbf6ff573,0xbf7cfe73,0xbf49d112,0xbf6816a8,0xbf7a7302,0xbf7ff4e6,0xbf584853,0xbf73913f,0xbf7f84ab,0xbf7b61fc,
0xbf64aa59,0xbf7b61fc,0xbf7f0e58,0xbf6f6830,0xbf6ed89e,0xbf7f6ac7,0xbf791298,0xbf5c61c7,0xbf76ba07,0xbf7f9c18,0xbf6db293,0xbf42de29,
0xbf7c3b28,0xbf7bf531,0xbf5d2d53,0xbf239da9,0xbf7f4e6d,0xbf748422,0xbf47de65,0xbeff17b2,0xbd48fb30,0xbd621469,0xbd7b2b74,0xbd8a200a,
0xbe78cfcc,0xbe8b9507,0xbe9aa086,0xbea986c4,0xbedae880,0xbef41f07,0xbf064b82,0xbf121eb0,0xbf187fc0,0xbf286605,0xbf373a23,0xbf44e3f5,
0xbf3daef9,0xbf4e8d90,0xbf5d2d53,0xbf696591,0xbf5b941a,0xbf6aab7b,0xbf75dec6,0xbf7cfe73,0xbf710908,0xbf7b61fc,0xbf7fd397,0xbf7e4323,
0xbf7d3aac,0xbf7fe129,0xbf7a7302,0xbf6d1c1d,0xbf7fb10f,0xbf77f110,0xbf660f88,0xbf4ac77f,0xbf7853f8,0xbf63f473,0xbf43e200,0xbf19c200,
0xbf676bd8,0xbf44e3f5,0xbf15f6d9,0xbebb31a0,0xbf4d9f02,0xbf1c420c,0xbebe1d4a,0xbdd53db9,0xbf2beb4a,0xbed8106b,0xbe09cf86,0x3e28defc,
0xbf039c3d,0xbe5a3997,0x3de1bc2e,0x3ed8106b,0xbeac7cd4,0x3bc90f88,0x3eb263ef,0x3f26050a,0xbe164083,0x3e667c66,0x3f10d3cd,0x3f53fac3,
0xbd96a905,0xbda3308c,0xbdafb680,0xbdbc3ac3,0xbeb8442a,0xbec6d529,0xbed53641,0xbee363fa,0xbf1d7fd1,0xbf286605,0xbf32c8c9,0xbf3ca003,
0xbf514d3d,0xbf5c61c7,0xbf660f88,0xbf6e46be,0xbf731447,0xbf7a1e84,0xbf7e70b0,0xbf7ffec4,0xbf7fec43,0xbf7e9bc9,0xbf791298,0xbf6f6830,
0xbf76ba07,0xbf696591,0xbf5695e5,0xbf3ebc1b,0xbf584853,0xbf3ca003,0xbf1b02c6,0xbee900b7,0xbf273656,0xbef9a02d,0xbe9aa086,0xbdd53db9,
0xbecf7bca,0xbe419b37,0x3d16c32c,0x3e8586ce,0xbdfab273,0x3e039502,0x3ebe1d4a,0x3f173c07,0x3e2f10a2,0x3eddbe79,0x3f299415,0x3f577026,
0x3ee63375,0x3f31a81d,0x3f604621,0x3f7ac516,0x3f3085bb,0x3f628210,0x3f7cbfc9,0x3f7c7eb0,0x3f5ebe05,0x3f7c7eb0,0x3f7baccd,0x3f5c61c7,
0x3f79c79d,0x3f7cfe73,0x3f5d2d53,0x3f1ebc12,0x3f800000,0x3f800000,0x3f800000,0x3f800000,0x3f7ffec4,0x3f7fe129,0x3f7f9c18,0x3f7f2f9d,
0x3f7ffb11,0x3f7f84ab,0x3f7e70b0,0x3f7cbfc9,0x3f7ff4e6,0x3f7eea9d,0x3f7c7eb0,0x3f78b47b,0x3f7fec43,0x3f7e1324,0x3f79c79d,0x3f731447,
0x3f7fe129,0x3f7cfe73,0x3f764d97,0x3f6be858,0x3f7fd397,0x3f7baccd,0x3f721352,0x3f633c5a,0x3f7fc38f,0x3f7a1e84,0x3f6d1c1d,0x3f591e6a,
0x3f7fb10f,0x3f7853f8,0x3f676bd8,0x3f4d9f02,0x3f7f9c18,0x3f764d97,0x3f6106f2,0x3f40d0da,0x3f7f84ab,0x3f740bdd,0x3f59f26a,0x3f32c8c9,
0x3f7f6ac7,0x3f718f57,0x3f5233c6,0x3f239da9,0x3f7f4e6d,0x3f6ed89e,0x3f49d112,0x3f13682a,0x3f7f2f9d,0x3f6be858,0x3f40d0da,0x3f0242b1,
0x3f7f0e58,0x3f68bf3c,0x3f373a23,0x3ee0924f,0x3f7eea9d,0x3f655e0b,0x3f2d1469,0x3ebb31a0,0x3f800000,0x3f800000,0x3f800000,0x3f800000,
0x3f7e9bc9,0x3f7de0b1,0x3f7cfe73,0x3f7bf531,0x3f7a7302,0x3f778bc5,0x3f740bdd,0x3f6ff573,0x3f73913f,0x3f6d1c1d,0x3f655e0b,0x3f5c61c7,
0x3f6a09a7,0x3f5ebe05,0x3f514d3d,0x3f41d870,0x3f5df6be,0x3f4cae79,0x3f385216,0x3f212ff9,0x3f4f7a1f,0x3f373a23,0x3f1b02c6,0x3ef6e0cb,
0x3f3ebc1b,0x3f1ebc12,0x3ef41f07,0x3ea395c5,0x3f2beb4a,0x3f039c3d,0x3eac7cd4,0x3e164083,0x3f173c07,0x3ecc9b8b,0x3e419b37,0xbcfb49ba,
0x3f00e7e4,0x3e8e9a22,0x3d16c32c,0xbe541501,0x3ed25a09,0x3e1c76de,0xbdee3876,0xbec1071e,0x3ea09ae5,0x3cc90ab0,0xbe888e93,0xbf08f59b,
0x3e5a3997,0xbdd53db9,0xbed25a09,0xbf2d1469,0x3de1bc2e,0xbe6c9a7f,0xbf0b9a6b,0xbf4bbbf8,0x3bc90f88,0xbeb554ec,0xbf2ac082,0xbf63f473,
0x3f800000,0x3f800000,0x3f800000,0x3f800000,0x3f7ac516,0x3f796e4e,0x3f77f110,0x3f764d97,0x3f6b4b0c,0x3f660f88,0x3f604621,0x3f59f26a,
0x3f5233c6,0x3f46e22a,0x3f3a7ca4,0x3f2d1469,0x3f3085bb,0x3f1d7fd1,0x3f08f59b,0x3ee63375,0x3f07a136,0x3ed8106b,0x3e9d9e78,0x3e419b37,
0x3eb263ef,0x3e541501,0x3d7b2b74,0xbdafb680,0x3e1c76de,0xbc96c9b6,0xbe419b37,0xbeb554ec,0xbd48fb30,0xbe78cfcc,0xbedae880,0xbf187fc0,
0xbe7ee6e1,0xbee900b7,0xbf239da9,0xbf4ac77f,0xbee0924f,0xbf24d225,0xbf4f7a1f,0xbf6db293,0xbf1c420c,0xbf4cae79,0xbf6e46be,0xbf7e9bc9,
0xbf41d870,0xbf6a09a7,0xbf7e1324,0xbf7c3b28,0xbf5f8327,0xbf7b61fc,0xbf7de0b1,0xbf66becc,0xbf740bdd,0xbf7fd397,0xbf6db293,0xbf3fc767,
0xbf7e9bc9,0xbf772417,0xbf4e8d90,0xbf0a48ad,0x3f800000,0x3f800000,0x3f800000,0x3f800000,0x3f748422,0x3f7294f8,0x3f708066,0x3f6e46be,
0x3f531849,0x3f4bbbf8,0x3f43e200,0x3f3b8f3b,0x3f1ebc12,0x3f0f8784,0x3eff17b2,0x3eddbe79,0x3eb8442a,0x3e888e93,0x3e2f10a2,0x3d96a905,
0x3d8a200a,0xbd621469,0xbe354098,0xbe97a117,0xbe6c9a7f,0xbebe1d4a,0xbf00e7e4,0xbf1ff6cb,0xbf0242b1,0xbf26050a,0xbf44e3f5,0xbf5df6be,
0xbf3daef9,0xbf5b941a,0xbf710908,0xbf7d3aac,0xbf6816a8,0xbf7a1e84,0xbf7ffec4,0xbf796e4e,0xbf7dabcc,0xbf7e70b0,0xbf6ff573,0xbf531849,
0xbf7c7eb0,0xbf6816a8,0xbf42de29,0xbf0f8784,0xbf64aa59,0xbf396842,0xbefc5d27,0xbe605c13,0xbf385216,0xbeee9479,0xbe28defc,0x3e1c76de,
0xbef6e0cb,0xbe22abb6,0x3e3b6ecf,0x3f00e7e4,0xbe4dee60,0x3e28defc,0x3f0242b1,0x3f48d8b3,0,0,0,0,
0x3bc90f88,0x3cfb49ba,0x3d621469,0x3da3308c,0x3c490e90,0x3d7b2b74,0x3de1bc2e,0x3e22abb6,0x3c96c9b6,0x3dbc3ac3,0x3e28defc,0x3e72b651,
0x3cc90ab0,0x3dfab273,0x3e605c13,0x3ea09ae5,0x3cfb49ba,0x3e1c76de,0x3e8b9507,0x3ec6d529,0x3d16c32c,0x3e3b6ecf,0x3ea68f12,0x3eebcbbb,
0x3d2fe007,0x3e5a3997,0x3ec1071e,0x3f07a136,0x3d48fb30,0x3e78cfcc,0x3edae880,0x3f187fc0,0x3d621469,0x3e8b9507,0x3ef41f07,0x3f286605,
0x3d7b2b74,0x3e9aa086,0x3f064b82,0x3f373a23,0x3d8a200a,0x3ea986c4,0x3f121eb0,0x3f44e3f5,0x3d96a905,0x3eb8442a,0x3f1d7fd1,0x3f514d3d,
0x3da3308c,0x3ec6d529,0x3f286605,0x3f5c61c7,0x3dafb680,0x3ed53641,0x3f32c8c9,0x3f660f88,0x3dbc3ac3,0x3ee363fa,0x3f3ca003,0x3f6e46be,
0,0,0,0,0x3dd53db9,0x3e039502,0x3e1c76de,0x3e354098,0x3e541501,0x3e827dc0,0x3e9aa086,0x3eb263ef,
0x3e9d9e78,0x3ec1071e,0x3ee363fa,0x3f0242b1,0x3ecf7bca,0x3efc5d27,0x3f13682a,0x3f273656,0x3eff17b2,0x3f19c200,0x3f31a81d,0x3f46e22a,
0x3f15f6d9,0x3f32c8c9,0x3f4bbbf8,0x3f604621,0x3f2ac082,0x3f48d8b3,0x3f6106f2,0x3f7294f8,0x3f3daef9,0x3f5b941a,0x3f710908,0x3f7d3aac,
0x3f4e8d90,0x3f6aab7b,0x3f7b61fc,0x3f7fe129,0x3f5d2d53,0x3f75dec6,0x3f7fd397,0x3f7a7302,0x3f696591,0x3f7cfe73,0x3f7e4323,0x3f6d1c1d,
0x3f731447,0x3f7fec43,0x3f76ba07,0x3f584853,0x3f7a1e84,0x3f7e9bc9,0x3f696591,0x3f3ca003,0x3f7e70b0,0x3f791298,0x3f5695e5,0x3f1b02c6,
0x3f7ffec4,0x3f6f6830,0x3f3ebc1b,0x3ee900b7,0,0,0,0,0x3e4dee60,0x3e667c66,0x3e7ee6e1,0x3e8b9507,
0x3ec9b953,0x3ee0924f,0x3ef6e0cb,0x3f064b82,0x3f121eb0,0x3f212ff9,0x3f2f61a5,0x3f3ca003,0x3f396842,0x3f49d112,0x3f584853,0x3f64aa59,
0x3f591e6a,0x3f6816a8,0x3f73913f,0x3f7b61fc,0x3f6ff573,0x3f7a7302,0x3f7f84ab,0x3f7f0e58,0x3f7cfe73,0x3f7ff4e6,0x3f7b61fc,0x3f6f6830,
0x3f7fb10f,0x3f7853f8,0x3f676bd8,0x3f4d9f02,0x3f77f110,0x3f63f473,0x3f44e3f5,0x3f1c420c,0x3f660f88,0x3f43e200,0x3f15f6d9,0x3ebe1d4a,
0x3f4ac77f,0x3f19c200,0x3ebb31a0,0x3dd53db9,0x3f273656,0x3ecf7bca,0x3dfab273,0xbe2f10a2,0x3ef9a02d,0x3e419b37,0xbe039502,0xbeddbe79,
0x3e9aa086,0xbd16c32c,0xbebe1d4a,0xbf299415,0x3dd53db9,0xbe8586ce,0xbf173c07,0xbf577026,0,0,0,0,
0x3e97a117,0x3ea395c5,0x3eaf713a,0x3ebb31a0,0x3f10d3cd,0x3f1b02c6,0x3f24d225,0x3f2e3bde,0x3f48d8b3,0x3f53fac3,0x3f5df6be,0x3f66becc,
0x3f6ed89e,0x3f76ba07,0x3f7c3b28,0x3f7f4e6d,0x3f7f6ac7,0x3f7f9c18,0x3f7bf531,0x3f748422,0x3f791298,0x3f6db293,0x3f5d2d53,0x3f47de65,
0x3f5c61c7,0x3f42de29,0x3f239da9,0x3eff17b2,0x3f2beb4a,0x3f039c3d,0x3eac7cd4,0x3e164083,0x3ed8106b,0x3e5a3997,0xbbc90f88,0xbe667c66,
0x3e09cf86,0xbde1bc2e,0xbeb263ef,0xbf10d3cd,0xbe28defc,0xbed8106b,0xbf26050a,0xbf53fac3,0xbee63375,0xbf3085bb,0xbf5ebe05,0xbf79c79d,
0xbf31a81d,0xbf628210,0xbf7c7eb0,0xbf7cfe73,0xbf604621,0xbf7cbfc9,0xbf7baccd,0xbf5d2d53,0xbf7ac516,0xbf7c7eb0,0xbf5c61c7,0xbf1ebc12,
0x3400,0x3400,0x3400,0x3400,0xa64633f6,0xaf8b330e,0xb22f3113,0xb3a82ca5,0xaa3e33d9,0xb2a73072,0xb3d9aa3e,0xb072b2a7,
0xaca533a8,0xb3f62646,0xaf8bb30e,0x3113b22f,0xae1f3364,0xb364ae1f,0x2e1fb364,0x33642e1f,0xaf8b330e,0xb113b22f,0x33a8aca5,0xa64633f6,
0xb07232a7,0xaa3eb3d9,0x32a73072,0xb3d92a3e,0xb113322f,0x2ca5b3a8,0x264633f6,0xaf8bb30e,0xb1a831a8,0x31a8b1a8,0xb1a831a8,0x31a8b1a8,
0xb22f3113,0x33a8aca5,0xb3f6a646,0x330e2f8b,0xb2a73072,0x33d92a3e,0xb072b2a7,0xaa3e33d9,0xb30e2f8b,0x322f3113,0x2ca5b3a8,0xb3f62646,
0xb3642e1f,0x2e1f3364,0x3364ae1f,0xae1fb364,0xb3a82ca5,0xa64633f6,0x330e2f8b,0x322fb113,0xb3d92a3e,0xb07232a7,0x2a3e33d9,0x32a73072,
0xb3f62646,0xb30e2f8b,0xb113322f,0xaca533a8,0x3400,0x3400,0x3400,0x3400,0xb3f6a646,0xb30eaf8b,0xb113b22f,0xaca5b3a8,
0x2a3eb3d9,0x32a7b072,0x33d92a3e,0x307232a7,0x33a82ca5,0x264633f6,0xb30e2f8b,0xb22fb113,0xae1f3364,0xb364ae1f,0x2e1fb364,0x33642e1f,
0xb30eaf8b,0x322fb113,0x2ca533a8,0xb3f6a646,0x3072b2a7,0x2a3e33d9,0xb2a7b072,0x33d9aa3e,0x322f3113,0xb3a8aca5,0x33f6a646,0xb30e2f8b,
0xb1a831a8,0x31a8b1a8,0xb1a831a8,0x31a8b1a8,0xb113b22f,0x2ca533a8,0x2646b3f6,0xaf8b330e,0x32a7b072,0xb3d9aa3e,0x307232a7,0x2a3eb3d9,
0x2f8b330e,0x3113b22f,0xb3a8aca5,0x264633f6,0xb3642e1f,0x2e1f3364,0x3364ae1f,0xae1fb364,0xaca5b3a8,0xb3f6a646,0xaf8b330e,0x3113322f,
0x33d9aa3e,0x3072b2a7,0xaa3eb3d9,0xb2a7b072,0x264633f6,0x2f8b330e,0x322f3113,0x33a82ca5,0x3400,0x3400,0x3400,0x3400,
0x2646b3f6,0x2f8bb30e,0x322fb113,0x33a8aca5,0xaa3e33d9,0xb2a73072,0xb3d9aa3e,0xb072b2a7,0x2ca5b3a8,0x33f6a646,0x2f8b330e,0xb113322f,
0xae1f3364,0xb364ae1f,0x2e1fb364,0x33642e1f,0x2f8bb30e,0x3113322f,0xb3a82ca5,0x2646b3f6,0xb07232a7,0xaa3eb3d9,0x32a73072,0xb3d92a3e,
0x3113b22f,0xaca533a8,0xa646b3f6,0x2f8b330e,0xb1a831a8,0x31a8b1a8,0xb1a831a8,0x31a8b1a8,0x322fb113,0xb3a82ca5,0x33f62646,0xb30eaf8b,
0xb2a73072,0x33d92a3e,0xb072b2a7,0xaa3e33d9,0x330eaf8b,0xb22fb113,0xaca533a8,0x33f6a646,0xb3642e1f,0x2e1f3364,0x3364ae1f,0xae1fb364,
0x33a8aca5,0x2646b3f6,0xb30eaf8b,0xb22f3113,0xb3d92a3e,0xb07232a7,0x2a3e33d9,0x32a73072,0x33f6a646,0x330eaf8b,0x3113b22f,0x2ca5b3a8,
0x3400,0x3400,0x3400,0x3400,0x33f62646,0x330e2f8b,0x3113322f,0x2ca533a8,0x2a3eb3d9,0x32a7b072,0x33d92a3e,0x307232a7,
0xb3a8aca5,0xa646b3f6,0x330eaf8b,0x322f3113,0xae1f3364,0xb364ae1f,0x2e1fb364,0x33642e1f,0x330e2f8b,0xb22f3113,0xaca5b3a8,0x33f62646,
0x3072b2a7,0x2a3e33d9,0xb2a7b072,0x33d9aa3e,0xb22fb113,0x33a82ca5,0xb3f62646,0x330eaf8b,0xb1a831a8,0x31a8b1a8,0xb1a831a8,0x31a8b1a8,
0x3113322f,0xaca5b3a8,0xa64633f6,0x2f8bb30e,0x32a7b072,0xb3d9aa3e,0x307232a7,0x2a3eb3d9,0xaf8bb30e,0xb113322f,0x33a82ca5,0xa646b3f6,
0xb3642e1f,0x2e1f3364,0x3364ae1f,0xae1fb364,0x2ca533a8,0x33f62646,0x2f8bb30e,0xb113b22f,0x33d9aa3e,0x3072b2a7,0xaa3eb3d9,0xb2a7b072,
0xa646b3f6,0xaf8bb30e,0xb22fb113,0xb3a8aca5,0x34000000,0x34000000,0x34000000,0x34000000,0x33f62646,0x330e2f8b,0x3113322f,0x2ca533a8,
0x33d92a3e,0x307232a7,0xaa3e33d9,0xb2a73072,0x33a82ca5,0x264633f6,0xb30e2f8b,0xb22fb113,0x33642e1f,0xae1f3364,0xb364ae1f,0x2e1fb364,
0x330e2f8b,0xb22f3113,0xaca5b3a8,0x33f62646,0x32a73072,0xb3d92a3e,0x3072b2a7,0x2a3e33d9,0x322f3113,0xb3a8aca5,0x33f6a646,0xb30e2f8b,
0x31a831a8,0xb1a8b1a8,0x31a831a8,0xb1a8b1a8,0x3113322f,0xaca5b3a8,0xa64633f6,0x2f8bb30e,0x307232a7,0x2a3eb3d9,0xb2a73072,0x33d92a3e,
0x2f8b330e,0x3113b22f,0xb3a8aca5,0x264633f6,0x2e1f3364,0x3364ae1f,0xae1fb364,0xb3642e1f,0x2ca533a8,0x33f62646,0x2f8bb30e,0xb113b22f,
0x2a3e33d9,0x32a73072,0x33d9aa3e,0x3072b2a7,0x264633f6,0x2f8b330e,0x322f3113,0x33a82ca5,0x34000000,0x34000000,0x34000000,0x34000000,
0xa64633f6,0xaf8b330e,0xb22f3113,0xb3a82ca5,0xb3d9aa3e,0xb072b2a7,0x2a3eb3d9,0x32a7b072,0x2ca5b3a8,0x33f6a646,0x2f8b330e,0xb113322f,
0x33642e1f,0xae1f3364,0xb364ae1f,0x2e1fb364,0xaf8b330e,0xb113b22f,0x33a8aca5,0xa64633f6,0xb2a7b072,0x33d9aa3e,0xb07232a7,0xaa3eb3d9,
0x3113b22f,0xaca533a8,0xa646b3f6,0x2f8b330e,0x31a831a8,0xb1a8b1a8,0x31a831a8,0xb1a8b1a8,0xb22f3113,0x33a8aca5,0xb3f6a646,0x330e2f8b,
0xb072b2a7,0xaa3e33d9,0x32a7b072,0xb3d9aa3e,0x330eaf8b,0xb22fb113,0xaca533a8,0x33f6a646,0x2e1f3364,0x3364ae1f,0xae1fb364,0xb3642e1f,
0xb3a82ca5,0xa64633f6,0x330e2f8b,0x322fb113,0xaa3eb3d9,0xb2a7b072,0xb3d92a3e,0xb07232a7,0x33f6a646,0x330eaf8b,0x3113b22f,0x2ca5b3a8,
0x34000000,0x34000000,0x34000000,0x34000000,0xb3f6a646,0xb30eaf8b,0xb113b22f,0xaca5b3a8,0x33d92a3e,0x307232a7,0xaa3e33d9,0xb2a73072,
0xb3a8aca5,0xa646b3f6,0x330eaf8b,0x322f3113,0x33642e1f,0xae1f3364,0xb364ae1f,0x2e1fb364,0xb30eaf8b,0x322fb113,0x2ca533a8,0xb3f6a646,
0x32a73072,0xb3d92a3e,0x3072b2a7,0x2a3e33d9,0xb22fb113,0x33a82ca5,0xb3f62646,0x330eaf8b,0x31a831a8,0xb1a8b1a8,0x31a831a8,0xb1a8b1a8,
0xb113b22f,0x2ca533a8,0x2646b3f6,0xaf8b330e,0x307232a7,0x2a3eb3d9,0xb2a73072,0x33d92a3e,0xaf8bb30e,0xb113322f,0x33a82ca5,0xa646b3f6,
0x2e1f3364,0x3364ae1f,0xae1fb364,0xb3642e1f,0xaca5b3a8,0xb3f6a646,0xaf8b330e,0x3113322f,0x2a3e33d9,0x32a73072,0x33d9aa3e,0x3072b2a7,
0xa646b3f6,0xaf8bb30e,0xb22fb113,0xb3a8aca5,0x34000000,0x34000000,0x34000000,0x34000000,0x2646b3f6,0x2f8bb30e,0x322fb113,0x33a8aca5,
0xb3d9aa3e,0xb072b2a7,0x2a3eb3d9,0x32a7b072,0xaca533a8,0xb3f62646,0xaf8bb30e,0x3113b22f,0x33642e1f,0xae1f3364,0xb364ae1f,0x2e1fb364,
0x2f8bb30e,0x3113322f,0xb3a82ca5,0x2646b3f6,0xb2a7b072,0x33d9aa3e,0xb07232a7,0xaa3eb3d9,0xb113322f,0x2ca5b3a8,0x264633f6,0xaf8bb30e,
0x31a831a8,0xb1a8b1a8,0x31a831a8,0xb1a8b1a8,0x322fb113,0xb3a82ca5,0x33f62646,0xb30eaf8b,0xb072b2a7,0xaa3e33d9,0x32a7b072,0xb3d9aa3e,
0xb30e2f8b,0x322f3113,0x2ca5b3a8,0xb3f62646,0x2e1f3364,0x3364ae1f,0xae1fb364,0xb3642e1f,0x33a8aca5,0x2646b3f6,0xb30eaf8b,0xb22f3113,
0xaa3eb3d9,0xb2a7b072,0xb3d92a3e,0xb07232a7,0xb3f62646,0xb30e2f8b,0xb113322f,0xaca533a8,0x3400,0x28b233ea,0x2ca533a8,0x2ed7333b,
0x3400,0x301d32dd,0x330e2f8b,0x33fea248,0x3400,0x326d30c4,0x33a8aca5,0x2d64b388,0x3400,0x33c32bc6,0x2f8bb30e,0xb1edb15f,
0x3400,0x33eaa8b2,0xaca5b3a8,0xb33b2ed7,0x3400,0x32ddb01d,0xb30eaf8b,0x224833fe,0x3400,0x30c4b26d,0xb3a82ca5,0x33882d64,
0x3400,0x2bc6b3c3,0xaf8b330e,0x315fb1ed,0x3400,0xa8b2b3ea,0x2ca533a8,0xaed7b33b,0x3400,0xb01db2dd,0x330e2f8b,0xb3fe2248,
0x3400,0xb26db0c4,0x33a8aca5,0xad643388,0x3400,0xb3c3abc6,0x2f8bb30e,0x31ed315f,0x3400,0xb3ea28b2,0xaca5b3a8,0x333baed7,
0x3400,0xb2dd301d,0xb30eaf8b,0xa248b3fe,0x3400,0xb0c4326d,0xb3a82ca5,0xb388ad64,0x3400,0xabc633c3,0xaf8b330e,0xb15f31ed,
0x307232a7,0x315f31ed,0x322f3113,0x32dd301d,0x32a7b072,0x2ed7b33b,0xa646b3f6,0xb0c4b26d,0xb072b2a7,0xb3fea248,0xb113322f,0x2bc633c3,
0xb2a73072,0x2d643388,0x33f6a646,0x28b2b3ea,0x307232a7,0x31edb15f,0xb22fb113,0xb01d32dd,0x32a7b072,0xb33baed7,0x264633f6,0x326db0c4,
0xb072b2a7,0xa24833fe,0x3113b22f,0xb3c32bc6,0xb2a73072,0x3388ad64,0xb3f62646,0x33ea28b2,0x307232a7,0xb15fb1ed,0x322f3113,0xb2ddb01d,
0x32a7b072,0xaed7333b,0xa646b3f6,0x30c4326d,0xb072b2a7,0x33fe2248,0xb113322f,0xabc6b3c3,0xb2a73072,0xad64b388,0x33f6a646,0xa8b233ea,
0x307232a7,0xb1ed315f,0xb22fb113,0x301db2dd,0x32a7b072,0x333b2ed7,0x264633f6,0xb26d30c4,0xb072b2a7,0x2248b3fe,0x3113b22f,0x33c3abc6,
0xb2a73072,0xb3882d64,0xb3f62646,0xb3eaa8b2,0x33642e1f,0x33c32bc6,0x33f62646,0x33fea248,0xb364ae1f,0xb3ea28b2,0xb22f3113,0xad643388,
0x33642e1f,0x32ddb01d,0x2646b3f6,0xb1edb15f,0xb364ae1f,0xb0c4326d,0x3113322f,0x333baed7,0x33642e1f,0x2bc6b3c3,0xb3f6a646,0x224833fe,
0xb364ae1f,0x28b233ea,0x322fb113,0xb388ad64,0x33642e1f,0xb01db2dd,0xa64633f6,0x315fb1ed,0xb364ae1f,0x326d30c4,0xb113b22f,0x2ed7333b,
0x33642e1f,0xb3c3abc6,0x33f62646,0xb3fe2248,0xb364ae1f,0x33eaa8b2,0xb22f3113,0x2d64b388,0x33642e1f,0xb2dd301d,0x2646b3f6,0x31ed315f,
0xb364ae1f,0x30c4b26d,0x3113322f,0xb33b2ed7,0x33642e1f,0xabc633c3,0xb3f6a646,0xa248b3fe,0xb364ae1f,0xa8b2b3ea,0x322fb113,0x33882d64,
0x33642e1f,0x301d32dd,0xa64633f6,0xb15f31ed,0xb364ae1f,0xb26db0c4,0xb113b22f,0xaed7b33b,0x33d9aa3e,0x3388ad64,0x330eaf8b,0x326db0c4,
0x2a3e33d9,0x315f31ed,0x33a82ca5,0x33c3abc6,0xb3d92a3e,0xaed7333b,0x2f8b330e,0x33ea28b2,0xaa3eb3d9,0xb3fea248,0xaca533a8,0x32dd301d,
0x33d9aa3e,0xad64b388,0xb30e2f8b,0x30c4326d,0x2a3e33d9,0x31edb15f,0xb3a8aca5,0x2bc633c3,0xb3d92a3e,0x333b2ed7,0xaf8bb30e,0xa8b233ea,
0xaa3eb3d9,0xa24833fe,0x2ca5b3a8,0xb01d32dd,0x33d9aa3e,0xb3882d64,0x330eaf8b,0xb26d30c4,0x2a3e33d9,0xb15fb1ed,0x33a82ca5,0xb3c32bc6,
0xb3d92a3e,0x2ed7b33b,0x2f8b330e,0xb3eaa8b2,0xaa3eb3d9,0x33fe2248,0xaca533a8,0xb2ddb01d,0x33d9aa3e,0x2d643388,0xb30e2f8b,0xb0c4b26d,
0x2a3e33d9,0xb1ed315f,0xb3a8aca5,0xabc6b3c3,0xb3d92a3e,0xb33baed7,0xaf8bb30e,0x28b2b3ea,0xaa3eb3d9,0x2248b3fe,0x2ca5b3a8,0x301db2dd,
0x34000000,0x33eaa8b2,0x33a8aca5,0x333baed7,0x34000000,0x32ddb01d,0x2f8bb30e,0xa248b3fe,0x34000000,0x30c4b26d,0xaca5b3a8,0xb388ad64,
0x34000000,0x2bc6b3c3,0xb30eaf8b,0xb15f31ed,0x34000000,0xa8b2b3ea,0xb3a82ca5,0x2ed7333b,0x34000000,0xb01db2dd,0xaf8b330e,0x33fea248,
0x34000000,0xb26db0c4,0x2ca533a8,0x2d64b388,0x34000000,0xb3c3abc6,0x330e2f8b,0xb1edb15f,0x34000000,0xb3ea28b2,0x33a8aca5,0xb33b2ed7,
0x34000000,0xb2dd301d,0x2f8bb30e,0x224833fe,0x34000000,0xb0c4326d,0xaca5b3a8,0x33882d64,0x34000000,0xabc633c3,0xb30eaf8b,0x315fb1ed,
0x34000000,0x28b233ea,0xb3a82ca5,0xaed7b33b,0x34000000,0x301d32dd,0xaf8b330e,0xb3fe2248,0x34000000,0x326d30c4,0x2ca533a8,0xad643388,
0x34000000,0x33c32bc6,0x330e2f8b,0x31ed315f,0x32a7b072,0x31edb15f,0x3113b22f,0x301db2dd,0xb072b2a7,0xb33baed7,0xb3f62646,0xb26d30c4,
0xb2a73072,0xa24833fe,0x322f3113,0x33c3abc6,0x307232a7,0x3388ad64,0xa646b3f6,0xb3eaa8b2,0x32a7b072,0xb15fb1ed,0xb113322f,0x32dd301d,
0xb072b2a7,0xaed7333b,0x33f6a646,0xb0c4b26d,0xb2a73072,0x33fe2248,0xb22fb113,0x2bc633c3,0x307232a7,0xad64b388,0x264633f6,0x28b2b3ea,
0x32a7b072,0xb1ed315f,0x3113b22f,0xb01d32dd,0xb072b2a7,0x333b2ed7,0xb3f62646,0x326db0c4,0xb2a73072,0x2248b3fe,0x322f3113,0xb3c32bc6,
0x307232a7,0xb3882d64,0xa646b3f6,0x33ea28b2,0x32a7b072,0x315f31ed,0xb113322f,0xb2ddb01d,0xb072b2a7,0x2ed7b33b,0x33f6a646,0x30c4326d,
0xb2a73072,0xb3fea248,0xb22fb113,0xabc6b3c3,0x307232a7,0x2d643388,0x264633f6,0xa8b233ea,0x2e1fb364,0x2bc6b3c3,0x2646b3f6,0xa248b3fe,
0xae1f3364,0x28b233ea,0x3113322f,0x33882d64,0x2e1fb364,0xb01db2dd,0xb3f6a646,0xb15f31ed,0xae1f3364,0x326d30c4,0x322fb113,0xaed7b33b,
0x2e1fb364,0xb3c3abc6,0xa64633f6,0x33fea248,0xae1f3364,0x33eaa8b2,0xb113b22f,0xad643388,0x2e1fb364,0xb2dd301d,0x33f62646,0xb1edb15f,
0xae1f3364,0x30c4b26d,0xb22f3113,0x333baed7,0x2e1fb364,0xabc633c3,0x2646b3f6,0x224833fe,0xae1f3364,0xa8b2b3ea,0x3113322f,0xb388ad64,
0x2e1fb364,0x301d32dd,0xb3f6a646,0x315fb1ed,0xae1f3364,0xb26db0c4,0x322fb113,0x2ed7333b,0x2e1fb364,0x33c32bc6,0xa64633f6,0xb3fe2248,
0xae1f3364,0xb3ea28b2,0xb113b22f,0x2d64b388,0x2e1fb364,0x32ddb01d,0x33f62646,0x31ed315f,0xae1f3364,0xb0c4326d,0xb22f3113,0xb33b2ed7,
0xaa3eb3d9,0xad64b388,0xaf8bb30e,0xb0c4b26d,0x33d9aa3e,0x31edb15f,0x2ca5b3a8,0xabc6b3c3,0x2a3e33d9,0x333b2ed7,0x330eaf8b,0x28b2b3ea,
0xb3d92a3e,0xa24833fe,0x33a82ca5,0x301db2dd,0xaa3eb3d9,0xb3882d64,0x2f8b330e,0x326db0c4,0x33d9aa3e,0xb15fb1ed,0xaca533a8,0x33c3abc6,
0x2a3e33d9,0x2ed7b33b,0xb30e2f8b,0x33ea28b2,0xb3d92a3e,0x33fe2248,0xb3a8aca5,0x32dd301d,0xaa3eb3d9,0x2d643388,0xaf8bb30e,0x30c4326d,
0x33d9aa3e,0xb1ed315f,0x2ca5b3a8,0x2bc633c3,0x2a3e33d9,0xb33baed7,0x330eaf8b,0xa8b233ea,0xb3d92a3e,0x2248b3fe,0x33a82ca5,0xb01d32dd,
0xaa3eb3d9,0x3388ad64,0x2f8b330e,0xb26d30c4,0x33d9aa3e,0x315f31ed,0xaca533a8,0xb3c32bc6,0x2a3e33d9,0xaed7333b,0xb30e2f8b,0xb3eaa8b2,
0xb3d92a3e,0xb3fea248,0xb3a8aca5,0xb2ddb01d,0x3f800000,0x3f7ffd39,0x3f7ff4e6,0x3f7fe705,0x3f800000,0x3f7fdaaf,0x3f7f6ac7,0x3f7eb069,
0x3f800000,0x3f7f90b1,0x3f7e4323,0x3f7c187a,0x3f800000,0x3f7f1f49,0x3f7c7eb0,0x3f7822d1,0x3f800000,0x3f7e868b,0x3f7a1e84,0x3f72d4eb,
0x3f800000,0x3f7dc68c,0x3f772417,0x3f6c3624,0x3f800000,0x3f7cdf6c,0x3f73913f,0x3f644fac,0x3f800000,0x3f7bd14d,0x3f6f6830,0x3f5b2c79,
0x3f800000,0x3f7a9c59,0x3f6aab7b,0x3f50d937,0x3f800000,0x3f7940c0,0x3f655e0b,0x3f456439,0x3f800000,0x3f77beb7,0x3f5f8327,0x3f38dd65,
0x3f800000,0x3f76167a,0x3f591e6a,0x3f2b561b,0x3f800000,0x3f74484b,0x3f5233c6,0x3f1ce11f,0x3f800000,0x3f725470,0x3f4ac77f,0x3f0d9281,
0x3f800000,0x3f703b37,0x3f42de29,0x3efafef7,0x3f800000,0x3f6dfcf2,0x3f3a7ca4,0x3ed97cb9,0x3f7fd397,0x3f7fba9e,0x3f7f9c18,0x3f7f7808,
0x3f7dabcc,0x3f7c5d3a,0x3f7ac516,0x3f78e3d6,0x3f791298,0x3f75341d,0x3f708066,0x3f6afb8c,0x3f721352,0x3f6a5ad9,0x3f6106f2,0x3f5627fe,
0x3f68bf3c,0x3f5bfb34,0x3f4cae79,0x3f3b0629,0x3f5d2d53,0x3f4a4c87,0x3f33e7bc,0x3f1a6293,0x3f4f7a1f,0x3f3592e7,0x3f173c07,0x3eea6681,
0x3f3fc767,0x3f1e1e22,0x3eee9479,0x3e9920fe,0x3f2e3bde,0x3f044889,0x3ea986c4,0x3e06b26e,0x3f1b02c6,0x3ed0eb2a,0x3e419b37,0xbd2351cb,
0x3f064b82,0x3e9620d2,0x3d2fe007,0xbe57278f,0x3ee0924f,0x3e3228d4,0xbdd53db9,0xbebf926f,0x3eb263ef,0x3d55880e,0xbe7ee6e1,0xbf06f686,
0x3e827dc0,0xbd9064b4,0xbec6d529,0xbf2a2a80,0x3e22abb6,0xbe44b0b9,0xbf04f484,0xbf485bca,0x3d7b2b74,0xbe9f1cdf,0xbf239da9,0xbf60a6cf,
0x3f7f4e6d,0x3f7f1f49,0x3f7eea9d,0x3f7eb069,0x3f76ba07,0x3f74484b,0x3f718f57,0x3f6e8ff8,0x3f64aa59,0x3f5d924d,0x3f55b993,0x3f4d26fd,
0x3f49d112,0x3f3c17d9,0x3f2d1469,0x3f1ce11f,0x3f273656,0x3f11796b,0x3ef41f07,0x3ec27b55,0x3efc5d27,0x3ebf926f,0x3e7ee6e1,0x3df475c0,
0x3ea09ae5,0x3e25c58c,0x3bc90f88,0xbe195be0,0x3dfab273,0xbd6ea038,0xbe72b651,0xbed0eb2a,0xbd96a905,0xbe8d17c0,0xbeee9479,0xbf2302d3,
0xbe888e93,0xbef58035,0xbf2ac082,0xbf51c0c2,0xbee63375,0xbf28fd41,0xbf53fac3,0xbf714c7a,0xbf1d7fd1,0xbf4f0417,0xbf708066,0xbf7f5ce9,
0xbf41d870,0xbf6afb8c,0xbf7e9bc9,0xbf7aed37,0xbf5ebe05,0xbf7b87b2,0xbf7d7474,0xbf644fac,0xbf731447,0xbf7fdaaf,0xbf6d1c1d,0xbf3d27b8,
0xbf7e1324,0xbf77beb7,0xbf4e8d90,0xbf084b92,0x3f7e70b0,0x3f7e2b72,0x3f7de0b1,0x3f7d906e,0x3f6b4b0c,0x3f67c187,0x3f63f473,0x3f5fe4e9,
0x3f43e200,0x3f39f2ac,0x3f2f61a5,0x3f24381a,0x3f0b9a6b,0x3ef2bd43,0x3ecc9b8b,0x3ea5129f,0x3e8e9a22,0x3e3228d4,0x3d8a200a,0xbd2351cb,
0xbc490e90,0xbe1324ca,0xbe8b9507,0xbecb2aae,0xbe9aa086,0xbee4cbfe,0xbf14b039,0xbf33587a,0xbf10d3cd,0xbf34768f,0xbf5233c6,0xbf6912ae,
0xbf47de65,0xbf644fac,0xbf772417,0xbf7f90b1,0xbf6db293,0xbf7d1cdd,0xbf7f2f9d,0xbf73ced9,0xbf7f0e58,0xbf7c5d3a,0xbf696591,0xbf476085,
0xbf7a7302,0xbf62241a,0xbf385216,0xbf003a06,0xbf604621,0xbf311722,0xbee363fa,0xbe1f917b,0xbf32c8c9,0xbedc53c1,0xbdee3876,0x3e57278f,
0xbeebcbbb,0xbe007745,0x3e667c66,0x3f0c42c9,0xbe3b6ecf,0x3e44b0b9,0x3f0a48ad,0x3f4fefa8,0,0xbc16cb58,0xbc96c9b6,0xbce22a7a,
0,0xbd0a342f,0xbd8a200a,0xbdcefdb7,0,0xbd6ea038,0xbdee3876,0xbe3228d4,0,0xbda973ba,0xbe28defc,0xbe7bdba4,
0,0xbddb7d37,0xbe5a3997,0xbea21887,0,0xbe06b26e,0xbe8586ce,0xbec5625c,0,0xbe1f917b,0xbe9d9e78,0xbee79a5d,
0,0xbe3857ec,0xbeb554ec,0xbf044889,0,0xbe5101f1,0xbecc9b8b,0xbf140c5f,0,0xbe698bba,0xbee363fa,0xbf2302d3,
0,0xbe80f8c0,0xbef9a02d,0xbf311722,0,0xbe8d17c0,0xbf07a136,0xbf3e35c5,0,0xbe9920fe,0xbf121eb0,0xbf4a4c87,
0,0xbea5129f,0xbf1c420c,0xbf554aa4,0,0xbeb0eacb,0xbf26050a,0xbf5f20db,0,0xbebca7af,0xbf2f61a5,0xbf67c187,
0xbd16c32c,0xbd3c6dd5,0xbd621469,0xbd83db0a,0xbe09cf86,0xbe2bf804,0xbe4dee60,0xbe6fa8b2,0xbe6c9a7f,0xbe931f35,0xbeaf713a,0xbecb2aae,
0xbea68f12,0xbece0bea,0xbef41f07,0xbf0c42c9,0xbed53641,0xbf02ef9f,0xbf19c200,0xbf2ecef7,0xbf00e7e4,0xbf1ce11f,0xbf36206c,0xbf4c3578,
0xbf15f6d9,0xbf34768f,0xbf4e8d90,0xbf6398ac,0xbf299415,0xbf495521,0xbf628210,0xbf74484b,0xbf3b8f3b,0xbf5b2c79,0xbf718f57,0xbf7dc68c,
0xbf4bbbf8,0xbf69b7e4,0xbf7b61fc,0xbf7fcbe2,0xbf59f26a,0xbf74bf62,0xbf7fc38f,0xbf7a4910,0xbf660f88,0xbf7c187a,0xbf7e9bc9,0xbf6d67a1,
0xbf6ff573,0xbf7fa6e3,0xbf77f110,0xbf5988ad,0xbf778bc5,0xbf7f5ce9,0xbf6be858,0xbf3f41fc,0xbf7cbfc9,0xbf7b3bab,0xbf5ac450,0xbf1f599f,
0xbf7f84ab,0xbf73530e,0xbf44e3f5,0xbef58035,0xbd96a905,0xbda973ba,0xbdbc3ac3,0xbdcefdb7,0xbe888e93,0xbe9920fe,0xbea986c4,0xbeb9bb1e,
0xbee63375,0xbf003a06,0xbf0cead0,0xbf19210f,0xbf1d7fd1,0xbf2da859,0xbf3ca003,0xbf4a4c87,0xbf41d870,0xbf52a649,0xbf6106f2,0xbf6cd007,
0xbf5ebe05,0xbf6d67a1,0xbf77f110,0xbf7e2b72,0xbf731447,0xbf7c9f8a,0xbf7ffec4,0xbf7d1cdd,0xbf7e1324,0xbf7f90b1,0xbf78b47b,0xbf69b7e4,
0xbf7f4e6d,0xbf76167a,0xbf628210,0xbf456439,0xbf76ba07,0xbf60a6cf,0xbf3ebc1b,0xbf12c39a,0xbf64aa59,0xbf404c5c,0xbf0f8784,0xbeab0201,
0xbf49d112,0xbf16999f,0xbeaf713a,0xbd9064b4,0xbf273656,0xbecb2aae,0xbdd53db9,0x3e4ada4f,0xbefc5d27,0xbe3e853e,0x3e1008b7,0x3ee79a5d,
0xbea09ae5,0x3d0a342f,0x3ec1071e,0x3f2c800f,0xbdfab273,0x3e80f8c0,0x3f173c07,0x3f58b3a1,0xbde1bc2e,0xbdf475c0,0xbe039502,0xbe0cec4a,
0xbec9b953,0xbed97cb9,0xbee900b7,0xbef840c8,0xbf24d225,0xbf2ff3e6,0xbf3a7ca4,0xbf446337,0xbf5695e5,0xbf61668a,0xbf6aab7b,0xbf725470,
0xbf75dec6,0xbf7c187a,0xbf7f6ac7,0xbf7fcbe2,0xbf7ffb11,0xbf7d57de,0xbf764d97,0xbf6afb8c,0xbf740bdd,0xbf650479,0xbf5064af,0xbf36ad7f,
0xbf531849,0xbf3592e7,0xbf121eb0,0xbed3c867,0xbf1ff6cb,0xbee79a5d,0xbe8586ce,0xbd6ea038,0xbebe1d4a,0xbe195be0,0x3da3308c,0x3e9c1faf,
0xbdafb680,0x3e2bf804,0x3ed25a09,0x3f209393,0x3e541501,0x3eeff7fb,0x3f31a81d,0x3f5d924d,0x3ef6e0cb,0x3f38dd65,0x3f655e0b,0x3f7cdf6c,
0x3f373a23,0x3f671599,0x3f7e4323,0x3f7a4910,0x3f633c5a,0x3f7dfa38,0x3f796e4e,0x3f5627fe,0x3f7baccd,0x3f7b3bab,0x3f577026,0x3f1553b7,
0x3f800000,0x3f800000,0x3f800000,0x3f800000,0x3f7ffd39,0x3f7fdaaf,0x3f7f90b1,0x3f7f1f49,0x3f7ff4e6,0x3f7f6ac7,0x3f7e4323,0x3f7c7eb0,
0x3f7fe705,0x3f7eb069,0x3f7c187a,0x3f7822d1,0x3f7fd397,0x3f7dabcc,0x3f791298,0x3f721352,0x3f7fba9e,0x3f7c5d3a,0x3f75341d,0x3f6a5ad9,
0x3f7f9c18,0x3f7ac516,0x3f708066,0x3f6106f2,0x3f7f7808,0x3f78e3d6,0x3f6afb8c,0x3f5627fe,0x3f7f4e6d,0x3f76ba07,0x3f64aa59,0x3f49d112,
0x3f7f1f49,0x3f74484b,0x3f5d924d,0x3f3c17d9,0x3f7eea9d,0x3f718f57,0x3f55b993,0x3f2d1469,0x3f7eb069,0x3f6e8ff8,0x3f4d26fd,0x3f1ce11f,
0x3f7e70b0,0x3f6b4b0c,0x3f43e200,0x3f0b9a6b,0x3f7e2b72,0x3f67c187,0x3f39f2ac,0x3ef2bd43,0x3f7de0b1,0x3f63f473,0x3f2f61a5,0x3ecc9b8b,
0x3f7d906e,0x3f5fe4e9,0x3f24381a,0x3ea5129f,0x3f800000,0x3f800000,0x3f800000,0x3f800000,0x3f7e868b,0x3f7dc68c,0x3f7cdf6c,0x3f7bd14d,
0x3f7a1e84,0x3f772417,0x3f73913f,0x3f6f6830,0x3f72d4eb,0x3f6c3624,0x3f644fac,0x3f5b2c79,0x3f68bf3c,0x3f5d2d53,0x3f4f7a1f,0x3f3fc767,
0x3f5bfb34,0x3f4a4c87,0x3f3592e7,0x3f1e1e22,0x3f4cae79,0x3f33e7bc,0x3f173c07,0x3eee9479,0x3f3b0629,0x3f1a6293,0x3eea6681,0x3e9920fe,
0x3f273656,0x3efc5d27,0x3ea09ae5,0x3dfab273,0x3f11796b,0x3ebf926f,0x3e25c58c,0xbd6ea038,0x3ef41f07,0x3e7ee6e1,0x3bc90f88,0xbe72b651,
0x3ec27b55,0x3df475c0,0xbe195be0,0xbed0eb2a,0x3e8e9a22,0xbc490e90,0xbe9aa086,0xbf10d3cd,0x3e3228d4,0xbe1324ca,0xbee4cbfe,0xbf34768f,
0x3d8a200a,0xbe8b9507,0xbf14b039,0xbf5233c6,0xbd2351cb,0xbecb2aae,0xbf33587a,0xbf6912ae,0x3f800000,0x3f800000,0x3f800000,0x3f800000,
0x3f7a9c59,0x3f7940c0,0x3f77beb7,0x3f76167a,0x3f6aab7b,0x3f655e0b,0x3f5f8327,0x3f591e6a,0x3f50d937,0x3f456439,0x3f38dd65,0x3f2b561b,
0x3f2e3bde,0x3f1b02c6,0x3f064b82,0x3ee0924f,0x3f044889,0x3ed0eb2a,0x3e9620d2,0x3e3228d4,0x3ea986c4,0x3e419b37,0x3d2fe007,0xbdd53db9,
0x3e06b26e,0xbd2351cb,0xbe57278f,0xbebf926f,0xbd96a905,0xbe888e93,0xbee63375,0xbf1d7fd1,0xbe8d17c0,0xbef58035,0xbf28fd41,0xbf4f0417,
0xbeee9479,0xbf2ac082,0xbf53fac3,0xbf708066,0xbf2302d3,0xbf51c0c2,0xbf714c7a,0xbf7f5ce9,0xbf47de65,0xbf6db293,0xbf7f0e58,0xbf7a7302,
0xbf644fac,0xbf7d1cdd,0xbf7c5d3a,0xbf62241a,0xbf772417,0xbf7f2f9d,0xbf696591,0xbf385216,0xbf7f90b1,0xbf73ced9,0xbf476085,0xbf003a06,
0x3f800000,0x3f800000,0x3f800000,0x3f800000,0x3f74484b,0x3f725470,0x3f703b37,0x3f6dfcf2,0x3f5233c6,0x3f4ac77f,0x3f42de29,0x3f3a7ca4,
0x3f1ce11f,0x3f0d9281,0x3efafef7,0x3ed97cb9,0x3eb263ef,0x3e827dc0,0x3e22abb6,0x3d7b2b74,0x3d55880e,0xbd9064b4,0xbe44b0b9,0xbe9f1cdf,
0xbe7ee6e1,0xbec6d529,0xbf04f484,0xbf239da9,0xbf06f686,0xbf2a2a80,0xbf485bca,0xbf60a6cf,0xbf41d870,0xbf5ebe05,0xbf731447,0xbf7e1324,
0xbf6afb8c,0xbf7b87b2,0xbf7fdaaf,0xbf77beb7,0xbf7e9bc9,0xbf7d7474,0xbf6d1c1d,0xbf4e8d90,0xbf7aed37,0xbf644fac,0xbf3d27b8,0xbf084b92,
0xbf604621,0xbf32c8c9,0xbeebcbbb,0xbe3b6ecf,0xbf311722,0xbedc53c1,0xbe007745,0x3e44b0b9,0xbee363fa,0xbdee3876,0x3e667c66,0x3f0a48ad,
0xbe1f917b,0x3e57278f,0x3f0c42c9,0x3f4fefa8,0,0,0,0,0x3c16cb58,0x3d0a342f,0x3d6ea038,0x3da973ba,
0x3c96c9b6,0x3d8a200a,0x3dee3876,0x3e28defc,0x3ce22a7a,0x3dcefdb7,0x3e3228d4,0x3e7bdba4,0x3d16c32c,0x3e09cf86,0x3e6c9a7f,0x3ea68f12,
0x3d3c6dd5,0x3e2bf804,0x3e931f35,0x3ece0bea,0x3d621469,0x3e4dee60,0x3eaf713a,0x3ef41f07,0x3d83db0a,0x3e6fa8b2,0x3ecb2aae,0x3f0c42c9,
0x3d96a905,0x3e888e93,0x3ee63375,0x3f1d7fd1,0x3da973ba,0x3e9920fe,0x3f003a06,0x3f2da859,0x3dbc3ac3,0x3ea986c4,0x3f0cead0,0x3f3ca003,
0x3dcefdb7,0x3eb9bb1e,0x3f19210f,0x3f4a4c87,0x3de1bc2e,0x3ec9b953,0x3f24d225,0x3f5695e5,0x3df475c0,0x3ed97cb9,0x3f2ff3e6,0x3f61668a,
0x3e039502,0x3ee900b7,0x3f3a7ca4,0x3f6aab7b,0x3e0cec4a,0x3ef840c8,0x3f446337,0x3f725470,0,0,0,0,
0x3ddb7d37,0x3e06b26e,0x3e1f917b,0x3e3857ec,0x3e5a3997,0x3e8586ce,0x3e9d9e78,0x3eb554ec,0x3ea21887,0x3ec5625c,0x3ee79a5d,0x3f044889,
0x3ed53641,0x3f00e7e4,0x3f15f6d9,0x3f299415,0x3f02ef9f,0x3f1ce11f,0x3f34768f,0x3f495521,0x3f19c200,0x3f36206c,0x3f4e8d90,0x3f628210,
0x3f2ecef7,0x3f4c3578,0x3f6398ac,0x3f74484b,0x3f41d870,0x3f5ebe05,0x3f731447,0x3f7e1324,0x3f52a649,0x3f6d67a1,0x3f7c9f8a,0x3f7f90b1,
0x3f6106f2,0x3f77f110,0x3f7ffec4,0x3f78b47b,0x3f6cd007,0x3f7e2b72,0x3f7d1cdd,0x3f69b7e4,0x3f75dec6,0x3f7ffb11,0x3f740bdd,0x3f531849,
0x3f7c187a,0x3f7d57de,0x3f650479,0x3f3592e7,0x3f7f6ac7,0x3f764d97,0x3f5064af,0x3f121eb0,0x3f7fcbe2,0x3f6afb8c,0x3f36ad7f,0x3ed3c867,
0,0,0,0,0x3e5101f1,0x3e698bba,0x3e80f8c0,0x3e8d17c0,0x3ecc9b8b,0x3ee363fa,0x3ef9a02d,0x3f07a136,
0x3f140c5f,0x3f2302d3,0x3f311722,0x3f3e35c5,0x3f3b8f3b,0x3f4bbbf8,0x3f59f26a,0x3f660f88,0x3f5b2c79,0x3f69b7e4,0x3f74bf62,0x3f7c187a,
0x3f718f57,0x3f7b61fc,0x3f7fc38f,0x3f7e9bc9,0x3f7dc68c,0x3f7fcbe2,0x3f7a4910,0x3f6d67a1,0x3f7f4e6d,0x3f76ba07,0x3f64aa59,0x3f49d112,
0x3f76167a,0x3f60a6cf,0x3f404c5c,0x3f16999f,0x3f628210,0x3f3ebc1b,0x3f0f8784,0x3eaf713a,0x3f456439,0x3f12c39a,0x3eab0201,0x3d9064b4,
0x3f1ff6cb,0x3ebe1d4a,0x3dafb680,0xbe541501,0x3ee79a5d,0x3e195be0,0xbe2bf804,0xbeeff7fb,0x3e8586ce,0xbda3308c,0xbed25a09,0xbf31a81d,
0x3d6ea038,0xbe9c1faf,0xbf209393,0xbf5d924d,0,0,0,0,0x3e9920fe,0x3ea5129f,0x3eb0eacb,0x3ebca7af,
0x3f121eb0,0x3f1c420c,0x3f26050a,0x3f2f61a5,0x3f4a4c87,0x3f554aa4,0x3f5f20db,0x3f67c187,0x3f6ff573,0x3f778bc5,0x3f7cbfc9,0x3f7f84ab,
0x3f7fa6e3,0x3f7f5ce9,0x3f7b3bab,0x3f73530e,0x3f77f110,0x3f6be858,0x3f5ac450,0x3f44e3f5,0x3f5988ad,0x3f3f41fc,0x3f1f599f,0x3ef58035,
0x3f273656,0x3efc5d27,0x3ea09ae5,0x3dfab273,0x3ecb2aae,0x3e3e853e,0xbd0a342f,0xbe80f8c0,0x3dd53db9,0xbe1008b7,0xbec1071e,0xbf173c07,
0xbe4ada4f,0xbee79a5d,0xbf2c800f,0xbf58b3a1,0xbef6e0cb,0xbf373a23,0xbf633c5a,0xbf7baccd,0xbf38dd65,0xbf671599,0xbf7dfa38,0xbf7b3bab,
0xbf655e0b,0xbf7e4323,0xbf796e4e,0xbf577026,0xbf7cdf6c,0xbf7a4910,0xbf5627fe,0xbf1553b7,0x3400,0x3400,0x3400,0x3400,
0xa8b233ea,0xb01d32dd,0xb26d30c4,0xb3c32bc6,0xaca533a8,0xb30e2f8b,0xb3a8aca5,0xaf8bb30e,0xaed7333b,0xb3fea248,0xad64b388,0x31edb15f,
0xb07232a7,0xb2a7b072,0x3072b2a7,0x32a73072,0xb15f31ed,0xaed7b33b,0x33fea248,0xad643388,0xb22f3113,0x2646b3f6,0x3113322f,0xb3f6a646,
0xb2dd301d,0x30c4b26d,0xabc633c3,0xa8b2b3ea,0xb3642e1f,0x3364ae1f,0xb3642e1f,0x3364ae1f,0xb3c32bc6,0x33ea28b2,0xb2ddb01d,0x30c4326d,
0xb3f62646,0x322f3113,0xa646b3f6,0xb113322f,0xb3fea248,0x2d643388,0x31edb15f,0xb33baed7,0xb3d9aa3e,0xaa3e33d9,0x33d92a3e,0x2a3eb3d9,
0xb388ad64,0xb15f31ed,0x2ed7333b,0x33fea248,0xb30eaf8b,0xb3a82ca5,0xaf8b330e,0x2ca533a8,0xb26db0c4,0xb3c3abc6,0xb3ea28b2,0xb2dd301d,
0x3400,0x3400,0x3400,0x3400,0xb3eaa8b2,0xb2ddb01d,0xb0c4b26d,0xabc6b3c3,0x2ca5b3a8,0x330eaf8b,0x33a82ca5,0x2f8b330e,
0x333b2ed7,0xa24833fe,0xb3882d64,0xb15fb1ed,0xb07232a7,0xb2a7b072,0x3072b2a7,0x32a73072,0xb1edb15f,0x333baed7,0x224833fe,0xb388ad64,
0x322fb113,0xa64633f6,0xb113b22f,0x33f62646,0x301d32dd,0xb26db0c4,0x33c32bc6,0xb3ea28b2,0xb3642e1f,0x3364ae1f,0xb3642e1f,0x3364ae1f,
0xabc6b3c3,0xa8b233ea,0x301db2dd,0xb26d30c4,0x33f6a646,0xb22fb113,0x264633f6,0x3113b22f,0xa24833fe,0x3388ad64,0xb15fb1ed,0xaed7333b,
0xb3d9aa3e,0xaa3e33d9,0x33d92a3e,0x2a3eb3d9,0x2d64b388,0xb1edb15f,0xb33b2ed7,0x224833fe,0x330e2f8b,0x33a8aca5,0x2f8bb30e,0xaca5b3a8,
0xb0c4326d,0xabc633c3,0x28b233ea,0x301d32dd,0x3400,0x3400,0x3400,0x3400,0x28b2b3ea,0x301db2dd,0x326db0c4,0x33c3abc6,
0xaca533a8,0xb30e2f8b,0xb3a8aca5,0xaf8bb30e,0x2ed7b33b,0x33fe2248,0x2d643388,0xb1ed315f,0xb07232a7,0xb2a7b072,0x3072b2a7,0x32a73072,
0x315fb1ed,0x2ed7333b,0xb3fe2248,0x2d64b388,0xb22f3113,0x2646b3f6,0x3113322f,0xb3f6a646,0x32ddb01d,0xb0c4326d,0x2bc6b3c3,0x28b233ea,
0xb3642e1f,0x3364ae1f,0xb3642e1f,0x3364ae1f,0x33c3abc6,0xb3eaa8b2,0x32dd301d,0xb0c4b26d,0xb3f62646,0x322f3113,0xa646b3f6,0xb113322f,
0x33fe2248,0xad64b388,0xb1ed315f,0x333b2ed7,0xb3d9aa3e,0xaa3e33d9,0x33d92a3e,0x2a3eb3d9,0x33882d64,0x315fb1ed,0xaed7b33b,0xb3fe2248,
0xb30eaf8b,0xb3a82ca5,0xaf8b330e,0x2ca533a8,0x326d30c4,0x33c32bc6,0x33eaa8b2,0x32ddb01d,0x3400,0x3400,0x3400,0x3400,
0x33ea28b2,0x32dd301d,0x30c4326d,0x2bc633c3,0x2ca5b3a8,0x330eaf8b,0x33a82ca5,0x2f8b330e,0xb33baed7,0x2248b3fe,0x3388ad64,0x315f31ed,
0xb07232a7,0xb2a7b072,0x3072b2a7,0x32a73072,0x31ed315f,0xb33b2ed7,0xa248b3fe,0x33882d64,0x322fb113,0xa64633f6,0xb113b22f,0x33f62646,
0xb01db2dd,0x326d30c4,0xb3c3abc6,0x33eaa8b2,0xb3642e1f,0x3364ae1f,0xb3642e1f,0x3364ae1f,0x2bc633c3,0x28b2b3ea,0xb01d32dd,0x326db0c4,
0x33f6a646,0xb22fb113,0x264633f6,0x3113b22f,0x2248b3fe,0xb3882d64,0x315f31ed,0x2ed7b33b,0xb3d9aa3e,0xaa3e33d9,0x33d92a3e,0x2a3eb3d9,
0xad643388,0x31ed315f,0x333baed7,0xa248b3fe,0x330e2f8b,0x33a8aca5,0x2f8bb30e,0xaca5b3a8,0x30c4b26d,0x2bc6b3c3,0xa8b2b3ea,0xb01db2dd,
0x34000000,0x34000000,0x34000000,0x34000000,0x33ea28b2,0x32dd301d,0x30c4326d,0x2bc633c3,0x33a82ca5,0x2f8b330e,0xaca533a8,0xb30e2f8b,
0x333b2ed7,0xa24833fe,0xb3882d64,0xb15fb1ed,0x32a73072,0xb07232a7,0xb2a7b072,0x3072b2a7,0x31ed315f,0xb33b2ed7,0xa248b3fe,0x33882d64,
0x3113322f,0xb3f6a646,0x322fb113,0xa64633f6,0x301d32dd,0xb26db0c4,0x33c32bc6,0xb3ea28b2,0x2e1f3364,0xae1fb364,0x2e1f3364,0xae1fb364,
0x2bc633c3,0x28b2b3ea,0xb01d32dd,0x326db0c4,0x264633f6,0x3113b22f,0xb3f62646,0x322f3113,0xa24833fe,0x3388ad64,0xb15fb1ed,0xaed7333b,
0xaa3e33d9,0x33d92a3e,0x2a3eb3d9,0xb3d9aa3e,0xad643388,0x31ed315f,0x333baed7,0xa248b3fe,0xaf8b330e,0x2ca533a8,0x330e2f8b,0x33a8aca5,
0xb0c4326d,0xabc633c3,0x28b233ea,0x301d32dd,0x34000000,0x34000000,0x34000000,0x34000000,0xa8b233ea,0xb01d32dd,0xb26d30c4,0xb3c32bc6,
0xb3a8aca5,0xaf8bb30e,0x2ca5b3a8,0x330eaf8b,0x2ed7b33b,0x33fe2248,0x2d643388,0xb1ed315f,0x32a73072,0xb07232a7,0xb2a7b072,0x3072b2a7,
0xb15f31ed,0xaed7b33b,0x33fea248,0xad643388,0xb113b22f,0x33f62646,0xb22f3113,0x2646b3f6,0x32ddb01d,0xb0c4326d,0x2bc6b3c3,0x28b233ea,
0x2e1f3364,0xae1fb364,0x2e1f3364,0xae1fb364,0xb3c32bc6,0x33ea28b2,0xb2ddb01d,0x30c4326d,0xa646b3f6,0xb113322f,0x33f6a646,0xb22fb113,
0x33fe2248,0xad64b388,0xb1ed315f,0x333b2ed7,0xaa3e33d9,0x33d92a3e,0x2a3eb3d9,0xb3d9aa3e,0xb388ad64,0xb15f31ed,0x2ed7333b,0x33fea248,
0x2f8bb30e,0xaca5b3a8,0xb30eaf8b,0xb3a82ca5,0x326d30c4,0x33c32bc6,0x33eaa8b2,0x32ddb01d,0x34000000,0x34000000,0x34000000,0x34000000,
0xb3eaa8b2,0xb2ddb01d,0xb0c4b26d,0xabc6b3c3,0x33a82ca5,0x2f8b330e,0xaca533a8,0xb30e2f8b,0xb33baed7,0x2248b3fe,0x3388ad64,0x315f31ed,
0x32a73072,0xb07232a7,0xb2a7b072,0x3072b2a7,0xb1edb15f,0x333baed7,0x224833fe,0xb388ad64,0x3113322f,0xb3f6a646,0x322fb113,0xa64633f6,
0xb01db2dd,0x326d30c4,0xb3c3abc6,0x33eaa8b2,0x2e1f3364,0xae1fb364,0x2e1f3364,0xae1fb364,0xabc6b3c3,0xa8b233ea,0x301db2dd,0xb26d30c4,
0x264633f6,0x3113b22f,0xb3f62646,0x322f3113,0x2248b3fe,0xb3882d64,0x315f31ed,0x2ed7b33b,0xaa3e33d9,0x33d92a3e,0x2a3eb3d9,0xb3d9aa3e,
0x2d64b388,0xb1edb15f,0xb33b2ed7,0x224833fe,0xaf8b330e,0x2ca533a8,0x330e2f8b,0x33a8aca5,0x30c4b26d,0x2bc6b3c3,0xa8b2b3ea,0xb01db2dd,
0x34000000,0x34000000,0x34000000,0x34000000,0x28b2b3ea,0x301db2dd,0x326db0c4,0x33c3abc6,0xb3a8aca5,0xaf8bb30e,0x2ca5b3a8,0x330eaf8b,
0xaed7333b,0xb3fea248,0xad64b388,0x31edb15f,0x32a73072,0xb07232a7,0xb2a7b072,0x3072b2a7,0x315fb1ed,0x2ed7333b,0xb3fe2248,0x2d64b388,
0xb113b22f,0x33f62646,0xb22f3113,0x2646b3f6,0xb2dd301d,0x30c4b26d,0xabc633c3,0xa8b2b3ea,0x2e1f3364,0xae1fb364,0x2e1f3364,0xae1fb364,
0x33c3abc6,0xb3eaa8b2,0x32dd301d,0xb0c4b26d,0xa646b3f6,0xb113322f,0x33f6a646,0xb22fb113,0xb3fea248,0x2d643388,0x31edb15f,0xb33baed7,
0xaa3e33d9,0x33d92a3e,0x2a3eb3d9,0xb3d9aa3e,0x33882d64,0x315fb1ed,0xaed7b33b,0xb3fe2248,0x2f8bb30e,0xaca5b3a8,0xb30eaf8b,0xb3a82ca5,
0xb26db0c4,0xb3c3abc6,0xb3ea28b2,0xb2dd301d,0x3400,0x2a3e33d9,0x2e1f3364,0x307232a7,0x3400,0x307232a7,0x33642e1f,0x33d9aa3e,
0x3400,0x32a73072,0x3364ae1f,0x2a3eb3d9,0x3400,0x33d92a3e,0x2e1fb364,0xb2a7b072,0x3400,0x33d9aa3e,0xae1fb364,0xb2a73072,
0x3400,0x32a7b072,0xb364ae1f,0x2a3e33d9,0x3400,0x3072b2a7,0xb3642e1f,0x33d92a3e,0x3400,0x2a3eb3d9,0xae1f3364,0x3072b2a7,
0x3400,0xaa3eb3d9,0x2e1f3364,0xb072b2a7,0x3400,0xb072b2a7,0x33642e1f,0xb3d92a3e,0x3400,0xb2a7b072,0x3364ae1f,0xaa3e33d9,
0x3400,0xb3d9aa3e,0x2e1fb364,0x32a73072,0x3400,0xb3d92a3e,0xae1fb364,0x32a7b072,0x3400,0xb2a73072,0xb364ae1f,0xaa3eb3d9,
0x3400,0xb07232a7,0xb3642e1f,0xb3d9aa3e,0x3400,0xaa3e33d9,0xae1f3364,0xb07232a7,0x31a831a8,0x32a73072,0x33642e1f,0x33d92a3e,
0x31a8b1a8,0x2a3eb3d9,0xae1fb364,0xb2a7b072,0xb1a8b1a8,0xb3d92a3e,0xae1f3364,0x307232a7,0xb1a831a8,0x307232a7,0x3364ae1f,0xaa3eb3d9,
0x31a831a8,0x3072b2a7,0xb364ae1f,0xaa3e33d9,0x31a8b1a8,0xb3d9aa3e,0x2e1f3364,0x3072b2a7,0xb1a8b1a8,0x2a3e33d9,0x2e1fb364,0xb2a73072,
0xb1a831a8,0x32a7b072,0xb3642e1f,0x33d9aa3e,0x31a831a8,0xb2a7b072,0x33642e1f,0xb3d9aa3e,0x31a8b1a8,0xaa3e33d9,0xae1fb364,0x32a73072,
0xb1a8b1a8,0x33d9aa3e,0xae1f3364,0xb072b2a7,0xb1a831a8,0xb072b2a7,0x3364ae1f,0x2a3e33d9,0x31a831a8,0xb07232a7,0xb364ae1f,0x2a3eb3d9,
0x31a8b1a8,0x33d92a3e,0x2e1f3364,0xb07232a7,0xb1a8b1a8,0xaa3eb3d9,0x2e1fb364,0x32a7b072,0xb1a831a8,0xb2a73072,0xb3642e1f,0xb3d92a3e,
0x34000000,0x33d9aa3e,0x3364ae1f,0x32a7b072,0xb4000000,0xb2a73072,0xae1f3364,0x2a3e33d9,0x34000000,0x3072b2a7,0xae1fb364,0xb3d9aa3e,
0xb4000000,0xaa3e33d9,0x33642e1f,0x3072b2a7,0x34000000,0xaa3eb3d9,0xb3642e1f,0x307232a7,0xb4000000,0x307232a7,0x2e1fb364,0xb3d92a3e,
0x34000000,0xb2a7b072,0x2e1f3364,0x2a3eb3d9,0xb4000000,0x33d92a3e,0xb364ae1f,0x32a73072,0x34000000,0xb3d92a3e,0x3364ae1f,0xb2a73072,
0xb4000000,0x32a7b072,0xae1f3364,0xaa3eb3d9,0x34000000,0xb07232a7,0xae1fb364,0x33d92a3e,0xb4000000,0x2a3eb3d9,0x33642e1f,0xb07232a7,
0x34000000,0x2a3e33d9,0xb3642e1f,0xb072b2a7,0xb4000000,0xb072b2a7,0x2e1fb364,0x33d9aa3e,0x34000000,0x32a73072,0x2e1f3364,0xaa3e33d9,
0xb4000000,0xb3d9aa3e,0xb364ae1f,0xb2a7b072,0x31a8b1a8,0x3072b2a7,0x2e1fb364,0x2a3eb3d9,0x31a831a8,0x33d92a3e,0x3364ae1f,0x3072b2a7,
0xb1a831a8,0x2a3e33d9,0x33642e1f,0x32a7b072,0xb1a8b1a8,0xb2a73072,0x2e1f3364,0x33d9aa3e,0x31a8b1a8,0xb2a7b072,0xae1f3364,0x33d92a3e,
0x31a831a8,0x2a3eb3d9,0xb3642e1f,0x32a73072,0xb1a831a8,0x33d9aa3e,0xb364ae1f,0x307232a7,0xb1a8b1a8,0x307232a7,0xae1fb364,0x2a3e33d9,
0x31a8b1a8,0xb07232a7,0x2e1fb364,0xaa3e33d9,0x31a831a8,0xb3d9aa3e,0x3364ae1f,0xb07232a7,0xb1a831a8,0xaa3eb3d9,0x33642e1f,0xb2a73072,
0xb1a8b1a8,0x32a7b072,0x2e1f3364,0xb3d92a3e,0x31a8b1a8,0x32a73072,0xae1f3364,0xb3d9aa3e,0x31a831a8,0xaa3e33d9,0xb3642e1f,0xb2a7b072,
0xb1a831a8,0xb3d92a3e,0xb364ae1f,0xb072b2a7,0xb1a8b1a8,0xb072b2a7,0xae1fb364,0xaa3eb3d9,0x34000000,0x33d9aa3e,0x3364ae1f,0x32a7b072,
0x34000000,0x32a7b072,0x2e1fb364,0xaa3eb3d9,0x34000000,0x3072b2a7,0xae1fb364,0xb3d9aa3e,0x34000000,0x2a3eb3d9,0xb364ae1f,0xb07232a7,
0x34000000,0xaa3eb3d9,0xb3642e1f,0x307232a7,0x34000000,0xb072b2a7,0xae1f3364,0x33d9aa3e,0x34000000,0xb2a7b072,0x2e1f3364,0x2a3eb3d9,
0x34000000,0xb3d9aa3e,0x33642e1f,0xb2a7b072,0x34000000,0xb3d92a3e,0x3364ae1f,0xb2a73072,0x34000000,0xb2a73072,0x2e1fb364,0x2a3e33d9,
0x34000000,0xb07232a7,0xae1fb364,0x33d92a3e,0x34000000,0xaa3e33d9,0xb364ae1f,0x3072b2a7,0x34000000,0x2a3e33d9,0xb3642e1f,0xb072b2a7,
0x34000000,0x307232a7,0xae1f3364,0xb3d92a3e,0x34000000,0x32a73072,0x2e1f3364,0xaa3e33d9,0x34000000,0x33d92a3e,0x33642e1f,0x32a73072,
0x31a8b1a8,0x3072b2a7,0x2e1fb364,0x2a3eb3d9,0xb1a8b1a8,0xb3d9aa3e,0xb3642e1f,0xb07232a7,0xb1a831a8,0x2a3e33d9,0x33642e1f,0x32a7b072,
0x31a831a8,0x32a7b072,0xae1fb364,0xb3d92a3e,0x31a8b1a8,0xb2a7b072,0xae1f3364,0x33d92a3e,0xb1a8b1a8,0xaa3e33d9,0x3364ae1f,0xb2a7b072,
0xb1a831a8,0x33d9aa3e,0xb364ae1f,0x307232a7,0x31a831a8,0xb072b2a7,0x2e1f3364,0xaa3eb3d9,0x31a8b1a8,0xb07232a7,0x2e1fb364,0xaa3e33d9,
0xb1a8b1a8,0x33d92a3e,0xb3642e1f,0x3072b2a7,0xb1a831a8,0xaa3eb3d9,0x33642e1f,0xb2a73072,0x31a831a8,0xb2a73072,0xae1fb364,0x33d9aa3e,
0x31a8b1a8,0x32a73072,0xae1f3364,0xb3d9aa3e,0xb1a8b1a8,0x2a3eb3d9,0x3364ae1f,0x32a73072,0xb1a831a8,0xb3d92a3e,0xb364ae1f,0xb072b2a7,
0x31a831a8,0x307232a7,0x2e1f3364,0x2a3e33d9,0xb400,0xaa3eb3d9,0xae1fb364,0xb072b2a7,0x3400,0x307232a7,0x33642e1f,0x33d9aa3e,
0xb400,0xb2a7b072,0xb3642e1f,0xaa3e33d9,0x3400,0x33d92a3e,0x2e1fb364,0xb2a7b072,0xb400,0xb3d92a3e,0x2e1f3364,0x32a7b072,
0x3400,0x32a7b072,0xb364ae1f,0x2a3e33d9,0xb400,0xb07232a7,0x3364ae1f,0xb3d9aa3e,0x3400,0x2a3eb3d9,0xae1f3364,0x3072b2a7,
0xb400,0x2a3e33d9,0xae1fb364,0x307232a7,0x3400,0xb072b2a7,0x33642e1f,0xb3d92a3e,0xb400,0x32a73072,0xb3642e1f,0x2a3eb3d9,
0x3400,0xb3d9aa3e,0x2e1fb364,0x32a73072,0xb400,0x33d9aa3e,0x2e1f3364,0xb2a73072,0x3400,0xb2a73072,0xb364ae1f,0xaa3eb3d9,
0xb400,0x3072b2a7,0x3364ae1f,0x33d92a3e,0x3400,0xaa3e33d9,0xae1f3364,0xb07232a7,0xb1a8b1a8,0xb2a7b072,0xb364ae1f,0xb3d9aa3e,
0x31a8b1a8,0x2a3eb3d9,0xae1fb364,0xb2a7b072,0x31a831a8,0x33d9aa3e,0x2e1fb364,0xb072b2a7,0xb1a831a8,0x307232a7,0x3364ae1f,0xaa3eb3d9,
0xb1a8b1a8,0xb07232a7,0x33642e1f,0x2a3eb3d9,0x31a8b1a8,0xb3d9aa3e,0x2e1f3364,0x3072b2a7,0x31a831a8,0xaa3eb3d9,0xae1f3364,0x32a7b072,
0xb1a831a8,0x32a7b072,0xb3642e1f,0x33d9aa3e,0xb1a8b1a8,0x32a73072,0xb364ae1f,0x33d92a3e,0x31a8b1a8,0xaa3e33d9,0xae1fb364,0x32a73072,
0x31a831a8,0xb3d92a3e,0x2e1fb364,0x307232a7,0xb1a831a8,0xb072b2a7,0x3364ae1f,0x2a3e33d9,0xb1a8b1a8,0x3072b2a7,0x33642e1f,0xaa3e33d9,
0x31a8b1a8,0x33d92a3e,0x2e1f3364,0xb07232a7,0x31a831a8,0x2a3e33d9,0xae1f3364,0xb2a73072,0xb1a831a8,0xb2a73072,0xb3642e1f,0xb3d92a3e,
0x3f800000,0x3f7ffb11,0x3f7fec43,0x3f7fd397,0x3f800000,0x3f7fd397,0x3f7f4e6d,0x3f7e70b0,0x3f800000,0x3f7f84ab,0x3f7e1324,0x3f7baccd,
0x3f800000,0x3f7f0e58,0x3f7c3b28,0x3f778bc5,0x3f800000,0x3f7e70b0,0x3f79c79d,0x3f721352,0x3f800000,0x3f7dabcc,0x3f76ba07,0x3f6b4b0c,
0x3f800000,0x3f7cbfc9,0x3f731447,0x3f633c5a,0x3f800000,0x3f7baccd,0x3f6ed89e,0x3f59f26a,0x3f800000,0x3f7a7302,0x3f6a09a7,0x3f4f7a1f,
0x3f800000,0x3f791298,0x3f64aa59,0x3f43e200,0x3f800000,0x3f778bc5,0x3f5ebe05,0x3f373a23,0x3f800000,0x3f75dec6,0x3f584853,0x3f299415,
0x3f800000,0x3f740bdd,0x3f514d3d,0x3f1b02c6,0x3f800000,0x3f721352,0x3f49d112,0x3f0b9a6b,0x3f800000,0x3f6ff573,0x3f41d870,0x3ef6e0cb,
0x3f800000,0x3f6db293,0x3f396842,0x3ed53641,0x3f7fb10f,0x3f7f84ab,0x3f7f4e6d,0x3f7f0e58,0x3f7d3aac,0x3f7baccd,0x3f79c79d,0x3f778bc5,
0x3f7853f8,0x3f740bdd,0x3f6ed89e,0x3f68bf3c,0x3f710908,0x3f68bf3c,0x3f5ebe05,0x3f531849,0x3f676bd8,0x3f59f26a,0x3f49d112,0x3f373a23,
0x3f5b941a,0x3f47de65,0x3f3085bb,0x3f15f6d9,0x3f4d9f02,0x3f32c8c9,0x3f13682a,0x3ee0924f,0x3f3daef9,0x3f1b02c6,0x3ee63375,0x3e8e9a22,
0x3f2beb4a,0x3f00e7e4,0x3ea09ae5,0x3de1bc2e,0x3f187fc0,0x3ec9b953,0x3e2f10a2,0xbd7b2b74,0x3f039c3d,0x3e8e9a22,0x3cc90ab0,0xbe6c9a7f,
0x3edae880,0x3e22abb6,0xbdfab273,0xbec9b953,0x3eac7cd4,0x3d16c32c,0xbe888e93,0xbf0b9a6b,0x3e78cfcc,0xbdafb680,0xbecf7bca,0xbf2e3bde,
0x3e164083,0xbe541501,0xbf08f59b,0xbf4bbbf8,0x3d48fb30,0xbea68f12,0xbf273656,0xbf633c5a,0x3f7ec46d,0x3f7e70b0,0x3f7e1324,0x3f7dabcc,
0x3f74fa0b,0x3f721352,0x3f6ed89e,0x3f6b4b0c,0x3f61c598,0x3f59f26a,0x3f514d3d,0x3f47de65,0x3f45e403,0x3f373a23,0x3f273656,0x3f15f6d9,
0x3f226799,0x3f0b9a6b,0x3ee63375,0x3eb263ef,0x3ef15aea,0x3eb263ef,0x3e605c13,0x3dafb680,0x3e94a031,0x3e09cf86,0xbcc90ab0,0xbe3b6ecf,
0x3dc8bd36,0xbdafb680,0xbe888e93,0xbee0924f,0xbdc8bd36,0xbe9aa086,0xbefc5d27,0xbf299415,0xbe94a031,0xbf00e7e4,0xbf3085bb,0xbf5695e5,
0xbef15aea,0xbf2e3bde,0xbf584853,0xbf740bdd,0xbf226799,0xbf531849,0xbf731447,0xbf7fd397,0xbf45e403,0xbf6db293,0xbf7f4e6d,0xbf791298,
0xbf61c598,0xbf7cbfc9,0xbf7c3b28,0xbf604621,0xbf74fa0b,0xbf7f84ab,0xbf6a09a7,0xbf373a23,0xbf7ec46d,0xbf75dec6,0xbf49d112,0xbf00e7e4,
0x3f7d3aac,0x3f7cbfc9,0x3f7c3b28,0x3f7baccd,0x3f676bd8,0x3f633c5a,0x3f5ebe05,0x3f59f26a,0x3f3daef9,0x3f32c8c9,0x3f273656,0x3f1b02c6,
0x3f039c3d,0x3ee0924f,0x3eb8442a,0x3e8e9a22,0x3e78cfcc,0x3e09cf86,0x3cc90ab0,0xbdafb680,0xbd48fb30,0xbe3b6ecf,0xbea09ae5,0xbee0924f,
0xbeac7cd4,0xbef6e0cb,0xbf1d7fd1,0xbf3b8f3b,0xbf187fc0,0xbf3b8f3b,0xbf584853,0xbf6db293,0xbf4d9f02,0xbf68bf3c,0xbf79c79d,0xbf7ffb11,
0xbf710908,0xbf7e70b0,0xbf7e1324,0xbf6ff573,0xbf7fb10f,0xbf7a7302,0xbf64aa59,0xbf3fc767,0xbf7853f8,0xbf5d2d53,0xbf3085bb,0xbeebcbbb,
0xbf5b941a,0xbf299415,0xbecf7bca,0xbde1bc2e,0xbf2beb4a,0xbec9b953,0xbd96a905,0x3e827dc0,0xbedae880,0xbdafb680,0x3e888e93,0x3f15f6d9,
0xbe164083,0x3e6c9a7f,0x3f13682a,0x3f5695e5,0,0xbc490e90,0xbcc90ab0,0xbd16c32c,0,0xbd16c32c,0xbd96a905,0xbde1bc2e,
0,0xbd7b2b74,0xbdfab273,0xbe3b6ecf,0,0xbdafb680,0xbe2f10a2,0xbe827dc0,0,0xbde1bc2e,0xbe605c13,0xbea68f12,
0,0xbe09cf86,0xbe888e93,0xbec9b953,0,0xbe22abb6,0xbea09ae5,0xbeebcbbb,0,0xbe3b6ecf,0xbeb8442a,0xbf064b82,
0,0xbe541501,0xbecf7bca,0xbf15f6d9,0,0xbe6c9a7f,0xbee63375,0xbf24d225,0,0xbe827dc0,0xbefc5d27,0xbf32c8c9,
0,0xbe8e9a22,0xbf08f59b,0xbf3fc767,0,0xbe9aa086,0xbf13682a,0xbf4bbbf8,0,0xbea68f12,0xbf1d7fd1,0xbf5695e5,
0,0xbeb263ef,0xbf273656,0xbf604621,0,0xbebe1d4a,0xbf3085bb,0xbf68bf3c,0xbd48fb30,0xbd7b2b74,0xbd96a905,0xbdafb680,
0xbe164083,0xbe3b6ecf,0xbe605c13,0xbe827dc0,0xbe78cfcc,0xbe9aa086,0xbeb8442a,0xbed53641,0xbeac7cd4,0xbed53641,0xbefc5d27,0xbf10d3cd,
0xbedae880,0xbf064b82,0xbf1d7fd1,0xbf32c8c9,0xbf039c3d,0xbf1ff6cb,0xbf396842,0xbf4f7a1f,0xbf187fc0,0xbf373a23,0xbf514d3d,0xbf660f88,
0xbf2beb4a,0xbf4bbbf8,0xbf64aa59,0xbf75dec6,0xbf3daef9,0xbf5d2d53,0xbf731447,0xbf7e70b0,0xbf4d9f02,0xbf6b4b0c,0xbf7c3b28,0xbf7f84ab,
0xbf5b941a,0xbf75dec6,0xbf7fec43,0xbf791298,0xbf676bd8,0xbf7cbfc9,0xbf7e1324,0xbf6b4b0c,0xbf710908,0xbf7fd397,0xbf76ba07,0xbf5695e5,
0xbf7853f8,0xbf7f0e58,0xbf6a09a7,0xbf3b8f3b,0xbf7d3aac,0xbf7a7302,0xbf584853,0xbf1b02c6,0xbf7fb10f,0xbf721352,0xbf41d870,0xbeebcbbb,
0xbdc8bd36,0xbde1bc2e,0xbdfab273,0xbe09cf86,0xbe94a031,0xbea68f12,0xbeb8442a,0xbec9b953,0xbef15aea,0xbf064b82,0xbf13682a,0xbf1ff6cb,
0xbf226799,0xbf32c8c9,0xbf41d870,0xbf4f7a1f,0xbf45e403,0xbf5695e5,0xbf64aa59,0xbf6ff573,0xbf61c598,0xbf6ff573,0xbf79c79d,0xbf7f0e58,
0xbf74fa0b,0xbf7dabcc,0xbf7fec43,0xbf7baccd,0xbf7ec46d,0xbf7f0e58,0xbf76ba07,0xbf660f88,0xbf7ec46d,0xbf740bdd,0xbf5ebe05,0xbf3fc767,
0xbf74fa0b,0xbf5d2d53,0xbf396842,0xbf0b9a6b,0xbf61c598,0xbf3b8f3b,0xbf08f59b,0xbe9aa086,0xbf45e403,0xbf10d3cd,0xbea09ae5,0xbd16c32c,
0xbf226799,0xbebe1d4a,0xbd96a905,0x3e6c9a7f,0xbef15aea,0xbe22abb6,0x3e2f10a2,0x3ef6e0cb,0xbe94a031,0x3d7b2b74,0x3ecf7bca,0x3f32c8c9,
0xbdc8bd36,0x3e8e9a22,0x3f1d7fd1,0x3f5d2d53,0xbe164083,0xbe22abb6,0xbe2f10a2,0xbe3b6ecf,0xbedae880,0xbeebcbbb,0xbefc5d27,0xbf064b82,
0xbf2beb4a,0xbf373a23,0xbf41d870,0xbf4bbbf8,0xbf5b941a,0xbf660f88,0xbf6ed89e,0xbf75dec6,0xbf7853f8,0xbf7dabcc,0xbf7fec43,0xbf7f0e58,
0xbf7fb10f,0xbf7baccd,0xbf731447,0xbf660f88,0xbf710908,0xbf604621,0xbf49d112,0xbf2e3bde,0xbf4d9f02,0xbf2e3bde,0xbf08f59b,0xbebe1d4a,
0xbf187fc0,0xbed53641,0xbe605c13,0xbc490e90,0xbeac7cd4,0xbde1bc2e,0x3dfab273,0x3eb263ef,0xbd48fb30,0x3e541501,0x3ee63375,0x3f299415,
0x3e78cfcc,0x3f00e7e4,0x3f396842,0x3f633c5a,0x3f039c3d,0x3f3fc767,0x3f6a09a7,0x3f7e70b0,0x3f3daef9,0x3f6b4b0c,0x3f7f4e6d,0x3f778bc5,
0x3f676bd8,0x3f7f0e58,0x3f76ba07,0x3f4f7a1f,0x3f7d3aac,0x3f791298,0x3f514d3d,0x3f0b9a6b,0x3f800000,0x3f800000,0x3f800000,0x3f800000,
0x3f7ffb11,0x3f7fd397,0x3f7f84ab,0x3f7f0e58,0x3f7fec43,0x3f7f4e6d,0x3f7e1324,0x3f7c3b28,0x3f7fd397,0x3f7e70b0,0x3f7baccd,0x3f778bc5,
0x3f7fb10f,0x3f7d3aac,0x3f7853f8,0x3f710908,0x3f7f84ab,0x3f7baccd,0x3f740bdd,0x3f68bf3c,0x3f7f4e6d,0x3f79c79d,0x3f6ed89e,0x3f5ebe05,
0x3f7f0e58,0x3f778bc5,0x3f68bf3c,0x3f531849,0x3f7ec46d,0x3f74fa0b,0x3f61c598,0x3f45e403,0x3f7e70b0,0x3f721352,0x3f59f26a,0x3f373a23,
0x3f7e1324,0x3f6ed89e,0x3f514d3d,0x3f273656,0x3f7dabcc,0x3f6b4b0c,0x3f47de65,0x3f15f6d9,0x3f7d3aac,0x3f676bd8,0x3f3daef9,0x3f039c3d,
0x3f7cbfc9,0x3f633c5a,0x3f32c8c9,0x3ee0924f,0x3f7c3b28,0x3f5ebe05,0x3f273656,0x3eb8442a,0x3f7baccd,0x3f59f26a,0x3f1b02c6,0x3e8e9a22,
0x3f800000,0x3f800000,0x3f800000,0x3f800000,0x3f7e70b0,0x3f7dabcc,0x3f7cbfc9,0x3f7baccd,0x3f79c79d,0x3f76ba07,0x3f731447,0x3f6ed89e,
0x3f721352,0x3f6b4b0c,0x3f633c5a,0x3f59f26a,0x3f676bd8,0x3f5b941a,0x3f4d9f02,0x3f3daef9,0x3f59f26a,0x3f47de65,0x3f32c8c9,0x3f1b02c6,
0x3f49d112,0x3f3085bb,0x3f13682a,0x3ee63375,0x3f373a23,0x3f15f6d9,0x3ee0924f,0x3e8e9a22,0x3f226799,0x3ef15aea,0x3e94a031,0x3dc8bd36,
0x3f0b9a6b,0x3eb263ef,0x3e09cf86,0xbdafb680,0x3ee63375,0x3e605c13,0xbcc90ab0,0xbe888e93,0x3eb263ef,0x3dafb680,0xbe3b6ecf,0xbee0924f,
0x3e78cfcc,0xbd48fb30,0xbeac7cd4,0xbf187fc0,0x3e09cf86,0xbe3b6ecf,0xbef6e0cb,0xbf3b8f3b,0x3cc90ab0,0xbea09ae5,0xbf1d7fd1,0xbf584853,
0xbdafb680,0xbee0924f,0xbf3b8f3b,0xbf6db293,0x3f800000,0x3f800000,0x3f800000,0x3f800000,0x3f7a7302,0x3f791298,0x3f778bc5,0x3f75dec6,
0x3f6a09a7,0x3f64aa59,0x3f5ebe05,0x3f584853,0x3f4f7a1f,0x3f43e200,0x3f373a23,0x3f299415,0x3f2beb4a,0x3f187fc0,0x3f039c3d,0x3edae880,
0x3f00e7e4,0x3ec9b953,0x3e8e9a22,0x3e22abb6,0x3ea09ae5,0x3e2f10a2,0x3cc90ab0,0xbdfab273,0x3de1bc2e,0xbd7b2b74,0xbe6c9a7f,0xbec9b953,
0xbdc8bd36,0xbe94a031,0xbef15aea,0xbf226799,0xbe9aa086,0xbf00e7e4,0xbf2e3bde,0xbf531849,0xbefc5d27,0xbf3085bb,0xbf584853,0xbf731447,
0xbf299415,0xbf5695e5,0xbf740bdd,0xbf7fd397,0xbf4d9f02,0xbf710908,0xbf7fb10f,0xbf7853f8,0xbf68bf3c,0xbf7e70b0,0xbf7a7302,0xbf5d2d53,
0xbf79c79d,0xbf7e1324,0xbf64aa59,0xbf3085bb,0xbf7ffb11,0xbf6ff573,0xbf3fc767,0xbeebcbbb,0x3f800000,0x3f800000,0x3f800000,0x3f800000,
0x3f740bdd,0x3f721352,0x3f6ff573,0x3f6db293,0x3f514d3d,0x3f49d112,0x3f41d870,0x3f396842,0x3f1b02c6,0x3f0b9a6b,0x3ef6e0cb,0x3ed53641,
0x3eac7cd4,0x3e78cfcc,0x3e164083,0x3d48fb30,0x3d16c32c,0xbdafb680,0xbe541501,0xbea68f12,0xbe888e93,0xbecf7bca,0xbf08f59b,0xbf273656,
0xbf0b9a6b,0xbf2e3bde,0xbf4bbbf8,0xbf633c5a,0xbf45e403,0xbf61c598,0xbf74fa0b,0xbf7ec46d,0xbf6db293,0xbf7cbfc9,0xbf7f84ab,0xbf75dec6,
0xbf7f4e6d,0xbf7c3b28,0xbf6a09a7,0xbf49d112,0xbf791298,0xbf604621,0xbf373a23,0xbf00e7e4,0xbf5b941a,0xbf2beb4a,0xbedae880,0xbe164083,
0xbf299415,0xbec9b953,0xbdafb680,0x3e6c9a7f,0xbecf7bca,0xbd96a905,0x3e888e93,0x3f13682a,0xbde1bc2e,0x3e827dc0,0x3f15f6d9,0x3f5695e5,
0,0,0,0,0x3c490e90,0x3d16c32c,0x3d7b2b74,0x3dafb680,0x3cc90ab0,0x3d96a905,0x3dfab273,0x3e2f10a2,
0x3d16c32c,0x3de1bc2e,0x3e3b6ecf,0x3e827dc0,0x3d48fb30,0x3e164083,0x3e78cfcc,0x3eac7cd4,0x3d7b2b74,0x3e3b6ecf,0x3e9aa086,0x3ed53641,
0x3d96a905,0x3e605c13,0x3eb8442a,0x3efc5d27,0x3dafb680,0x3e827dc0,0x3ed53641,0x3f10d3cd,0x3dc8bd36,0x3e94a031,0x3ef15aea,0x3f226799,
0x3de1bc2e,0x3ea68f12,0x3f064b82,0x3f32c8c9,0x3dfab273,0x3eb8442a,0x3f13682a,0x3f41d870,0x3e09cf86,0x3ec9b953,0x3f1ff6cb,0x3f4f7a1f,
0x3e164083,0x3edae880,0x3f2beb4a,0x3f5b941a,0x3e22abb6,0x3eebcbbb,0x3f373a23,0x3f660f88,0x3e2f10a2,0x3efc5d27,0x3f41d870,0x3f6ed89e,
0x3e3b6ecf,0x3f064b82,0x3f4bbbf8,0x3f75dec6,0,0,0,0,0x3de1bc2e,0x3e09cf86,0x3e22abb6,0x3e3b6ecf,
0x3e605c13,0x3e888e93,0x3ea09ae5,0x3eb8442a,0x3ea68f12,0x3ec9b953,0x3eebcbbb,0x3f064b82,0x3edae880,0x3f039c3d,0x3f187fc0,0x3f2beb4a,
0x3f064b82,0x3f1ff6cb,0x3f373a23,0x3f4bbbf8,0x3f1d7fd1,0x3f396842,0x3f514d3d,0x3f64aa59,0x3f32c8c9,0x3f4f7a1f,0x3f660f88,0x3f75dec6,
0x3f45e403,0x3f61c598,0x3f74fa0b,0x3f7ec46d,0x3f5695e5,0x3f6ff573,0x3f7dabcc,0x3f7f0e58,0x3f64aa59,0x3f79c79d,0x3f7fec43,0x3f76ba07,
0x3f6ff573,0x3f7f0e58,0x3f7baccd,0x3f660f88,0x3f7853f8,0x3f7fb10f,0x3f710908,0x3f4d9f02,0x3f7dabcc,0x3f7baccd,0x3f604621,0x3f2e3bde,
0x3f7fec43,0x3f731447,0x3f49d112,0x3f08f59b,0x3f7f0e58,0x3f660f88,0x3f2e3bde,0x3ebe1d4a,0,0,0,0,
0x3e541501,0x3e6c9a7f,0x3e827dc0,0x3e8e9a22,0x3ecf7bca,0x3ee63375,0x3efc5d27,0x3f08f59b,0x3f15f6d9,0x3f24d225,0x3f32c8c9,0x3f3fc767,
0x3f3daef9,0x3f4d9f02,0x3f5b941a,0x3f676bd8,0x3f5d2d53,0x3f6b4b0c,0x3f75dec6,0x3f7cbfc9,0x3f731447,0x3f7c3b28,0x3f7fec43,0x3f7e1324,
0x3f7e70b0,0x3f7f84ab,0x3f791298,0x3f6b4b0c,0x3f7ec46d,0x3f74fa0b,0x3f61c598,0x3f45e403,0x3f740bdd,0x3f5d2d53,0x3f3b8f3b,0x3f10d3cd,
0x3f5ebe05,0x3f396842,0x3f08f59b,0x3ea09ae5,0x3f3fc767,0x3f0b9a6b,0x3e9aa086,0x3d16c32c,0x3f187fc0,0x3eac7cd4,0x3d48fb30,0xbe78cfcc,
0x3ed53641,0x3de1bc2e,0xbe541501,0xbf00e7e4,0x3e605c13,0xbdfab273,0xbee63375,0xbf396842,0x3c490e90,0xbeb263ef,0xbf299415,0xbf633c5a,
0,0,0,0,0x3e9aa086,0x3ea68f12,0x3eb263ef,0x3ebe1d4a,0x3f13682a,0x3f1d7fd1,0x3f273656,0x3f3085bb,
0x3f4bbbf8,0x3f5695e5,0x3f604621,0x3f68bf3c,0x3f710908,0x3f7853f8,0x3f7d3aac,0x3f7fb10f,0x3f7fd397,0x3f7f0e58,0x3f7a7302,0x3f721352,
0x3f76ba07,0x3f6a09a7,0x3f584853,0x3f41d870,0x3f5695e5,0x3f3b8f3b,0x3f1b02c6,0x3eebcbbb,0x3f226799,0x3ef15aea,0x3e94a031,0x3dc8bd36,
0x3ebe1d4a,0x3e22abb6,0xbd7b2b74,0xbe8e9a22,0x3d96a905,0xbe2f10a2,0xbecf7bca,0xbf1d7fd1,0xbe6c9a7f,0xbef6e0cb,0xbf32c8c9,0xbf5d2d53,
0xbf039c3d,0xbf3daef9,0xbf676bd8,0xbf7d3aac,0xbf3fc767,0xbf6b4b0c,0xbf7f0e58,0xbf791298,0xbf6a09a7,0xbf7f4e6d,0xbf76ba07,0xbf514d3d,
0xbf7e70b0,0xbf778bc5,0xbf4f7a1f,0xbf0b9a6b,0x3400,0x3400,0x3400,0x3400,0xaa3e33d9,0xb07232a7,0xb2a73072,0xb3d92a3e,
0xae1f3364,0xb3642e1f,0xb364ae1f,0xae1fb364,0xb07232a7,0xb3d9aa3e,0xaa3eb3d9,0x32a7b072,0xb1a831a8,0xb1a8b1a8,0x31a8b1a8,0x31a831a8,
0xb2a73072,0xaa3eb3d9,0x33d92a3e,0xb07232a7,0xb3642e1f,0x2e1fb364,0x2e1f3364,0xb364ae1f,0xb3d92a3e,0x32a7b072,0xb07232a7,0x2a3eb3d9,
0xb4000000,0x34000000,0xb4000000,0x34000000,0xb3d9aa3e,0x32a73072,0xb072b2a7,0x2a3e33d9,0xb364ae1f,0x2e1f3364,0x2e1fb364,0xb3642e1f,
0xb2a7b072,0xaa3e33d9,0x33d9aa3e,0xb072b2a7,0xb1a8b1a8,0xb1a831a8,0x31a831a8,0x31a8b1a8,0xb072b2a7,0xb3d92a3e,0xaa3e33d9,0x32a73072,
0xae1fb364,0xb364ae1f,0xb3642e1f,0xae1f3364,0xaa3eb3d9,0xb072b2a7,0xb2a7b072,0xb3d9aa3e,0x3400,0x3400,0x3400,0x3400,
0xb3d9aa3e,0xb2a7b072,0xb072b2a7,0xaa3eb3d9,0x2e1fb364,0x3364ae1f,0x33642e1f,0x2e1f3364,0x32a73072,0xaa3e33d9,0xb3d92a3e,0xb072b2a7,
0xb1a831a8,0xb1a8b1a8,0x31a8b1a8,0x31a831a8,0xb072b2a7,0x33d9aa3e,0xaa3e33d9,0xb2a7b072,0x3364ae1f,0xae1f3364,0xae1fb364,0x33642e1f,
0x2a3e33d9,0xb072b2a7,0x32a73072,0xb3d9aa3e,0xb4000000,0x34000000,0xb4000000,0x34000000,0x2a3eb3d9,0xb07232a7,0x32a7b072,0xb3d92a3e,
0x33642e1f,0xae1fb364,0xae1f3364,0x3364ae1f,0xb07232a7,0x33d92a3e,0xaa3eb3d9,0xb2a73072,0xb1a8b1a8,0xb1a831a8,0x31a831a8,0x31a8b1a8,
0x32a7b072,0xaa3eb3d9,0xb3d9aa3e,0xb07232a7,0x2e1f3364,0x33642e1f,0x3364ae1f,0x2e1fb364,0xb3d92a3e,0xb2a73072,0xb07232a7,0xaa3e33d9,
0x3400,0x3400,0x3400,0x3400,0x2a3eb3d9,0x3072b2a7,0x32a7b072,0x33d9aa3e,0xae1f3364,0xb3642e1f,0xb364ae1f,0xae1fb364,
0x3072b2a7,0x33d92a3e,0x2a3e33d9,0xb2a73072,0xb1a831a8,0xb1a8b1a8,0x31a8b1a8,0x31a831a8,0x32a7b072,0x2a3e33d9,0xb3d9aa3e,0x3072b2a7,
0xb3642e1f,0x2e1fb364,0x2e1f3364,0xb364ae1f,0x33d9aa3e,0xb2a73072,0x3072b2a7,0xaa3e33d9,0xb4000000,0x34000000,0xb4000000,0x34000000,
0x33d92a3e,0xb2a7b072,0x307232a7,0xaa3eb3d9,0xb364ae1f,0x2e1f3364,0x2e1fb364,0xb3642e1f,0x32a73072,0x2a3eb3d9,0xb3d92a3e,0x307232a7,
0xb1a8b1a8,0xb1a831a8,0x31a831a8,0x31a8b1a8,0x307232a7,0x33d9aa3e,0x2a3eb3d9,0xb2a7b072,0xae1fb364,0xb364ae1f,0xb3642e1f,0xae1f3364,
0x2a3e33d9,0x307232a7,0x32a73072,0x33d92a3e,0x3400,0x3400,0x3400,0x3400,0x33d92a3e,0x32a73072,0x307232a7,0x2a3e33d9,
0x2e1fb364,0x3364ae1f,0x33642e1f,0x2e1f3364,0xb2a7b072,0x2a3eb3d9,0x33d9aa3e,0x307232a7,0xb1a831a8,0xb1a8b1a8,0x31a8b1a8,0x31a831a8,
0x307232a7,0xb3d92a3e,0x2a3eb3d9,0x32a73072,0x3364ae1f,0xae1f3364,0xae1fb364,0x33642e1f,0xaa3eb3d9,0x307232a7,0xb2a7b072,0x33d92a3e,
0xb4000000,0x34000000,0xb4000000,0x34000000,0xaa3e33d9,0x3072b2a7,0xb2a73072,0x33d9aa3e,0x33642e1f,0xae1fb364,0xae1f3364,0x3364ae1f,
0x3072b2a7,0xb3d9aa3e,0x2a3e33d9,0x32a7b072,0xb1a8b1a8,0xb1a831a8,0x31a831a8,0x31a8b1a8,0xb2a73072,0x2a3e33d9,0x33d92a3e,0x3072b2a7,
0x2e1f3364,0x33642e1f,0x3364ae1f,0x2e1fb364,0x33d9aa3e,0x32a7b072,0x3072b2a7,0x2a3eb3d9,0x34000000,0x34000000,0x34000000,0x34000000,
0x33d92a3e,0x32a73072,0x307232a7,0x2a3e33d9,0x33642e1f,0x2e1f3364,0xae1f3364,0xb3642e1f,0x32a73072,0xaa3e33d9,0xb3d92a3e,0xb072b2a7,
0x31a831a8,0xb1a831a8,0xb1a8b1a8,0x31a8b1a8,0x307232a7,0xb3d92a3e,0x2a3eb3d9,0x32a73072,0x2e1f3364,0xb364ae1f,0x3364ae1f,0xae1f3364,
0x2a3e33d9,0xb072b2a7,0x32a73072,0xb3d9aa3e,0x3400,0xb400,0x3400,0xb400,0xaa3e33d9,0x3072b2a7,0xb2a73072,0x33d9aa3e,
0xae1f3364,0x3364ae1f,0xb364ae1f,0x2e1f3364,0xb07232a7,0x33d92a3e,0xaa3eb3d9,0xb2a73072,0xb1a831a8,0x31a831a8,0x31a8b1a8,0xb1a8b1a8,
0xb2a73072,0x2a3e33d9,0x33d92a3e,0x3072b2a7,0xb3642e1f,0xae1f3364,0x2e1f3364,0x33642e1f,0xb3d92a3e,0xb2a73072,0xb07232a7,0xaa3e33d9,
0x34000000,0x34000000,0x34000000,0x34000000,0xaa3e33d9,0xb07232a7,0xb2a73072,0xb3d92a3e,0xb364ae1f,0xae1fb364,0x2e1fb364,0x3364ae1f,
0x3072b2a7,0x33d92a3e,0x2a3e33d9,0xb2a73072,0x31a831a8,0xb1a831a8,0xb1a8b1a8,0x31a8b1a8,0xb2a73072,0xaa3eb3d9,0x33d92a3e,0xb07232a7,
0xae1fb364,0x33642e1f,0xb3642e1f,0x2e1fb364,0x33d9aa3e,0xb2a73072,0x3072b2a7,0xaa3e33d9,0x3400,0xb400,0x3400,0xb400,
0xb3d9aa3e,0x32a73072,0xb072b2a7,0x2a3e33d9,0x2e1fb364,0xb3642e1f,0x33642e1f,0xae1fb364,0x32a73072,0x2a3eb3d9,0xb3d92a3e,0x307232a7,
0xb1a831a8,0x31a831a8,0x31a8b1a8,0xb1a8b1a8,0xb072b2a7,0xb3d92a3e,0xaa3e33d9,0x32a73072,0x3364ae1f,0x2e1fb364,0xae1fb364,0xb364ae1f,
0x2a3e33d9,0x307232a7,0x32a73072,0x33d92a3e,0x34000000,0x34000000,0x34000000,0x34000000,0xb3d9aa3e,0xb2a7b072,0xb072b2a7,0xaa3eb3d9,
0x33642e1f,0x2e1f3364,0xae1f3364,0xb3642e1f,0xb2a7b072,0x2a3eb3d9,0x33d9aa3e,0x307232a7,0x31a831a8,0xb1a831a8,0xb1a8b1a8,0x31a8b1a8,
0xb072b2a7,0x33d9aa3e,0xaa3e33d9,0xb2a7b072,0x2e1f3364,0xb364ae1f,0x3364ae1f,0xae1f3364,0xaa3eb3d9,0x307232a7,0xb2a7b072,0x33d92a3e,
0x3400,0xb400,0x3400,0xb400,0x2a3eb3d9,0xb07232a7,0x32a7b072,0xb3d92a3e,0xae1f3364,0x3364ae1f,0xb364ae1f,0x2e1f3364,
0x3072b2a7,0xb3d9aa3e,0x2a3e33d9,0x32a7b072,0xb1a831a8,0x31a831a8,0x31a8b1a8,0xb1a8b1a8,0x32a7b072,0xaa3eb3d9,0xb3d9aa3e,0xb07232a7,
0xb3642e1f,0xae1f3364,0x2e1f3364,0x33642e1f,0x33d9aa3e,0x32a7b072,0x3072b2a7,0x2a3eb3d9,0x34000000,0x34000000,0x34000000,0x34000000,
0x2a3eb3d9,0x3072b2a7,0x32a7b072,0x33d9aa3e,0xb364ae1f,0xae1fb364,0x2e1fb364,0x3364ae1f,0xb07232a7,0xb3d9aa3e,0xaa3eb3d9,0x32a7b072,
0x31a831a8,0xb1a831a8,0xb1a8b1a8,0x31a8b1a8,0x32a7b072,0x2a3e33d9,0xb3d9aa3e,0x3072b2a7,0xae1fb364,0x33642e1f,0xb3642e1f,0x2e1fb364,
0xb3d92a3e,0x32a7b072,0xb07232a7,0x2a3eb3d9,0x3400,0xb400,0x3400,0xb400,0x33d92a3e,0xb2a7b072,0x307232a7,0xaa3eb3d9,
0x2e1fb364,0xb3642e1f,0x33642e1f,0xae1fb364,0xb2a7b072,0xaa3e33d9,0x33d9aa3e,0xb072b2a7,0xb1a831a8,0x31a831a8,0x31a8b1a8,0xb1a8b1a8,
0x307232a7,0x33d9aa3e,0x2a3eb3d9,0xb2a7b072,0x3364ae1f,0x2e1fb364,0xae1fb364,0xb364ae1f,0xaa3eb3d9,0xb072b2a7,0xb2a7b072,0xb3d9aa3e,
0x3400,0x2bc633c3,0x2f8b330e,0x315f31ed,0x3400,0x30c4326d,0x33a82ca5,0x3388ad64,0x3400,0x32dd301d,0x330eaf8b,0x2248b3fe,
0x3400,0x33ea28b2,0x2ca5b3a8,0xb33baed7,0x3400,0x33c3abc6,0xaf8bb30e,0xb1ed315f,0x3400,0x326db0c4,0xb3a8aca5,0x2d643388,
0x3400,0x301db2dd,0xb30e2f8b,0x33fe2248,0x3400,0x28b2b3ea,0xaca533a8,0x2ed7b33b,0x3400,0xabc6b3c3,0x2f8b330e,0xb15fb1ed,
0x3400,0xb0c4b26d,0x33a82ca5,0xb3882d64,0x3400,0xb2ddb01d,0x330eaf8b,0xa24833fe,0x3400,0xb3eaa8b2,0x2ca5b3a8,0x333b2ed7,
0x3400,0xb3c32bc6,0xaf8bb30e,0x31edb15f,0x3400,0xb26d30c4,0xb3a8aca5,0xad64b388,0x3400,0xb01d32dd,0xb30e2f8b,0xb3fea248,
0x3400,0xa8b233ea,0xaca533a8,0xaed7333b,0x32a73072,0x33882d64,0x33f62646,0x33eaa8b2,0x3072b2a7,0xa248b3fe,0xb113b22f,0xb3c3abc6,
0xb2a7b072,0xb33b2ed7,0xa64633f6,0x326d30c4,0xb07232a7,0x31ed315f,0x322fb113,0xb01db2dd,0x32a73072,0x2d64b388,0xb3f6a646,0x28b233ea,
0x3072b2a7,0xb3fe2248,0x3113322f,0x2bc6b3c3,0xb2a7b072,0x2ed7333b,0x2646b3f6,0xb0c4326d,0xb07232a7,0x315fb1ed,0xb22f3113,0x32ddb01d,
0x32a73072,0xb388ad64,0x33f62646,0xb3ea28b2,0x3072b2a7,0x224833fe,0xb113b22f,0x33c32bc6,0xb2a7b072,0x333baed7,0xa64633f6,0xb26db0c4,
0xb07232a7,0xb1edb15f,0x322fb113,0x301d32dd,0x32a73072,0xad643388,0xb3f6a646,0xa8b2b3ea,0x3072b2a7,0x33fea248,0x3113322f,0xabc633c3,
0xb2a7b072,0xaed7b33b,0x2646b3f6,0x30c4b26d,0xb07232a7,0xb15f31ed,0xb22f3113,0xb2dd301d,0x3364ae1f,0x326db0c4,0x3113b22f,0x2ed7b33b,
0xb3642e1f,0xb01d32dd,0x264633f6,0x315f31ed,0x3364ae1f,0x28b2b3ea,0xb22fb113,0xb3882d64,0xb3642e1f,0x2bc633c3,0x33f6a646,0x2248b3fe,
0x3364ae1f,0xb0c4b26d,0xb113322f,0x333b2ed7,0xb3642e1f,0x32dd301d,0xa646b3f6,0xb1ed315f,0x3364ae1f,0xb3eaa8b2,0x322f3113,0xad64b388,
0xb3642e1f,0x33c3abc6,0xb3f62646,0x33fe2248,0x3364ae1f,0xb26d30c4,0x3113b22f,0xaed7333b,0xb3642e1f,0x301db2dd,0x264633f6,0xb15fb1ed,
0x3364ae1f,0xa8b233ea,0xb22fb113,0x3388ad64,0xb3642e1f,0xabc6b3c3,0x33f6a646,0xa24833fe,0x3364ae1f,0x30c4326d,0xb113322f,0xb33baed7,
0xb3642e1f,0xb2ddb01d,0xa646b3f6,0x31edb15f,0x3364ae1f,0x33ea28b2,0x322f3113,0x2d643388,0xb3642e1f,0xb3c32bc6,0xb3f62646,0xb3fea248,
0x2a3eb3d9,0xa248b3fe,0xaca5b3a8,0xb01db2dd,0x33d92a3e,0x333baed7,0x2f8bb30e,0xa8b2b3ea,0xaa3e33d9,0x31ed315f,0x33a8aca5,0x2bc6b3c3,
0xb3d9aa3e,0xad643388,0x330e2f8b,0x30c4b26d,0x2a3eb3d9,0xb3fe2248,0x2ca533a8,0x32ddb01d,0x33d92a3e,0xaed7b33b,0xaf8b330e,0x33eaa8b2,
0xaa3e33d9,0x315fb1ed,0xb3a82ca5,0x33c32bc6,0xb3d9aa3e,0x33882d64,0xb30eaf8b,0x326d30c4,0x2a3eb3d9,0x224833fe,0xaca5b3a8,0x301d32dd,
0x33d92a3e,0xb33b2ed7,0x2f8bb30e,0x28b233ea,0xaa3e33d9,0xb1edb15f,0x33a8aca5,0xabc633c3,0xb3d9aa3e,0x2d64b388,0x330e2f8b,0xb0c4326d,
0x2a3eb3d9,0x33fea248,0x2ca533a8,0xb2dd301d,0x33d92a3e,0x2ed7333b,0xaf8b330e,0xb3ea28b2,0xaa3e33d9,0xb15f31ed,0xb3a82ca5,0xb3c3abc6,
0xb3d9aa3e,0xb388ad64,0xb30eaf8b,0xb26db0c4,0x34000000,0x33c3abc6,0x330eaf8b,0x31edb15f,0x34000000,0x326db0c4,0x2ca5b3a8,0xad64b388,
0x34000000,0x301db2dd,0xaf8bb30e,0xb3fea248,0x34000000,0x28b2b3ea,0xb3a8aca5,0xaed7333b,0x34000000,0xabc6b3c3,0xb30e2f8b,0x315f31ed,
0x34000000,0xb0c4b26d,0xaca533a8,0x3388ad64,0x34000000,0xb2ddb01d,0x2f8b330e,0x2248b3fe,0x34000000,0xb3eaa8b2,0x33a82ca5,0xb33baed7,
0x34000000,0xb3c32bc6,0x330eaf8b,0xb1ed315f,0x34000000,0xb26d30c4,0x2ca5b3a8,0x2d643388,0x34000000,0xb01d32dd,0xaf8bb30e,0x33fe2248,
0x34000000,0xa8b233ea,0xb3a8aca5,0x2ed7b33b,0x34000000,0x2bc633c3,0xb30e2f8b,0xb15fb1ed,0x34000000,0x30c4326d,0xaca533a8,0xb3882d64,
0x34000000,0x32dd301d,0x2f8b330e,0xa24833fe,0x34000000,0x33ea28b2,0x33a82ca5,0x333b2ed7,0x3072b2a7,0x2d64b388,0x2646b3f6,0xa8b2b3ea,
0xb2a7b072,0xb3fe2248,0xb22f3113,0xabc633c3,0xb07232a7,0x2ed7333b,0x33f62646,0x30c4b26d,0x32a73072,0x315fb1ed,0xb113b22f,0xb2dd301d,
0x3072b2a7,0xb388ad64,0xa64633f6,0x33eaa8b2,0xb2a7b072,0x224833fe,0x322fb113,0xb3c3abc6,0xb07232a7,0x333baed7,0xb3f6a646,0x326d30c4,
0x32a73072,0xb1edb15f,0x3113322f,0xb01db2dd,0x3072b2a7,0xad643388,0x2646b3f6,0x28b233ea,0xb2a7b072,0x33fea248,0xb22f3113,0x2bc6b3c3,
0xb07232a7,0xaed7b33b,0x33f62646,0xb0c4326d,0x32a73072,0xb15f31ed,0xb113b22f,0x32ddb01d,0x3072b2a7,0x33882d64,0xa64633f6,0xb3ea28b2,
0xb2a7b072,0xa248b3fe,0x322fb113,0x33c32bc6,0xb07232a7,0xb33b2ed7,0xb3f6a646,0xb26db0c4,0x32a73072,0x31ed315f,0x3113322f,0x301d32dd,
0xae1fb364,0xb0c4b26d,0xb22fb113,0xb33baed7,0x2e1f3364,0x32dd301d,0x33f6a646,0x31edb15f,0xae1fb364,0xb3eaa8b2,0xb113322f,0x2d643388,
0x2e1f3364,0x33c3abc6,0xa646b3f6,0xb3fea248,0xae1fb364,0xb26d30c4,0x322f3113,0x2ed7b33b,0x2e1f3364,0x301db2dd,0xb3f62646,0x315f31ed,
0xae1fb364,0xa8b233ea,0x3113b22f,0xb3882d64,0x2e1f3364,0xabc6b3c3,0x264633f6,0x2248b3fe,0xae1fb364,0x30c4326d,0xb22fb113,0x333b2ed7,
0x2e1f3364,0xb2ddb01d,0x33f6a646,0xb1ed315f,0xae1fb364,0x33ea28b2,0xb113322f,0xad64b388,0x2e1f3364,0xb3c32bc6,0xa646b3f6,0x33fe2248,
0xae1fb364,0x326db0c4,0x322f3113,0xaed7333b,0x2e1f3364,0xb01d32dd,0xb3f62646,0xb15fb1ed,0xae1fb364,0x28b2b3ea,0x3113b22f,0x3388ad64,
0x2e1f3364,0x2bc633c3,0x264633f6,0xa24833fe,0xb3d9aa3e,0xb3fe2248,0xb3a82ca5,0xb2dd301d,0x2a3eb3d9,0xaed7b33b,0xb30eaf8b,0xb3ea28b2,
0x33d92a3e,0x315fb1ed,0xaca5b3a8,0xb3c3abc6,0xaa3e33d9,0x33882d64,0x2f8bb30e,0xb26db0c4,0xb3d9aa3e,0x224833fe,0x33a8aca5,0xb01db2dd,
0x2a3eb3d9,0xb33b2ed7,0x330e2f8b,0xa8b2b3ea,0x33d92a3e,0xb1edb15f,0x2ca533a8,0x2bc6b3c3,0xaa3e33d9,0x2d64b388,0xaf8b330e,0x30c4b26d,
0xb3d9aa3e,0x33fea248,0xb3a82ca5,0x32ddb01d,0x2a3eb3d9,0x2ed7333b,0xb30eaf8b,0x33eaa8b2,0x33d92a3e,0xb15f31ed,0xaca5b3a8,0x33c32bc6,
0xaa3e33d9,0xb388ad64,0x2f8bb30e,0x326d30c4,0xb3d9aa3e,0xa248b3fe,0x33a8aca5,0x301d32dd,0x2a3eb3d9,0x333baed7,0x330e2f8b,0x28b233ea,
0x33d92a3e,0x31ed315f,0x2ca533a8,0xabc633c3,0xaa3e33d9,0xad643388,0xaf8b330e,0xb0c4326d,0x3f800000,0x3f7ff84a,0x3f7fe129,0x3f7fba9e,
0x3f800000,0x3f7fcbe2,0x3f7f2f9d,0x3f7e2b72,0x3f800000,0x3f7f7808,0x3f7de0b1,0x3f7b3bab,0x3f800000,0x3f7efcc9,0x3f7bf531,0x3f76ef5b,
0x3f800000,0x3f7e5a38,0x3f796e4e,0x3f714c7a,0x3f800000,0x3f7d906e,0x3f764d97,0x3f6a5ad9,0x3f800000,0x3f7c9f8a,0x3f7294f8,0x3f62241a,
0x3f800000,0x3f7b87b2,0x3f6e46be,0x3f58b3a1,0x3f800000,0x3f7a4910,0x3f696591,0x3f4e1689,0x3f800000,0x3f78e3d6,0x3f63f473,0x3f425b89,
0x3f800000,0x3f77583a,0x3f5df6be,0x3f3592e7,0x3f800000,0x3f75a67b,0x3f577026,0x3f27ce61,0x3f800000,0x3f73ced9,0x3f5064af,0x3f19210f,
0x3f800000,0x3f71d19f,0x3f48d8b3,0x3f099f4e,0x3f800000,0x3f6faf1b,0x3f40d0da,0x3ef2bd43,0x3f800000,0x3f6d67a1,0x3f385216,0x3ed0eb2a,
0x3f7f84ab,0x3f7f3f54,0x3f7eea9d,0x3f7e868b,0x3f7cbfc9,0x3f7aed37,0x3f78b47b,0x3f76167a,0x3f778bc5,0x3f72d4eb,0x3f6d1c1d,0x3f666771,
0x3f6ff573,0x3f671599,0x3f5c61c7,0x3f4fefa8,0x3f660f88,0x3f57dc7f,0x3f46e22a,0x3f33587a,0x3f59f26a,0x3f456439,0x3f2d1469,0x3f11796b,
0x3f4bbbf8,0x3f2ff3e6,0x3f0f8784,0x3ed6a399,0x3f3b8f3b,0x3f17de12,0x3eddbe79,0x3e840270,0x3f299415,0x3efafef7,0x3e97a117,0x3db5f8da,
0x3f15f6d9,0x3ec27b55,0x3e1c76de,0xbda973ba,0x3f00e7e4,0x3e870ada,0x3bc90f88,0xbe80f8c0,0x3ed53641,0x3e1324ca,0xbe1008b7,0xbed3c867,
0x3ea68f12,0x3cafea69,0xbe919ddd,0xbf102dd5,0x3e6c9a7f,0xbdcefdb7,0xbed8106b,0xbf3238aa,0x3e09cf86,0xbe636c83,0xbf0cead0,0xbf4f0417,
0x3d16c32c,0xbeadf73c,0xbf2ac082,0xbf65b710,0x3f7e1324,0x3f7d906e,0x3f7cfe73,0x3f7c5d3a,0x3f731447,0x3f6faf1b,0x3f6be858,0x3f67c187,
0x3f5ebe05,0x3f5627fe,0x3f4cae79,0x3f425b89,0x3f41d870,0x3f3238aa,0x3f212ff9,0x3f0ee0db,0x3f1d7fd1,0x3f05a02c,0x3ed8106b,0x3ea21887,
0x3ee63375,0x3ea5129f,0x3e419b37,0x3d55880e,0x3e888e93,0x3ddb7d37,0xbd621469,0xbe5d4b1a,0x3d96a905,0xbde7fa9a,0xbe97a117,0xbeeff7fb,
0xbdfab273,0xbea80b1f,0xbf04f484,0xbf2ff3e6,0xbea09ae5,0xbf06f686,0xbf36206c,0xbf5b2c79,0xbefc5d27,0xbf33587a,0xbf5c61c7,0xbf76841b,
0xbf273656,0xbf570348,0xbf756d97,0xbf7fffb1,0xbf49d112,0xbf703b37,0xbf7fc38f,0xbf76ef5b,0xbf64aa59,0xbf7dc68c,0xbf7ac516,0xbf5bfb34,
0xbf76ba07,0xbf7efcc9,0xbf66becc,0xbf311722,0xbf7f4e6d,0xbf73ced9,0xbf44e3f5,0xbef2bd43,0x3f7baccd,0x3f7aed37,0x3f7a1e84,0x3f7940c0,
0x3f633c5a,0x3f5e5aa6,0x3f591e6a,0x3f5389c7,0x3f373a23,0x3f2b561b,0x3f1ebc12,0x3f11796b,0x3ef6e0cb,0x3ece0bea,0x3ea395c5,0x3e6fa8b2,
0x3e541501,0x3dc27c39,0xbc96c9b6,0xbe06b26e,0xbdafb680,0xbe636c83,0xbeb554ec,0xbef58035,0xbebe1d4a,0xbf044889,0xbf26050a,0xbf436051,
0xbf1ff6cb,0xbf425b89,0xbf5df6be,0xbf71d19f,0xbf531849,0xbf6cd007,0xbf7bf531,0xbf7fdaaf,0xbf740bdd,0xbf7f5ce9,0xbf7c7eb0,0xbf6b99fb,
0xbf7ffb11,0xbf7822d1,0xbf5f8327,0xbf37c655,0xbf75dec6,0xbf57dc7f,0xbf286605,0xbed6a399,0xbf5695e5,0xbf21cbfb,0xbebb31a0,0xbd83db0a,
0xbf24d225,0xbeb6ccc3,0xbcfb49ba,0x3e9920fe,0xbec9b953,0xbd3c6dd5,0x3e9d9e78,0x3f1f599f,0xbde1bc2e,0x3e8a11f7,0x3f1c420c,0x3f5cc7d1,
0,0xbc7b514b,0xbcfb49ba,0xbd3c6dd5,0,0xbd2351cb,0xbda3308c,0xbdf475c0,0,0xbd83db0a,0xbe039502,0xbe44b0b9,
0,0xbdb5f8da,0xbe354098,0xbe870ada,0,0xbde7fa9a,0xbe667c66,0xbeab0201,0,0xbe0cec4a,0xbe8b9507,0xbece0bea,
0,0xbe25c58c,0xbea395c5,0xbeeff7fb,0,0xbe3e853e,0xbebb31a0,0xbf084b92,0,0xbe57278f,0xbed25a09,0xbf17de12,
0,0xbe6fa8b2,0xbee900b7,0xbf269de3,0,0xbe840270,0xbeff17b2,0xbf34768f,0,0xbe901c2c,0xbf0a48ad,0xbf4154e1,
0,0xbe9c1faf,0xbf14b039,0xbf4d26fd,0,0xbea80b1f,0xbf1ebc12,0xbf57dc7f,0,0xbeb3dca5,0xbf286605,0xbf61668a,
0,0xbebf926f,0xbf31a81d,0xbf69b7e4,0xbd7b2b74,0xbd9cecf9,0xbdbc3ac3,0xbddb7d37,0xbe22abb6,0xbe4ada4f,0xbe72b651,0xbe8d17c0,
0xbe827dc0,0xbea21887,0xbec1071e,0xbedf28a9,0xbeb263ef,0xbedc53c1,0xbf0242b1,0xbf1553b7,0xbee0924f,0xbf099f4e,0xbf212ff9,0xbf36ad7f,
0xbf064b82,0xbf2302d3,0xbf3ca003,0xbf52a649,0xbf1b02c6,0xbf39f2ac,0xbf53fac3,0xbf686b39,0xbf2e3bde,0xbf4e1689,0xbf66becc,0xbf77583a,
0xbf3fc767,0xbf5f20db,0xbf748422,0xbf7efcc9,0xbf4f7a1f,0xbf6cd007,0xbf7cfe73,0xbf7f1f49,0xbf5d2d53,0xbf76ef5b,0xbf7ffec4,0xbf77beb7,
0xbf68bf3c,0xbf7d57de,0xbf7d7474,0xbf6912ae,0xbf721352,0xbf7ff0e3,0xbf756d97,0xbf5389c7,0xbf791298,0xbf7eb069,0xbf6816a8,0xbf37c655,
0xbf7dabcc,0xbf799b43,0xbf55b993,0xbf16999f,0xbf7fd397,0xbf70c501,0xbf3ebc1b,0xbee1fb6a,0xbdfab273,0xbe0cec4a,0xbe1c76de,0xbe2bf804,
0xbea09ae5,0xbeb3dca5,0xbec6d529,0xbed97cb9,0xbefc5d27,0xbf0c42c9,0xbf19c200,0xbf269de3,0xbf273656,0xbf37c655,0xbf46e22a,0xbf546b3b,
0xbf49d112,0xbf5a5ba0,0xbf6816a8,0xbf72d4eb,0xbf64aa59,0xbf725470,0xbf7b61fc,0xbf7fa6e3,0xbf76ba07,0xbf7e868b,0xbf7f9c18,0xbf79f35e,
0xbf7f4e6d,0xbf7e5a38,0xbf748422,0xbf62241a,0xbf7e1324,0xbf71d19f,0xbf5ac450,0xbf39f2ac,0xbf731447,0xbf5988ad,0xbf33e7bc,0xbf044889,
0xbf5ebe05,0xbf36ad7f,0xbf0242b1,0xbe8a11f7,0xbf41d870,0xbf0af1b7,0xbe919ddd,0xbb490fc6,0xbf1d7fd1,0xbeb0eacb,0xbd2fe007,0x3e870ada,
0xbee63375,0xbe06b26e,0x3e4dee60,0x3f02ef9f,0xbe888e93,0x3db5f8da,0x3eddbe79,0x3f38dd65,0xbd96a905,0x3e9c1faf,0x3f239da9,0x3f61668a,
0xbe3b6ecf,0xbe4ada4f,0xbe5a3997,0xbe698bba,0xbeebcbbb,0xbefdbabb,0xbf07a136,0xbf102dd5,0xbf32c8c9,0xbf3e35c5,0xbf48d8b3,0xbf52a649,
0xbf604621,0xbf6a5ad9,0xbf7294f8,0xbf78e3d6,0xbf7a7302,0xbf7ed7d4,0xbf7ff4e6,0xbf7dc68c,0xbf7f0e58,0xbf799b43,0xbf6f6830,0xbf60a6cf,
0xbf6db293,0xbf5b2c79,0xbf42de29,0xbf256bcb,0xbf47de65,0xbf269de3,0xbeff17b2,0xbea80b1f,0xbf10d3cd,0xbec27b55,0xbe354098,0x3d0a342f,
0xbe9aa086,0xbd9064b4,0x3e28defc,0x3ec8477c,0xbc490e90,0x3e7bdba4,0x3ef9a02d,0x3f3238aa,0x3e8e9a22,0x3f099f4e,0x3f40d0da,0x3f686b39,
0x3f0b9a6b,0x3f466354,0x3f6e46be,0x3f7f7808,0x3f43e200,0x3f6f20b0,0x3f7fe129,0x3f74484b,0x3f6b4b0c,0x3f7fba9e,0x3f73913f,0x3f485bca,
0x3f7e70b0,0x3f76841b,0x3f4ac77f,0x3f019573,0x3f800000,0x3f800000,0x3f800000,0x3f800000,0x3f7ff84a,0x3f7fcbe2,0x3f7f7808,0x3f7efcc9,
0x3f7fe129,0x3f7f2f9d,0x3f7de0b1,0x3f7bf531,0x3f7fba9e,0x3f7e2b72,0x3f7b3bab,0x3f76ef5b,0x3f7f84ab,0x3f7cbfc9,0x3f778bc5,0x3f6ff573,
0x3f7f3f54,0x3f7aed37,0x3f72d4eb,0x3f671599,0x3f7eea9d,0x3f78b47b,0x3f6d1c1d,0x3f5c61c7,0x3f7e868b,0x3f76167a,0x3f666771,0x3f4fefa8,
0x3f7e1324,0x3f731447,0x3f5ebe05,0x3f41d870,0x3f7d906e,0x3f6faf1b,0x3f5627fe,0x3f3238aa,0x3f7cfe73,0x3f6be858,0x3f4cae79,0x3f212ff9,
0x3f7c5d3a,0x3f67c187,0x3f425b89,0x3f0ee0db,0x3f7baccd,0x3f633c5a,0x3f373a23,0x3ef6e0cb,0x3f7aed37,0x3f5e5aa6,0x3f2b561b,0x3ece0bea,
0x3f7a1e84,0x3f591e6a,0x3f1ebc12,0x3ea395c5,0x3f7940c0,0x3f5389c7,0x3f11796b,0x3e6fa8b2,0x3f800000,0x3f800000,0x3f800000,0x3f800000,
0x3f7e5a38,0x3f7d906e,0x3f7c9f8a,0x3f7b87b2,0x3f796e4e,0x3f764d97,0x3f7294f8,0x3f6e46be,0x3f714c7a,0x3f6a5ad9,0x3f62241a,0x3f58b3a1,
0x3f660f88,0x3f59f26a,0x3f4bbbf8,0x3f3b8f3b,0x3f57dc7f,0x3f456439,0x3f2ff3e6,0x3f17de12,0x3f46e22a,0x3f2d1469,0x3f0f8784,0x3eddbe79,
0x3f33587a,0x3f11796b,0x3ed6a399,0x3e840270,0x3f1d7fd1,0x3ee63375,0x3e888e93,0x3d96a905,0x3f05a02c,0x3ea5129f,0x3ddb7d37,0xbde7fa9a,
0x3ed8106b,0x3e419b37,0xbd621469,0xbe97a117,0x3ea21887,0x3d55880e,0xbe5d4b1a,0xbeeff7fb,0x3e541501,0xbdafb680,0xbebe1d4a,0xbf1ff6cb,
0x3dc27c39,0xbe636c83,0xbf044889,0xbf425b89,0xbc96c9b6,0xbeb554ec,0xbf26050a,0xbf5df6be,0xbe06b26e,0xbef58035,0xbf436051,0xbf71d19f,
0x3f800000,0x3f800000,0x3f800000,0x3f800000,0x3f7a4910,0x3f78e3d6,0x3f77583a,0x3f75a67b,0x3f696591,0x3f63f473,0x3f5df6be,0x3f577026,
0x3f4e1689,0x3f425b89,0x3f3592e7,0x3f27ce61,0x3f299415,0x3f15f6d9,0x3f00e7e4,0x3ed53641,0x3efafef7,0x3ec27b55,0x3e870ada,0x3e1324ca,
0x3e97a117,0x3e1c76de,0x3bc90f88,0xbe1008b7,0x3db5f8da,0xbda973ba,0xbe80f8c0,0xbed3c867,0xbdfab273,0xbea09ae5,0xbefc5d27,0xbf273656,
0xbea80b1f,0xbf06f686,0xbf33587a,0xbf570348,0xbf04f484,0xbf36206c,0xbf5c61c7,0xbf756d97,0xbf2ff3e6,0xbf5b2c79,0xbf76841b,0xbf7fffb1,
0xbf531849,0xbf740bdd,0xbf7ffb11,0xbf75dec6,0xbf6cd007,0xbf7f5ce9,0xbf7822d1,0xbf57dc7f,0xbf7bf531,0xbf7c7eb0,0xbf5f8327,0xbf286605,
0xbf7fdaaf,0xbf6b99fb,0xbf37c655,0xbed6a399,0x3f800000,0x3f800000,0x3f800000,0x3f800000,0x3f73ced9,0x3f71d19f,0x3f6faf1b,0x3f6d67a1,
0x3f5064af,0x3f48d8b3,0x3f40d0da,0x3f385216,0x3f19210f,0x3f099f4e,0x3ef2bd43,0x3ed0eb2a,0x3ea68f12,0x3e6c9a7f,0x3e09cf86,0x3d16c32c,
0x3cafea69,0xbdcefdb7,0xbe636c83,0xbeadf73c,0xbe919ddd,0xbed8106b,0xbf0cead0,0xbf2ac082,0xbf102dd5,0xbf3238aa,0xbf4f0417,0xbf65b710,
0xbf49d112,0xbf64aa59,0xbf76ba07,0xbf7f4e6d,0xbf703b37,0xbf7dc68c,0xbf7efcc9,0xbf73ced9,0xbf7fc38f,0xbf7ac516,0xbf66becc,0xbf44e3f5,
0xbf76ef5b,0xbf5bfb34,0xbf311722,0xbef2bd43,0xbf5695e5,0xbf24d225,0xbec9b953,0xbde1bc2e,0xbf21cbfb,0xbeb6ccc3,0xbd3c6dd5,0x3e8a11f7,
0xbebb31a0,0xbcfb49ba,0x3e9d9e78,0x3f1c420c,0xbd83db0a,0x3e9920fe,0x3f1f599f,0x3f5cc7d1,0,0,0,0,
0x3c7b514b,0x3d2351cb,0x3d83db0a,0x3db5f8da,0x3cfb49ba,0x3da3308c,0x3e039502,0x3e354098,0x3d3c6dd5,0x3df475c0,0x3e44b0b9,0x3e870ada,
0x3d7b2b74,0x3e22abb6,0x3e827dc0,0x3eb263ef,0x3d9cecf9,0x3e4ada4f,0x3ea21887,0x3edc53c1,0x3dbc3ac3,0x3e72b651,0x3ec1071e,0x3f0242b1,
0x3ddb7d37,0x3e8d17c0,0x3edf28a9,0x3f1553b7,0x3dfab273,0x3ea09ae5,0x3efc5d27,0x3f273656,0x3e0cec4a,0x3eb3dca5,0x3f0c42c9,0x3f37c655,
0x3e1c76de,0x3ec6d529,0x3f19c200,0x3f46e22a,0x3e2bf804,0x3ed97cb9,0x3f269de3,0x3f546b3b,0x3e3b6ecf,0x3eebcbbb,0x3f32c8c9,0x3f604621,
0x3e4ada4f,0x3efdbabb,0x3f3e35c5,0x3f6a5ad9,0x3e5a3997,0x3f07a136,0x3f48d8b3,0x3f7294f8,0x3e698bba,0x3f102dd5,0x3f52a649,0x3f78e3d6,
0,0,0,0,0x3de7fa9a,0x3e0cec4a,0x3e25c58c,0x3e3e853e,0x3e667c66,0x3e8b9507,0x3ea395c5,0x3ebb31a0,
0x3eab0201,0x3ece0bea,0x3eeff7fb,0x3f084b92,0x3ee0924f,0x3f064b82,0x3f1b02c6,0x3f2e3bde,0x3f099f4e,0x3f2302d3,0x3f39f2ac,0x3f4e1689,
0x3f212ff9,0x3f3ca003,0x3f53fac3,0x3f66becc,0x3f36ad7f,0x3f52a649,0x3f686b39,0x3f77583a,0x3f49d112,0x3f64aa59,0x3f76ba07,0x3f7f4e6d,
0x3f5a5ba0,0x3f725470,0x3f7e868b,0x3f7e5a38,0x3f6816a8,0x3f7b61fc,0x3f7f9c18,0x3f748422,0x3f72d4eb,0x3f7fa6e3,0x3f79f35e,0x3f62241a,
0x3f7a7302,0x3f7f0e58,0x3f6db293,0x3f47de65,0x3f7ed7d4,0x3f799b43,0x3f5b2c79,0x3f269de3,0x3f7ff4e6,0x3f6f6830,0x3f42de29,0x3eff17b2,
0x3f7dc68c,0x3f60a6cf,0x3f256bcb,0x3ea80b1f,0,0,0,0,0x3e57278f,0x3e6fa8b2,0x3e840270,0x3e901c2c,
0x3ed25a09,0x3ee900b7,0x3eff17b2,0x3f0a48ad,0x3f17de12,0x3f269de3,0x3f34768f,0x3f4154e1,0x3f3fc767,0x3f4f7a1f,0x3f5d2d53,0x3f68bf3c,
0x3f5f20db,0x3f6cd007,0x3f76ef5b,0x3f7d57de,0x3f748422,0x3f7cfe73,0x3f7ffec4,0x3f7d7474,0x3f7efcc9,0x3f7f1f49,0x3f77beb7,0x3f6912ae,
0x3f7e1324,0x3f731447,0x3f5ebe05,0x3f41d870,0x3f71d19f,0x3f5988ad,0x3f36ad7f,0x3f0af1b7,0x3f5ac450,0x3f33e7bc,0x3f0242b1,0x3e919ddd,
0x3f39f2ac,0x3f044889,0x3e8a11f7,0x3b490fc6,0x3f10d3cd,0x3e9aa086,0x3c490e90,0xbe8e9a22,0x3ec27b55,0x3d9064b4,0xbe7bdba4,0xbf099f4e,
0x3e354098,0xbe28defc,0xbef9a02d,0xbf40d0da,0xbd0a342f,0xbec8477c,0xbf3238aa,0xbf686b39,0,0,0,0,
0x3e9c1faf,0x3ea80b1f,0x3eb3dca5,0x3ebf926f,0x3f14b039,0x3f1ebc12,0x3f286605,0x3f31a81d,0x3f4d26fd,0x3f57dc7f,0x3f61668a,0x3f69b7e4,
0x3f721352,0x3f791298,0x3f7dabcc,0x3f7fd397,0x3f7ff0e3,0x3f7eb069,0x3f799b43,0x3f70c501,0x3f756d97,0x3f6816a8,0x3f55b993,0x3f3ebc1b,
0x3f5389c7,0x3f37c655,0x3f16999f,0x3ee1fb6a,0x3f1d7fd1,0x3ee63375,0x3e888e93,0x3d96a905,0x3eb0eacb,0x3e06b26e,0xbdb5f8da,0xbe9c1faf,
0x3d2fe007,0xbe4dee60,0xbeddbe79,0xbf239da9,0xbe870ada,0xbf02ef9f,0xbf38dd65,0xbf61668a,0xbf0b9a6b,0xbf43e200,0xbf6b4b0c,0xbf7e70b0,
0xbf466354,0xbf6f20b0,0xbf7fba9e,0xbf76841b,0xbf6e46be,0xbf7fe129,0xbf73913f,0xbf4ac77f,0xbf7f7808,0xbf74484b,0xbf485bca,0xbf019573,
0x3400,0x3400,0x3400,0x3400,0xabc633c3,0xb0c4326d,0xb2dd301d,0xb3ea28b2,0xaf8b330e,0xb3a82ca5,0xb30eaf8b,0xaca5b3a8,
0xb15f31ed,0xb388ad64,0xa248b3fe,0x333baed7,0xb2a73072,0xb072b2a7,0x32a7b072,0x307232a7,0xb3882d64,0x2248b3fe,0x333b2ed7,0xb1ed315f,
0xb3f62646,0x3113b22f,0x264633f6,0xb22fb113,0xb3eaa8b2,0x33c3abc6,0xb26d30c4,0x301db2dd,0xb364ae1f,0x33642e1f,0xb364ae1f,0x33642e1f,
0xb26db0c4,0x301d32dd,0xa8b2b3ea,0xabc633c3,0xb113b22f,0xa64633f6,0x322fb113,0xb3f6a646,0xaed7b33b,0xb15f31ed,0x33882d64,0xa248b3fe,
0xaa3eb3d9,0xb3d92a3e,0x2a3e33d9,0x33d9aa3e,0x2248b3fe,0xb33baed7,0xb1ed315f,0x2d643388,0x2ca5b3a8,0xaf8bb30e,0xb3a8aca5,0xb30e2f8b,
0x301db2dd,0x28b2b3ea,0xabc6b3c3,0xb0c4b26d,0x3400,0x3400,0x3400,0x3400,0xb3c3abc6,0xb26db0c4,0xb01db2dd,0xa8b2b3ea,
0x2f8bb30e,0x33a8aca5,0x330e2f8b,0x2ca533a8,0x31ed315f,0xad643388,0xb3fe2248,0xaed7b33b,0xb2a73072,0xb072b2a7,0x32a7b072,0x307232a7,
0xad64b388,0x33fe2248,0xaed7333b,0xb15fb1ed,0x33f6a646,0xb113322f,0xa646b3f6,0x322f3113,0xa8b233ea,0xabc6b3c3,0x30c4326d,0xb2ddb01d,
0xb364ae1f,0x33642e1f,0xb364ae1f,0x33642e1f,0x30c4b26d,0xb2dd301d,0x33eaa8b2,0xb3c3abc6,0x3113322f,0x2646b3f6,0xb22f3113,0x33f62646,
0xb33b2ed7,0x31ed315f,0x2d64b388,0xb3fe2248,0xaa3eb3d9,0xb3d92a3e,0x2a3e33d9,0x33d9aa3e,0x33fe2248,0x2ed7b33b,0xb15fb1ed,0xb3882d64,
0xaca533a8,0x2f8b330e,0x33a82ca5,0x330eaf8b,0xb2ddb01d,0xb3eaa8b2,0xb3c32bc6,0xb26d30c4,0x3400,0x3400,0x3400,0x3400,
0x2bc6b3c3,0x30c4b26d,0x32ddb01d,0x33eaa8b2,0xaf8b330e,0xb3a82ca5,0xb30eaf8b,0xaca5b3a8,0x315fb1ed,0x33882d64,0x224833fe,0xb33b2ed7,
0xb2a73072,0xb072b2a7,0x32a7b072,0x307232a7,0x3388ad64,0xa24833fe,0xb33baed7,0x31edb15f,0xb3f62646,0x3113b22f,0x264633f6,0xb22fb113,
0x33ea28b2,0xb3c32bc6,0x326db0c4,0xb01d32dd,0xb364ae1f,0x33642e1f,0xb364ae1f,0x33642e1f,0x326d30c4,0xb01db2dd,0x28b233ea,0x2bc6b3c3,
0xb113b22f,0xa64633f6,0x322fb113,0xb3f6a646,0x2ed7333b,0x315fb1ed,0xb388ad64,0x224833fe,0xaa3eb3d9,0xb3d92a3e,0x2a3e33d9,0x33d9aa3e,
0xa24833fe,0x333b2ed7,0x31edb15f,0xad64b388,0x2ca5b3a8,0xaf8bb30e,0xb3a8aca5,0xb30e2f8b,0xb01d32dd,0xa8b233ea,0x2bc633c3,0x30c4326d,
0x3400,0x3400,0x3400,0x3400,0x33c32bc6,0x326d30c4,0x301d32dd,0x28b233ea,0x2f8bb30e,0x33a8aca5,0x330e2f8b,0x2ca533a8,
0xb1edb15f,0x2d64b388,0x33fea248,0x2ed7333b,0xb2a73072,0xb072b2a7,0x32a7b072,0x307232a7,0x2d643388,0xb3fea248,0x2ed7b33b,0x315f31ed,
0x33f6a646,0xb113322f,0xa646b3f6,0x322f3113,0x28b2b3ea,0x2bc633c3,0xb0c4b26d,0x32dd301d,0xb364ae1f,0x33642e1f,0xb364ae1f,0x33642e1f,
0xb0c4326d,0x32ddb01d,0xb3ea28b2,0x33c32bc6,0x3113322f,0x2646b3f6,0xb22f3113,0x33f62646,0x333baed7,0xb1edb15f,0xad643388,0x33fea248,
0xaa3eb3d9,0xb3d92a3e,0x2a3e33d9,0x33d9aa3e,0xb3fea248,0xaed7333b,0x315f31ed,0x3388ad64,0xaca533a8,0x2f8b330e,0x33a82ca5,0x330eaf8b,
0x32dd301d,0x33ea28b2,0x33c3abc6,0x326db0c4,0x34000000,0x34000000,0x34000000,0x34000000,0x33c32bc6,0x326d30c4,0x301d32dd,0x28b233ea,
0x330e2f8b,0x2ca533a8,0xaf8b330e,0xb3a82ca5,0x31ed315f,0xad643388,0xb3fe2248,0xaed7b33b,0x307232a7,0xb2a73072,0xb072b2a7,0x32a7b072,
0x2d643388,0xb3fea248,0x2ed7b33b,0x315f31ed,0x264633f6,0xb22fb113,0x33f6a646,0xb113322f,0xa8b233ea,0xabc6b3c3,0x30c4326d,0xb2ddb01d,
0xae1f3364,0x2e1fb364,0xae1f3364,0x2e1fb364,0xb0c4326d,0x32ddb01d,0xb3ea28b2,0x33c32bc6,0xb22f3113,0x33f62646,0xb113b22f,0xa64633f6,
0xb33b2ed7,0x31ed315f,0x2d64b388,0xb3fe2248,0xb3d92a3e,0x2a3e33d9,0x33d9aa3e,0xaa3eb3d9,0xb3fea248,0xaed7333b,0x315f31ed,0x3388ad64,
0xb3a8aca5,0xb30e2f8b,0xaca533a8,0x2f8b330e,0xb2ddb01d,0xb3eaa8b2,0xb3c32bc6,0xb26d30c4,0x34000000,0x34000000,0x34000000,0x34000000,
0xabc633c3,0xb0c4326d,0xb2dd301d,0xb3ea28b2,0xb30eaf8b,0xaca5b3a8,0x2f8bb30e,0x33a8aca5,0x315fb1ed,0x33882d64,0x224833fe,0xb33b2ed7,
0x307232a7,0xb2a73072,0xb072b2a7,0x32a7b072,0xb3882d64,0x2248b3fe,0x333b2ed7,0xb1ed315f,0xa646b3f6,0x322f3113,0xb3f62646,0x3113b22f,
0x33ea28b2,0xb3c32bc6,0x326db0c4,0xb01d32dd,0xae1f3364,0x2e1fb364,0xae1f3364,0x2e1fb364,0xb26db0c4,0x301d32dd,0xa8b2b3ea,0xabc633c3,
0x322fb113,0xb3f6a646,0x3113322f,0x2646b3f6,0x2ed7333b,0x315fb1ed,0xb388ad64,0x224833fe,0xb3d92a3e,0x2a3e33d9,0x33d9aa3e,0xaa3eb3d9,
0x2248b3fe,0xb33baed7,0xb1ed315f,0x2d643388,0x33a82ca5,0x330eaf8b,0x2ca5b3a8,0xaf8bb30e,0xb01d32dd,0xa8b233ea,0x2bc633c3,0x30c4326d,
0x34000000,0x34000000,0x34000000,0x34000000,0xb3c3abc6,0xb26db0c4,0xb01db2dd,0xa8b2b3ea,0x330e2f8b,0x2ca533a8,0xaf8b330e,0xb3a82ca5,
0xb1edb15f,0x2d64b388,0x33fea248,0x2ed7333b,0x307232a7,0xb2a73072,0xb072b2a7,0x32a7b072,0xad64b388,0x33fe2248,0xaed7333b,0xb15fb1ed,
0x264633f6,0xb22fb113,0x33f6a646,0xb113322f,0x28b2b3ea,0x2bc633c3,0xb0c4b26d,0x32dd301d,0xae1f3364,0x2e1fb364,0xae1f3364,0x2e1fb364,
0x30c4b26d,0xb2dd301d,0x33eaa8b2,0xb3c3abc6,0xb22f3113,0x33f62646,0xb113b22f,0xa64633f6,0x333baed7,0xb1edb15f,0xad643388,0x33fea248,
0xb3d92a3e,0x2a3e33d9,0x33d9aa3e,0xaa3eb3d9,0x33fe2248,0x2ed7b33b,0xb15fb1ed,0xb3882d64,0xb3a8aca5,0xb30e2f8b,0xaca533a8,0x2f8b330e,
0x32dd301d,0x33ea28b2,0x33c3abc6,0x326db0c4,0x34000000,0x34000000,0x34000000,0x34000000,0x2bc6b3c3,0x30c4b26d,0x32ddb01d,0x33eaa8b2,
0xb30eaf8b,0xaca5b3a8,0x2f8bb30e,0x33a8aca5,0xb15f31ed,0xb388ad64,0xa248b3fe,0x333baed7,0x307232a7,0xb2a73072,0xb072b2a7,0x32a7b072,
0x3388ad64,0xa24833fe,0xb33baed7,0x31edb15f,0xa646b3f6,0x322f3113,0xb3f62646,0x3113b22f,0xb3eaa8b2,0x33c3abc6,0xb26d30c4,0x301db2dd,
0xae1f3364,0x2e1fb364,0xae1f3364,0x2e1fb364,0x326d30c4,0xb01db2dd,0x28b233ea,0x2bc6b3c3,0x322fb113,0xb3f6a646,0x3113322f,0x2646b3f6,
0xaed7b33b,0xb15f31ed,0x33882d64,0xa248b3fe,0xb3d92a3e,0x2a3e33d9,0x33d9aa3e,0xaa3eb3d9,0xa24833fe,0x333b2ed7,0x31edb15f,0xad64b388,
0x33a82ca5,0x330eaf8b,0x2ca5b3a8,0xaf8bb30e,0x301db2dd,0x28b2b3ea,0xabc6b3c3,0xb0c4b26d,0x3400,0x2ca533a8,0x307232a7,0x322f3113,
0x3400,0x3113322f,0x33d92a3e,0x330eaf8b,0x3400,0x330e2f8b,0x32a7b072,0xa646b3f6,0x3400,0x33f62646,0x2a3eb3d9,0xb3a8aca5,
0x3400,0x33a8aca5,0xb072b2a7,0xb113322f,0x3400,0x322fb113,0xb3d9aa3e,0x2f8b330e,0x3400,0x2f8bb30e,0xb2a73072,0x33f6a646,
0x3400,0x2646b3f6,0xaa3e33d9,0x2ca5b3a8,0x3400,0xaca5b3a8,0x307232a7,0xb22fb113,0x3400,0xb113b22f,0x33d92a3e,0xb30e2f8b,
0x3400,0xb30eaf8b,0x32a7b072,0x264633f6,0x3400,0xb3f6a646,0x2a3eb3d9,0x33a82ca5,0x3400,0xb3a82ca5,0xb072b2a7,0x3113b22f,
0x3400,0xb22f3113,0xb3d9aa3e,0xaf8bb30e,0x3400,0xaf8b330e,0xb2a73072,0xb3f62646,0x3400,0xa64633f6,0xaa3e33d9,0xaca533a8,
0x33642e1f,0x33f62646,0x33d9aa3e,0x330eaf8b,0x2e1fb364,0xaca5b3a8,0xb2a7b072,0xb3f62646,0xb364ae1f,0xb22f3113,0x2a3e33d9,0x33a82ca5,
0xae1f3364,0x330e2f8b,0x3072b2a7,0xb22fb113,0x33642e1f,0x2646b3f6,0xb3d92a3e,0x2f8b330e,0x2e1fb364,0xb3a82ca5,0x32a73072,0xa646b3f6,
0xb364ae1f,0x3113322f,0xaa3eb3d9,0xaca533a8,0xae1f3364,0x2f8bb30e,0xb07232a7,0x3113b22f,0x33642e1f,0xb3f6a646,0x33d9aa3e,0xb30e2f8b,
0x2e1fb364,0x2ca533a8,0xb2a7b072,0x33f6a646,0xb364ae1f,0x322fb113,0x2a3e33d9,0xb3a8aca5,0xae1f3364,0xb30eaf8b,0x3072b2a7,0x322f3113,
0x33642e1f,0xa64633f6,0xb3d92a3e,0xaf8bb30e,0x2e1fb364,0x33a8aca5,0x32a73072,0x264633f6,0xb364ae1f,0xb113b22f,0xaa3eb3d9,0x2ca5b3a8,
0xae1f3364,0xaf8b330e,0xb07232a7,0xb113322f,0x31a8b1a8,0x2f8bb30e,0x2a3eb3d9,0xa646b3f6,0xb1a831a8,0xa64633f6,0x307232a7,0x33a82ca5,
0x31a8b1a8,0xaca5b3a8,0xb3d9aa3e,0xb113322f,0xb1a831a8,0x3113322f,0x32a7b072,0xaf8bb30e,0x31a8b1a8,0xb30eaf8b,0xaa3e33d9,0x33f6a646,
0xb1a831a8,0x33f62646,0xb072b2a7,0xaca533a8,0x31a8b1a8,0xb3a82ca5,0x33d92a3e,0xb22fb113,0xb1a831a8,0x322fb113,0xb2a73072,0x330eaf8b,
0x31a8b1a8,0xaf8b330e,0x2a3eb3d9,0x264633f6,0xb1a831a8,0x2646b3f6,0x307232a7,0xb3a8aca5,0x31a8b1a8,0x2ca533a8,0xb3d9aa3e,0x3113b22f,
0xb1a831a8,0xb113b22f,0x32a7b072,0x2f8b330e,0x31a8b1a8,0x330e2f8b,0xaa3e33d9,0xb3f62646,0xb1a831a8,0xb3f6a646,0xb072b2a7,0x2ca5b3a8,
0x31a8b1a8,0x33a8aca5,0x33d92a3e,0x322f3113,0xb1a831a8,0xb22f3113,0xb2a73072,0xb30e2f8b,0xae1fb364,0xb113b22f,0xb2a7b072,0xb3a8aca5,
0x3364ae1f,0x2f8bb30e,0xaa3eb3d9,0xb22fb113,0x2e1f3364,0x33f62646,0x3072b2a7,0xaf8bb30e,0xb3642e1f,0x2ca533a8,0x33d9aa3e,0xa646b3f6,
0xae1fb364,0xb22f3113,0x32a73072,0x2ca5b3a8,0x3364ae1f,0xb30eaf8b,0x2a3e33d9,0x3113b22f,0x2e1f3364,0x2646b3f6,0xb07232a7,0x330eaf8b,
0xb3642e1f,0x33a8aca5,0xb3d92a3e,0x33f6a646,0xae1fb364,0x3113322f,0xb2a7b072,0x33a82ca5,0x3364ae1f,0xaf8b330e,0xaa3eb3d9,0x322f3113,
0x2e1f3364,0xb3f6a646,0x3072b2a7,0x2f8b330e,0xb3642e1f,0xaca5b3a8,0x33d9aa3e,0x264633f6,0xae1fb364,0x322fb113,0x32a73072,0xaca533a8,
0x3364ae1f,0x330e2f8b,0x2a3e33d9,0xb113322f,0x2e1f3364,0xa64633f6,0xb07232a7,0xb30e2f8b,0xb3642e1f,0xb3a82ca5,0xb3d92a3e,0xb3f62646,
0x34000000,0x33a8aca5,0x32a7b072,0x3113b22f,0x34000000,0x322fb113,0x2a3eb3d9,0xaf8bb30e,0x34000000,0x2f8bb30e,0xb072b2a7,0xb3f62646,
0x34000000,0x2646b3f6,0xb3d9aa3e,0xaca533a8,0x34000000,0xaca5b3a8,0xb2a73072,0x322f3113,0x34000000,0xb113b22f,0xaa3e33d9,0x330eaf8b,
0x34000000,0xb30eaf8b,0x307232a7,0xa646b3f6,0x34000000,0xb3f6a646,0x33d92a3e,0xb3a8aca5,0x34000000,0xb3a82ca5,0x32a7b072,0xb113322f,
0x34000000,0xb22f3113,0x2a3eb3d9,0x2f8b330e,0x34000000,0xaf8b330e,0xb072b2a7,0x33f6a646,0x34000000,0xa64633f6,0xb3d9aa3e,0x2ca5b3a8,
0x34000000,0x2ca533a8,0xb2a73072,0xb22fb113,0x34000000,0x3113322f,0xaa3e33d9,0xb30e2f8b,0x34000000,0x330e2f8b,0x307232a7,0x264633f6,
0x34000000,0x33f62646,0x33d92a3e,0x33a82ca5,0x2e1fb364,0x2646b3f6,0xaa3eb3d9,0xaf8bb30e,0xb364ae1f,0xb3a82ca5,0xb07232a7,0x264633f6,
0xae1f3364,0x3113322f,0x33d9aa3e,0x2ca5b3a8,0x33642e1f,0x2f8bb30e,0xb2a7b072,0xb113322f,0x2e1fb364,0xb3f6a646,0x2a3e33d9,0x330eaf8b,
0xb364ae1f,0x2ca533a8,0x3072b2a7,0xb3f62646,0xae1f3364,0x322fb113,0xb3d92a3e,0x33a82ca5,0x33642e1f,0xb30eaf8b,0x32a73072,0xb22fb113,
0x2e1fb364,0xa64633f6,0xaa3eb3d9,0x2f8b330e,0xb364ae1f,0x33a8aca5,0xb07232a7,0xa646b3f6,0xae1f3364,0xb113b22f,0x33d9aa3e,0xaca533a8,
0x33642e1f,0xaf8b330e,0xb2a7b072,0x3113b22f,0x2e1fb364,0x33f62646,0x2a3e33d9,0xb30e2f8b,0xb364ae1f,0xaca5b3a8,0x3072b2a7,0x33f6a646,
0xae1f3364,0xb22f3113,0xb3d92a3e,0xb3a8aca5,0x33642e1f,0x330e2f8b,0x32a73072,0x322f3113,0xb1a8b1a8,0xb30eaf8b,0xb3d9aa3e,0xb3f62646,
0x31a831a8,0x33f62646,0x32a7b072,0x2ca5b3a8,0xb1a8b1a8,0xb3a82ca5,0xaa3e33d9,0x322f3113,0x31a831a8,0x322fb113,0xb072b2a7,0xb30e2f8b,
0xb1a8b1a8,0xaf8b330e,0x33d92a3e,0xa646b3f6,0x31a831a8,0x2646b3f6,0xb2a73072,0x33a82ca5,0xb1a8b1a8,0x2ca533a8,0x2a3eb3d9,0xb113322f,
0x31a831a8,0xb113b22f,0x307232a7,0xaf8bb30e,0xb1a8b1a8,0x330e2f8b,0xb3d9aa3e,0x33f6a646,0x31a831a8,0xb3f6a646,0x32a7b072,0xaca533a8,
0xb1a8b1a8,0x33a8aca5,0xaa3e33d9,0xb22fb113,0x31a831a8,0xb22f3113,0xb072b2a7,0x330eaf8b,0xb1a8b1a8,0x2f8bb30e,0x33d92a3e,0x264633f6,
0x31a831a8,0xa64633f6,0xb2a73072,0xb3a8aca5,0xb1a8b1a8,0xaca5b3a8,0x2a3eb3d9,0x3113b22f,0x31a831a8,0x3113322f,0x307232a7,0x2f8b330e,
0xb3642e1f,0xb22f3113,0xb07232a7,0xaca533a8,0xae1fb364,0xb30eaf8b,0xb3d92a3e,0xb113322f,0x3364ae1f,0x2646b3f6,0xb2a7b072,0xb30e2f8b,
0x2e1f3364,0x33a8aca5,0xaa3eb3d9,0xb3f62646,0xb3642e1f,0x3113322f,0x3072b2a7,0xb3a8aca5,0xae1fb364,0xaf8b330e,0x33d9aa3e,0xb22fb113,
0x3364ae1f,0xb3f6a646,0x32a73072,0xaf8bb30e,0x2e1f3364,0xaca5b3a8,0x2a3e33d9,0xa646b3f6,0xb3642e1f,0x322fb113,0xb07232a7,0x2ca5b3a8,
0xae1fb364,0x330e2f8b,0xb3d92a3e,0x3113b22f,0x3364ae1f,0xa64633f6,0xb2a7b072,0x330eaf8b,0x2e1f3364,0xb3a82ca5,0xaa3eb3d9,0x33f6a646,
0xb3642e1f,0xb113b22f,0x3072b2a7,0x33a82ca5,0xae1fb364,0x2f8bb30e,0x33d9aa3e,0x322f3113,0x3364ae1f,0x33f62646,0x32a73072,0x2f8b330e,
0x2e1f3364,0x2ca533a8,0x2a3e33d9,0x264633f6,0x3f800000,0x3f7ff4e6,0x3f7fd397,0x3f7f9c18,0x3f800000,0x3f7fc38f,0x3f7f0e58,0x3f7de0b1,
0x3f800000,0x3f7f6ac7,0x3f7dabcc,0x3f7ac516,0x3f800000,0x3f7eea9d,0x3f7baccd,0x3f764d97,0x3f800000,0x3f7e4323,0x3f791298,0x3f708066,
0x3f800000,0x3f7d7474,0x3f75dec6,0x3f696591,0x3f800000,0x3f7c7eb0,0x3f721352,0x3f6106f2,0x3f800000,0x3f7b61fc,0x3f6db293,0x3f577026,
0x3f800000,0x3f7a1e84,0x3f68bf3c,0x3f4cae79,0x3f800000,0x3f78b47b,0x3f633c5a,0x3f40d0da,0x3f800000,0x3f772417,0x3f5d2d53,0x3f33e7bc,
0x3f800000,0x3f756d97,0x3f5695e5,0x3f26050a,0x3f800000,0x3f73913f,0x3f4f7a1f,0x3f173c07,0x3f800000,0x3f718f57,0x3f47de65,0x3f07a136,
0x3f800000,0x3f6f6830,0x3f3fc767,0x3eee9479,0x3f800000,0x3f6d1c1d,0x3f373a23,0x3ecc9b8b,0x3f7f4e6d,0x3f7eea9d,0x3f7e70b0,0x3f7de0b1,
0x3f7c3b28,0x3f7a1e84,0x3f778bc5,0x3f748422,0x3f76ba07,0x3f718f57,0x3f6b4b0c,0x3f63f473,0x3f6ed89e,0x3f655e0b,0x3f59f26a,0x3f4cae79,
0x3f64aa59,0x3f55b993,0x3f43e200,0x3f2f61a5,0x3f584853,0x3f42de29,0x3f299415,0x3f0cead0,0x3f49d112,0x3f2d1469,0x3f0b9a6b,0x3ecc9b8b,
0x3f396842,0x3f14b039,0x3ed53641,0x3e72b651,0x3f273656,0x3ef41f07,0x3e8e9a22,0x3d8a200a,0x3f13682a,0x3ebb31a0,0x3e09cf86,0xbdd53db9,
0x3efc5d27,0x3e7ee6e1,0xbc490e90,0xbe8b9507,0x3ecf7bca,0x3e039502,0xbe22abb6,0xbeddbe79,0x3ea09ae5,0x3bc90f88,0xbe9aa086,0xbf14b039,
0x3e605c13,0xbdee3876,0xbee0924f,0xbf36206c,0x3dfab273,0xbe72b651,0xbf10d3cd,0xbf5233c6,0x3cc90ab0,0xbeb554ec,0xbf2e3bde,0xbf6816a8,
0x3f7d3aac,0x3f7c7eb0,0x3f7baccd,0x3f7ac516,0x3f710908,0x3f6d1c1d,0x3f68bf3c,0x3f63f473,0x3f5b941a,0x3f5233c6,0x3f47de65,0x3f3ca003,
0x3f3daef9,0x3f2d1469,0x3f1b02c6,0x3f07a136,0x3f187fc0,0x3eff17b2,0x3ec9b953,0x3e919ddd,0x3edae880,0x3e97a117,0x3e22abb6,0x3c96c9b6,
0x3e78cfcc,0x3da3308c,0xbdafb680,0xbe7ee6e1,0x3d48fb30,0xbe1008b7,0xbea68f12,0xbeff17b2,0xbe164083,0xbeb554ec,0xbf0b9a6b,0xbf36206c,
0xbeac7cd4,0xbf0cead0,0xbf3b8f3b,0xbf5f8327,0xbf039c3d,0xbf385216,0xbf604621,0xbf78b47b,0xbf2beb4a,0xbf5ac450,0xbf778bc5,0xbf7fe129,
0xbf4d9f02,0xbf7294f8,0xbf7ffb11,0xbf748422,0xbf676bd8,0xbf7e9bc9,0xbf791298,0xbf577026,0xbf7853f8,0xbf7e4323,0xbf633c5a,0xbf2ac082,
0xbf7fb10f,0xbf718f57,0xbf3fc767,0xbee363fa,0x3f79c79d,0x3f78b47b,0x3f778bc5,0x3f764d97,0x3f5ebe05,0x3f591e6a,0x3f531849,0x3f4cae79,
0x3f3085bb,0x3f239da9,0x3f15f6d9,0x3f07a136,0x3ee63375,0x3ebb31a0,0x3e8e9a22,0x3e419b37,0x3e2f10a2,0x3d621469,0xbd7b2b74,0xbe354098,
0xbdfab273,0xbe8586ce,0xbec9b953,0xbf04f484,0xbecf7bca,0xbf0cead0,0xbf2e3bde,0xbf4ac77f,0xbf273656,0xbf48d8b3,0xbf633c5a,0xbf756d97,
0xbf584853,0xbf708066,0xbf7dabcc,0xbf7f2f9d,0xbf76ba07,0xbf7fe129,0xbf7a7302,0xbf66becc,0xbf7fec43,0xbf756d97,0xbf59f26a,0xbf2f61a5,
0xbf731447,0xbf5233c6,0xbf1ff6cb,0xbec1071e,0xbf514d3d,0xbf19c200,0xbea68f12,0xbc96c9b6,0xbf1d7fd1,0xbea395c5,0x3c490e90,0x3eaf713a,
0xbeb8442a,0xbbc90f88,0x3eb263ef,0x3f286605,0xbd96a905,0x3e9d9e78,0x3f24d225,0x3f628210,0,0xbc96c9b6,0xbd16c32c,0xbd621469,
0,0xbd2fe007,0xbdafb680,0xbe039502,0,0xbd8a200a,0xbe09cf86,0xbe4dee60,0,0xbdbc3ac3,0xbe3b6ecf,0xbe8b9507,
0,0xbdee3876,0xbe6c9a7f,0xbeaf713a,0,0xbe1008b7,0xbe8e9a22,0xbed25a09,0,0xbe28defc,0xbea68f12,0xbef41f07,
0,0xbe419b37,0xbebe1d4a,0xbf0a48ad,0,0xbe5a3997,0xbed53641,0xbf19c200,0,0xbe72b651,0xbeebcbbb,0xbf286605,
0,0xbe8586ce,0xbf00e7e4,0xbf36206c,0,0xbe919ddd,0xbf0b9a6b,0xbf42de29,0,0xbe9d9e78,0xbf15f6d9,0xbf4e8d90,
0,0xbea986c4,0xbf1ff6cb,0xbf591e6a,0,0xbeb554ec,0xbf299415,0xbf628210,0,0xbec1071e,0xbf32c8c9,0xbf6aab7b,
0xbd96a905,0xbdbc3ac3,0xbde1bc2e,0xbe039502,0xbe2f10a2,0xbe5a3997,0xbe827dc0,0xbe97a117,0xbe888e93,0xbea986c4,0xbec9b953,0xbee900b7,
0xbeb8442a,0xbee363fa,0xbf064b82,0xbf19c200,0xbee63375,0xbf0cead0,0xbf24d225,0xbf3a7ca4,0xbf08f59b,0xbf26050a,0xbf3fc767,0xbf55b993,
0xbf1d7fd1,0xbf3ca003,0xbf5695e5,0xbf6aab7b,0xbf3085bb,0xbf5064af,0xbf68bf3c,0xbf78b47b,0xbf41d870,0xbf6106f2,0xbf75dec6,0xbf7f6ac7,
0xbf514d3d,0xbf6e46be,0xbf7dabcc,0xbf7e9bc9,0xbf5ebe05,0xbf77f110,0xbf7ffb11,0xbf764d97,0xbf6a09a7,0xbf7de0b1,0xbf7cbfc9,0xbf66becc,
0xbf731447,0xbf7ffec4,0xbf740bdd,0xbf5064af,0xbf79c79d,0xbf7e4323,0xbf660f88,0xbf33e7bc,0xbf7e1324,0xbf78b47b,0xbf531849,0xbf121eb0,
0xbf7fec43,0xbf6f6830,0xbf3b8f3b,0xbed8106b,0xbe164083,0xbe28defc,0xbe3b6ecf,0xbe4dee60,0xbeac7cd4,0xbec1071e,0xbed53641,0xbee900b7,
0xbf039c3d,0xbf121eb0,0xbf1ff6cb,0xbf2d1469,0xbf2beb4a,0xbf3ca003,0xbf4bbbf8,0xbf591e6a,0xbf4d9f02,0xbf5df6be,0xbf6b4b0c,0xbf756d97,
0xbf676bd8,0xbf748422,0xbf7cbfc9,0xbf7ff4e6,0xbf7853f8,0xbf7f2f9d,0xbf7f0e58,0xbf77f110,0xbf7fb10f,0xbf7d7474,0xbf721352,0xbf5df6be,
0xbf7d3aac,0xbf6f6830,0xbf5695e5,0xbf33e7bc,0xbf710908,0xbf55b993,0xbf2e3bde,0xbef9a02d,0xbf5b941a,0xbf31a81d,0xbef6e0cb,0xbe72b651,
0xbf3daef9,0xbf04f484,0xbe827dc0,0x3cfb49ba,0xbf187fc0,0xbea395c5,0xbc490e90,0x3e97a117,0xbedae880,0xbdd53db9,0x3e6c9a7f,0x3f0a48ad,
0xbe78cfcc,0x3dee3876,0x3eebcbbb,0x3f3ebc1b,0xbd48fb30,0x3ea986c4,0x3f299415,0x3f655e0b,0xbe605c13,0xbe72b651,0xbe827dc0,0xbe8b9507,
0xbefc5d27,0xbf07a136,0xbf10d3cd,0xbf19c200,0xbf396842,0xbf44e3f5,0xbf4f7a1f,0xbf591e6a,0xbf64aa59,0xbf6e46be,0xbf75dec6,0xbf7b61fc,
0xbf7c3b28,0xbf7f9c18,0xbf7f84ab,0xbf7bf531,0xbf7e1324,0xbf772417,0xbf6b4b0c,0xbf5ac450,0xbf6a09a7,0xbf55b993,0xbf3b8f3b,0xbf1c420c,
0xbf41d870,0xbf1ebc12,0xbeebcbbb,0xbe919ddd,0xbf08f59b,0xbeaf713a,0xbe09cf86,0x3da3308c,0xbe888e93,0xbcfb49ba,0x3e541501,0x3eddbe79,
0x3cc90ab0,0x3e919ddd,0x3f064b82,0x3f3a7ca4,0x3ea09ae5,0x3f121eb0,0x3f47de65,0x3f6d1c1d,0x3f13682a,0x3f4cae79,0x3f721352,0x3f7ff4e6,
0x3f49d112,0x3f7294f8,0x3f7ffb11,0x3f708066,0x3f6ed89e,0x3f7ffec4,0x3f6ff573,0x3f40d0da,0x3f7f4e6d,0x3f73913f,0x3f43e200,0x3eee9479,
0x3f800000,0x3f800000,0x3f800000,0x3f800000,0x3f7ff4e6,0x3f7fc38f,0x3f7f6ac7,0x3f7eea9d,0x3f7fd397,0x3f7f0e58,0x3f7dabcc,0x3f7baccd,
0x3f7f9c18,0x3f7de0b1,0x3f7ac516,0x3f764d97,0x3f7f4e6d,0x3f7c3b28,0x3f76ba07,0x3f6ed89e,0x3f7eea9d,0x3f7a1e84,0x3f718f57,0x3f655e0b,
0x3f7e70b0,0x3f778bc5,0x3f6b4b0c,0x3f59f26a,0x3f7de0b1,0x3f748422,0x3f63f473,0x3f4cae79,0x3f7d3aac,0x3f710908,0x3f5b941a,0x3f3daef9,
0x3f7c7eb0,0x3f6d1c1d,0x3f5233c6,0x3f2d1469,0x3f7baccd,0x3f68bf3c,0x3f47de65,0x3f1b02c6,0x3f7ac516,0x3f63f473,0x3f3ca003,0x3f07a136,
0x3f79c79d,0x3f5ebe05,0x3f3085bb,0x3ee63375,0x3f78b47b,0x3f591e6a,0x3f239da9,0x3ebb31a0,0x3f778bc5,0x3f531849,0x3f15f6d9,0x3e8e9a22,
0x3f764d97,0x3f4cae79,0x3f07a136,0x3e419b37,0x3f800000,0x3f800000,0x3f800000,0x3f800000,0x3f7e4323,0x3f7d7474,0x3f7c7eb0,0x3f7b61fc,
0x3f791298,0x3f75dec6,0x3f721352,0x3f6db293,0x3f708066,0x3f696591,0x3f6106f2,0x3f577026,0x3f64aa59,0x3f584853,0x3f49d112,0x3f396842,
0x3f55b993,0x3f42de29,0x3f2d1469,0x3f14b039,0x3f43e200,0x3f299415,0x3f0b9a6b,0x3ed53641,0x3f2f61a5,0x3f0cead0,0x3ecc9b8b,0x3e72b651,
0x3f187fc0,0x3edae880,0x3e78cfcc,0x3d48fb30,0x3eff17b2,0x3e97a117,0x3da3308c,0xbe1008b7,0x3ec9b953,0x3e22abb6,0xbdafb680,0xbea68f12,
0x3e919ddd,0x3c96c9b6,0xbe7ee6e1,0xbeff17b2,0x3e2f10a2,0xbdfab273,0xbecf7bca,0xbf273656,0x3d621469,0xbe8586ce,0xbf0cead0,0xbf48d8b3,
0xbd7b2b74,0xbec9b953,0xbf2e3bde,0xbf633c5a,0xbe354098,0xbf04f484,0xbf4ac77f,0xbf756d97,0x3f800000,0x3f800000,0x3f800000,0x3f800000,
0x3f7a1e84,0x3f78b47b,0x3f772417,0x3f756d97,0x3f68bf3c,0x3f633c5a,0x3f5d2d53,0x3f5695e5,0x3f4cae79,0x3f40d0da,0x3f33e7bc,0x3f26050a,
0x3f273656,0x3f13682a,0x3efc5d27,0x3ecf7bca,0x3ef41f07,0x3ebb31a0,0x3e7ee6e1,0x3e039502,0x3e8e9a22,0x3e09cf86,0xbc490e90,0xbe22abb6,
0x3d8a200a,0xbdd53db9,0xbe8b9507,0xbeddbe79,0xbe164083,0xbeac7cd4,0xbf039c3d,0xbf2beb4a,0xbeb554ec,0xbf0cead0,0xbf385216,0xbf5ac450,
0xbf0b9a6b,0xbf3b8f3b,0xbf604621,0xbf778bc5,0xbf36206c,0xbf5f8327,0xbf78b47b,0xbf7fe129,0xbf584853,0xbf76ba07,0xbf7fec43,0xbf731447,
0xbf708066,0xbf7fe129,0xbf756d97,0xbf5233c6,0xbf7dabcc,0xbf7a7302,0xbf59f26a,0xbf1ff6cb,0xbf7f2f9d,0xbf66becc,0xbf2f61a5,0xbec1071e,
0x3f800000,0x3f800000,0x3f800000,0x3f800000,0x3f73913f,0x3f718f57,0x3f6f6830,0x3f6d1c1d,0x3f4f7a1f,0x3f47de65,0x3f3fc767,0x3f373a23,
0x3f173c07,0x3f07a136,0x3eee9479,0x3ecc9b8b,0x3ea09ae5,0x3e605c13,0x3dfab273,0x3cc90ab0,0x3bc90f88,0xbdee3876,0xbe72b651,0xbeb554ec,
0xbe9aa086,0xbee0924f,0xbf10d3cd,0xbf2e3bde,0xbf14b039,0xbf36206c,0xbf5233c6,0xbf6816a8,0xbf4d9f02,0xbf676bd8,0xbf7853f8,0xbf7fb10f,
0xbf7294f8,0xbf7e9bc9,0xbf7e4323,0xbf718f57,0xbf7ffb11,0xbf791298,0xbf633c5a,0xbf3fc767,0xbf748422,0xbf577026,0xbf2ac082,0xbee363fa,
0xbf514d3d,0xbf1d7fd1,0xbeb8442a,0xbd96a905,0xbf19c200,0xbea395c5,0xbbc90f88,0x3e9d9e78,0xbea68f12,0x3c490e90,0x3eb263ef,0x3f24d225,
0xbc96c9b6,0x3eaf713a,0x3f286605,0x3f628210,0,0,0,0,0x3c96c9b6,0x3d2fe007,0x3d8a200a,0x3dbc3ac3,
0x3d16c32c,0x3dafb680,0x3e09cf86,0x3e3b6ecf,0x3d621469,0x3e039502,0x3e4dee60,0x3e8b9507,0x3d96a905,0x3e2f10a2,0x3e888e93,0x3eb8442a,
0x3dbc3ac3,0x3e5a3997,0x3ea986c4,0x3ee363fa,0x3de1bc2e,0x3e827dc0,0x3ec9b953,0x3f064b82,0x3e039502,0x3e97a117,0x3ee900b7,0x3f19c200,
0x3e164083,0x3eac7cd4,0x3f039c3d,0x3f2beb4a,0x3e28defc,0x3ec1071e,0x3f121eb0,0x3f3ca003,0x3e3b6ecf,0x3ed53641,0x3f1ff6cb,0x3f4bbbf8,
0x3e4dee60,0x3ee900b7,0x3f2d1469,0x3f591e6a,0x3e605c13,0x3efc5d27,0x3f396842,0x3f64aa59,0x3e72b651,0x3f07a136,0x3f44e3f5,0x3f6e46be,
0x3e827dc0,0x3f10d3cd,0x3f4f7a1f,0x3f75dec6,0x3e8b9507,0x3f19c200,0x3f591e6a,0x3f7b61fc,0,0,0,0,
0x3dee3876,0x3e1008b7,0x3e28defc,0x3e419b37,0x3e6c9a7f,0x3e8e9a22,0x3ea68f12,0x3ebe1d4a,0x3eaf713a,0x3ed25a09,0x3ef41f07,0x3f0a48ad,
0x3ee63375,0x3f08f59b,0x3f1d7fd1,0x3f3085bb,0x3f0cead0,0x3f26050a,0x3f3ca003,0x3f5064af,0x3f24d225,0x3f3fc767,0x3f5695e5,0x3f68bf3c,
0x3f3a7ca4,0x3f55b993,0x3f6aab7b,0x3f78b47b,0x3f4d9f02,0x3f676bd8,0x3f7853f8,0x3f7fb10f,0x3f5df6be,0x3f748422,0x3f7f2f9d,0x3f7d7474,
0x3f6b4b0c,0x3f7cbfc9,0x3f7f0e58,0x3f721352,0x3f756d97,0x3f7ff4e6,0x3f77f110,0x3f5df6be,0x3f7c3b28,0x3f7e1324,0x3f6a09a7,0x3f41d870,
0x3f7f9c18,0x3f772417,0x3f55b993,0x3f1ebc12,0x3f7f84ab,0x3f6b4b0c,0x3f3b8f3b,0x3eebcbbb,0x3f7bf531,0x3f5ac450,0x3f1c420c,0x3e919ddd,
0,0,0,0,0x3e5a3997,0x3e72b651,0x3e8586ce,0x3e919ddd,0x3ed53641,0x3eebcbbb,0x3f00e7e4,0x3f0b9a6b,
0x3f19c200,0x3f286605,0x3f36206c,0x3f42de29,0x3f41d870,0x3f514d3d,0x3f5ebe05,0x3f6a09a7,0x3f6106f2,0x3f6e46be,0x3f77f110,0x3f7de0b1,
0x3f75dec6,0x3f7dabcc,0x3f7ffb11,0x3f7cbfc9,0x3f7f6ac7,0x3f7e9bc9,0x3f764d97,0x3f66becc,0x3f7d3aac,0x3f710908,0x3f5b941a,0x3f3daef9,
0x3f6f6830,0x3f55b993,0x3f31a81d,0x3f04f484,0x3f5695e5,0x3f2e3bde,0x3ef6e0cb,0x3e827dc0,0x3f33e7bc,0x3ef9a02d,0x3e72b651,0xbcfb49ba,
0x3f08f59b,0x3e888e93,0xbcc90ab0,0xbea09ae5,0x3eaf713a,0x3cfb49ba,0xbe919ddd,0xbf121eb0,0x3e09cf86,0xbe541501,0xbf064b82,0xbf47de65,
0xbda3308c,0xbeddbe79,0xbf3a7ca4,0xbf6d1c1d,0,0,0,0,0x3e9d9e78,0x3ea986c4,0x3eb554ec,0x3ec1071e,
0x3f15f6d9,0x3f1ff6cb,0x3f299415,0x3f32c8c9,0x3f4e8d90,0x3f591e6a,0x3f628210,0x3f6aab7b,0x3f731447,0x3f79c79d,0x3f7e1324,0x3f7fec43,
0x3f7ffec4,0x3f7e4323,0x3f78b47b,0x3f6f6830,0x3f740bdd,0x3f660f88,0x3f531849,0x3f3b8f3b,0x3f5064af,0x3f33e7bc,0x3f121eb0,0x3ed8106b,
0x3f187fc0,0x3edae880,0x3e78cfcc,0x3d48fb30,0x3ea395c5,0x3dd53db9,0xbdee3876,0xbea986c4,0x3c490e90,0xbe6c9a7f,0xbeebcbbb,0xbf299415,
0xbe97a117,0xbf0a48ad,0xbf3ebc1b,0xbf655e0b,0xbf13682a,0xbf49d112,0xbf6ed89e,0xbf7f4e6d,0xbf4cae79,0xbf7294f8,0xbf7ffec4,0xbf73913f,
0xbf721352,0xbf7ffb11,0xbf6ff573,0xbf43e200,0xbf7ff4e6,0xbf708066,0xbf40d0da,0xbeee9479,0x3400,0x3400,0x3400,0x3400,
0xaca533a8,0xb113322f,0xb30e2f8b,0xb3f62646,0xb07232a7,0xb3d92a3e,0xb2a7b072,0xaa3eb3d9,0xb22f3113,0xb30eaf8b,0x2646b3f6,0x33a8aca5,
0xb3642e1f,0xae1fb364,0x3364ae1f,0x2e1f3364,0xb3f62646,0x2ca5b3a8,0x322f3113,0xb30e2f8b,0xb3d9aa3e,0x32a7b072,0xaa3e33d9,0xb072b2a7,
0xb30eaf8b,0x33f62646,0xb3a82ca5,0x322fb113,0xb1a8b1a8,0x31a831a8,0xb1a8b1a8,0x31a831a8,0xaf8bb30e,0x264633f6,0x2ca5b3a8,0xb113322f,
0xaa3eb3d9,0xb07232a7,0x33d9aa3e,0xb2a7b072,0x2646b3f6,0xb3a82ca5,0x3113322f,0x2f8bb30e,0x2e1fb364,0xb364ae1f,0xae1f3364,0x33642e1f,
0x3113b22f,0xaf8bb30e,0xb3f62646,0xaca533a8,0x32a7b072,0x2a3eb3d9,0xb072b2a7,0xb3d9aa3e,0x33a8aca5,0x322fb113,0x2f8bb30e,0x2646b3f6,
0x3400,0x3400,0x3400,0x3400,0xb3a8aca5,0xb22fb113,0xaf8bb30e,0xa646b3f6,0x3072b2a7,0x33d9aa3e,0x32a73072,0x2a3e33d9,
0x3113322f,0xaf8b330e,0xb3f6a646,0xaca5b3a8,0xb3642e1f,0xae1fb364,0x3364ae1f,0x2e1f3364,0xa646b3f6,0x33a82ca5,0xb113322f,0xaf8bb30e,
0x33d92a3e,0xb2a73072,0x2a3eb3d9,0x307232a7,0xaf8b330e,0x2646b3f6,0x2ca533a8,0xb113b22f,0xb1a8b1a8,0x31a831a8,0xb1a8b1a8,0x31a831a8,
0x330eaf8b,0xb3f62646,0x33a82ca5,0xb22fb113,0x2a3e33d9,0x3072b2a7,0xb3d92a3e,0x32a73072,0xb3f6a646,0x2ca533a8,0x322fb113,0xb30eaf8b,
0x2e1fb364,0xb364ae1f,0xae1f3364,0x33642e1f,0x322f3113,0x330eaf8b,0xa646b3f6,0xb3a8aca5,0xb2a73072,0xaa3e33d9,0x307232a7,0x33d92a3e,
0xaca5b3a8,0xb113b22f,0xb30eaf8b,0xb3f6a646,0x3400,0x3400,0x3400,0x3400,0x2ca5b3a8,0x3113b22f,0x330eaf8b,0x33f6a646,
0xb07232a7,0xb3d92a3e,0xb2a7b072,0xaa3eb3d9,0x322fb113,0x330e2f8b,0xa64633f6,0xb3a82ca5,0xb3642e1f,0xae1fb364,0x3364ae1f,0x2e1f3364,
0x33f6a646,0xaca533a8,0xb22fb113,0x330eaf8b,0xb3d9aa3e,0x32a7b072,0xaa3e33d9,0xb072b2a7,0x330e2f8b,0xb3f6a646,0x33a8aca5,0xb22f3113,
0xb1a8b1a8,0x31a831a8,0xb1a8b1a8,0x31a831a8,0x2f8b330e,0xa646b3f6,0xaca533a8,0x3113b22f,0xaa3eb3d9,0xb07232a7,0x33d9aa3e,0xb2a7b072,
0xa64633f6,0x33a8aca5,0xb113b22f,0xaf8b330e,0x2e1fb364,0xb364ae1f,0xae1f3364,0x33642e1f,0xb113322f,0x2f8b330e,0x33f6a646,0x2ca5b3a8,
0x32a7b072,0x2a3eb3d9,0xb072b2a7,0xb3d9aa3e,0xb3a82ca5,0xb22f3113,0xaf8b330e,0xa64633f6,0x3400,0x3400,0x3400,0x3400,
0x33a82ca5,0x322f3113,0x2f8b330e,0x264633f6,0x3072b2a7,0x33d9aa3e,0x32a73072,0x2a3e33d9,0xb113b22f,0x2f8bb30e,0x33f62646,0x2ca533a8,
0xb3642e1f,0xae1fb364,0x3364ae1f,0x2e1f3364,0x264633f6,0xb3a8aca5,0x3113b22f,0x2f8b330e,0x33d92a3e,0xb2a73072,0x2a3eb3d9,0x307232a7,
0x2f8bb30e,0xa64633f6,0xaca5b3a8,0x3113322f,0xb1a8b1a8,0x31a831a8,0xb1a8b1a8,0x31a831a8,0xb30e2f8b,0x33f6a646,0xb3a8aca5,0x322f3113,
0x2a3e33d9,0x3072b2a7,0xb3d92a3e,0x32a73072,0x33f62646,0xaca5b3a8,0xb22f3113,0x330e2f8b,0x2e1fb364,0xb364ae1f,0xae1f3364,0x33642e1f,
0xb22fb113,0xb30e2f8b,0x264633f6,0x33a82ca5,0xb2a73072,0xaa3e33d9,0x307232a7,0x33d92a3e,0x2ca533a8,0x3113322f,0x330e2f8b,0x33f62646,
0x34000000,0x34000000,0x34000000,0x34000000,0x33a82ca5,0x322f3113,0x2f8b330e,0x264633f6,0x32a73072,0x2a3e33d9,0xb07232a7,0xb3d92a3e,
0x3113322f,0xaf8b330e,0xb3f6a646,0xaca5b3a8,0x2e1f3364,0xb3642e1f,0xae1fb364,0x3364ae1f,0x264633f6,0xb3a8aca5,0x3113b22f,0x2f8b330e,
0xaa3e33d9,0xb072b2a7,0x33d92a3e,0xb2a73072,0xaf8b330e,0x2646b3f6,0x2ca533a8,0xb113b22f,0xb1a831a8,0x31a8b1a8,0xb1a831a8,0x31a8b1a8,
0xb30e2f8b,0x33f6a646,0xb3a8aca5,0x322f3113,0xb3d92a3e,0x32a73072,0xaa3eb3d9,0xb07232a7,0xb3f6a646,0x2ca533a8,0x322fb113,0xb30eaf8b,
0xb364ae1f,0xae1f3364,0x33642e1f,0x2e1fb364,0xb22fb113,0xb30e2f8b,0x264633f6,0x33a82ca5,0xb072b2a7,0xb3d9aa3e,0xb2a73072,0xaa3e33d9,
0xaca5b3a8,0xb113b22f,0xb30eaf8b,0xb3f6a646,0x34000000,0x34000000,0x34000000,0x34000000,0xaca533a8,0xb113322f,0xb30e2f8b,0xb3f62646,
0xb2a7b072,0xaa3eb3d9,0x3072b2a7,0x33d9aa3e,0x322fb113,0x330e2f8b,0xa64633f6,0xb3a82ca5,0x2e1f3364,0xb3642e1f,0xae1fb364,0x3364ae1f,
0xb3f62646,0x2ca5b3a8,0x322f3113,0xb30e2f8b,0x2a3eb3d9,0x307232a7,0xb3d9aa3e,0x32a7b072,0x330e2f8b,0xb3f6a646,0x33a8aca5,0xb22f3113,
0xb1a831a8,0x31a8b1a8,0xb1a831a8,0x31a8b1a8,0xaf8bb30e,0x264633f6,0x2ca5b3a8,0xb113322f,0x33d9aa3e,0xb2a7b072,0x2a3e33d9,0x3072b2a7,
0xa64633f6,0x33a8aca5,0xb113b22f,0xaf8b330e,0xb364ae1f,0xae1f3364,0x33642e1f,0x2e1fb364,0x3113b22f,0xaf8bb30e,0xb3f62646,0xaca533a8,
0x307232a7,0x33d92a3e,0x32a7b072,0x2a3eb3d9,0xb3a82ca5,0xb22f3113,0xaf8b330e,0xa64633f6,0x34000000,0x34000000,0x34000000,0x34000000,
0xb3a8aca5,0xb22fb113,0xaf8bb30e,0xa646b3f6,0x32a73072,0x2a3e33d9,0xb07232a7,0xb3d92a3e,0xb113b22f,0x2f8bb30e,0x33f62646,0x2ca533a8,
0x2e1f3364,0xb3642e1f,0xae1fb364,0x3364ae1f,0xa646b3f6,0x33a82ca5,0xb113322f,0xaf8bb30e,0xaa3e33d9,0xb072b2a7,0x33d92a3e,0xb2a73072,
0x2f8bb30e,0xa64633f6,0xaca5b3a8,0x3113322f,0xb1a831a8,0x31a8b1a8,0xb1a831a8,0x31a8b1a8,0x330eaf8b,0xb3f62646,0x33a82ca5,0xb22fb113,
0xb3d92a3e,0x32a73072,0xaa3eb3d9,0xb07232a7,0x33f62646,0xaca5b3a8,0xb22f3113,0x330e2f8b,0xb364ae1f,0xae1f3364,0x33642e1f,0x2e1fb364,
0x322f3113,0x330eaf8b,0xa646b3f6,0xb3a8aca5,0xb072b2a7,0xb3d9aa3e,0xb2a73072,0xaa3e33d9,0x2ca533a8,0x3113322f,0x330e2f8b,0x33f62646,
0x34000000,0x34000000,0x34000000,0x34000000,0x2ca5b3a8,0x3113b22f,0x330eaf8b,0x33f6a646,0xb2a7b072,0xaa3eb3d9,0x3072b2a7,0x33d9aa3e,
0xb22f3113,0xb30eaf8b,0x2646b3f6,0x33a8aca5,0x2e1f3364,0xb3642e1f,0xae1fb364,0x3364ae1f,0x33f6a646,0xaca533a8,0xb22fb113,0x330eaf8b,
0x2a3eb3d9,0x307232a7,0xb3d9aa3e,0x32a7b072,0xb30eaf8b,0x33f62646,0xb3a82ca5,0x322fb113,0xb1a831a8,0x31a8b1a8,0xb1a831a8,0x31a8b1a8,
0x2f8b330e,0xa646b3f6,0xaca533a8,0x3113b22f,0x33d9aa3e,0xb2a7b072,0x2a3e33d9,0x3072b2a7,0x2646b3f6,0xb3a82ca5,0x3113322f,0x2f8bb30e,
0xb364ae1f,0xae1f3364,0x33642e1f,0x2e1fb364,0xb113322f,0x2f8b330e,0x33f6a646,0x2ca5b3a8,0x307232a7,0x33d92a3e,0x32a7b072,0x2a3eb3d9,
0x33a8aca5,0x322fb113,0x2f8bb30e,0x2646b3f6,0x3400,0x2d643388,0x3113322f,0x32dd301d,0x3400,0x315f31ed,0x33f62646,0x326db0c4,
0x3400,0x333b2ed7,0x322fb113,0xabc6b3c3,0x3400,0x33fe2248,0x2646b3f6,0xb3eaa8b2,0x3400,0x3388ad64,0xb113b22f,0xb01d32dd,
0x3400,0x31edb15f,0xb3f6a646,0x30c4326d,0x3400,0x2ed7b33b,0xb22f3113,0x33c3abc6,0x3400,0x2248b3fe,0xa64633f6,0x28b2b3ea,
0x3400,0xad64b388,0x3113322f,0xb2ddb01d,0x3400,0xb15fb1ed,0x33f62646,0xb26d30c4,0x3400,0xb33baed7,0x322fb113,0x2bc633c3,
0x3400,0xb3fea248,0x2646b3f6,0x33ea28b2,0x3400,0xb3882d64,0xb113b22f,0x301db2dd,0x3400,0xb1ed315f,0xb3f6a646,0xb0c4b26d,
0x3400,0xaed7333b,0xb22f3113,0xb3c32bc6,0x3400,0xa24833fe,0xa64633f6,0xa8b233ea,0x33d92a3e,0x33eaa8b2,0x330eaf8b,0x315fb1ed,
0x2a3eb3d9,0xb01db2dd,0xb3a8aca5,0xb33b2ed7,0xb3d9aa3e,0xb0c4326d,0x2f8b330e,0x33fea248,0xaa3e33d9,0x33c32bc6,0x2ca5b3a8,0xb388ad64,
0x33d92a3e,0xa8b2b3ea,0xb30e2f8b,0x31ed315f,0x2a3eb3d9,0xb2dd301d,0x33a82ca5,0xaed7b33b,0xb3d9aa3e,0x326d30c4,0xaf8bb30e,0x224833fe,
0xaa3e33d9,0x2bc6b3c3,0xaca533a8,0x2d64b388,0x33d92a3e,0xb3ea28b2,0x330eaf8b,0xb15f31ed,0x2a3eb3d9,0x301d32dd,0xb3a8aca5,0x333baed7,
0xb3d9aa3e,0x30c4b26d,0x2f8b330e,0xb3fe2248,0xaa3e33d9,0xb3c3abc6,0x2ca5b3a8,0x33882d64,0x33d92a3e,0x28b233ea,0xb30e2f8b,0xb1edb15f,
0x2a3eb3d9,0x32ddb01d,0x33a82ca5,0x2ed7333b,0xb3d9aa3e,0xb26db0c4,0xaf8bb30e,0xa248b3fe,0xaa3e33d9,0xabc633c3,0xaca533a8,0xad643388,
0x2e1fb364,0x2248b3fe,0xaca5b3a8,0xb0c4b26d,0xae1f3364,0x2d643388,0x330e2f8b,0x33c3abc6,0x2e1fb364,0xb15fb1ed,0xb3a82ca5,0xa8b233ea,
0xae1f3364,0x333b2ed7,0x2f8bb30e,0xb2ddb01d,0x2e1fb364,0xb3fea248,0x2ca533a8,0x326db0c4,0xae1f3364,0x3388ad64,0xb30eaf8b,0x2bc633c3,
0x2e1fb364,0xb1ed315f,0x33a8aca5,0xb3eaa8b2,0xae1f3364,0x2ed7b33b,0xaf8b330e,0x301db2dd,0x2e1fb364,0xa24833fe,0xaca5b3a8,0x30c4326d,
0xae1f3364,0xad64b388,0x330e2f8b,0xb3c32bc6,0x2e1fb364,0x315f31ed,0xb3a82ca5,0x28b2b3ea,0xae1f3364,0xb33baed7,0x2f8bb30e,0x32dd301d,
0x2e1fb364,0x33fe2248,0x2ca533a8,0xb26d30c4,0xae1f3364,0xb3882d64,0xb30eaf8b,0xabc6b3c3,0x2e1fb364,0x31edb15f,0x33a8aca5,0x33ea28b2,
0xae1f3364,0xaed7333b,0xaf8b330e,0xb01d32dd,0xb2a7b072,0xb3c3abc6,0xb3f62646,0xb33b2ed7,0x3072b2a7,0xa8b2b3ea,0xb22fb113,0xb3fe2248,
0x32a73072,0x32ddb01d,0xa646b3f6,0xb388ad64,0xb07232a7,0x326d30c4,0x3113b22f,0xb1edb15f,0xb2a7b072,0xabc633c3,0x33f6a646,0xaed7b33b,
0x3072b2a7,0xb3ea28b2,0x322f3113,0xa248b3fe,0x32a73072,0xb01db2dd,0x264633f6,0x2d64b388,0xb07232a7,0x30c4b26d,0xb113322f,0x315fb1ed,
0xb2a7b072,0x33c32bc6,0xb3f62646,0x333baed7,0x3072b2a7,0x28b233ea,0xb22fb113,0x33fea248,0x32a73072,0xb2dd301d,0xa646b3f6,0x33882d64,
0xb07232a7,0xb26db0c4,0x3113b22f,0x31ed315f,0xb2a7b072,0x2bc6b3c3,0x33f6a646,0x2ed7333b,0x3072b2a7,0x33eaa8b2,0x322f3113,0x224833fe,
0x32a73072,0x301d32dd,0x264633f6,0xad643388,0xb07232a7,0xb0c4326d,0xb113322f,0xb15f31ed,0x34000000,0x3388ad64,0x322fb113,0x301db2dd,
0x34000000,0x31edb15f,0x2646b3f6,0xb0c4b26d,0x34000000,0x2ed7b33b,0xb113b22f,0xb3c32bc6,0x34000000,0x2248b3fe,0xb3f6a646,0xa8b233ea,
0x34000000,0xad64b388,0xb22f3113,0x32dd301d,0x34000000,0xb15fb1ed,0xa64633f6,0x326db0c4,0x34000000,0xb33baed7,0x3113322f,0xabc6b3c3,
0x34000000,0xb3fea248,0x33f62646,0xb3eaa8b2,0x34000000,0xb3882d64,0x322fb113,0xb01d32dd,0x34000000,0xb1ed315f,0x2646b3f6,0x30c4326d,
0x34000000,0xaed7333b,0xb113b22f,0x33c3abc6,0x34000000,0xa24833fe,0xb3f6a646,0x28b2b3ea,0x34000000,0x2d643388,0xb22f3113,0xb2ddb01d,
0x34000000,0x315f31ed,0xa64633f6,0xb26d30c4,0x34000000,0x333b2ed7,0x3113322f,0x2bc633c3,0x34000000,0x33fe2248,0x33f62646,0x33ea28b2,
0x2a3eb3d9,0xa8b2b3ea,0xaf8bb30e,0xb1edb15f,0xb3d9aa3e,0xb2dd301d,0xaca533a8,0x2ed7333b,0xaa3e33d9,0x326d30c4,0x330eaf8b,0xa248b3fe,
0x33d92a3e,0x2bc6b3c3,0xb3a8aca5,0xad643388,0x2a3eb3d9,0xb3ea28b2,0x2f8b330e,0x315fb1ed,0xb3d9aa3e,0x301d32dd,0x2ca5b3a8,0xb33b2ed7,
0xaa3e33d9,0x30c4b26d,0xb30e2f8b,0x33fea248,0x33d92a3e,0xb3c3abc6,0x33a82ca5,0xb388ad64,0x2a3eb3d9,0x28b233ea,0xaf8bb30e,0x31ed315f,
0xb3d9aa3e,0x32ddb01d,0xaca533a8,0xaed7b33b,0xaa3e33d9,0xb26db0c4,0x330eaf8b,0x224833fe,0x33d92a3e,0xabc633c3,0xb3a8aca5,0x2d64b388,
0x2a3eb3d9,0x33eaa8b2,0x2f8b330e,0xb15f31ed,0xb3d9aa3e,0xb01db2dd,0x2ca5b3a8,0x333baed7,0xaa3e33d9,0xb0c4326d,0xb30e2f8b,0xb3fe2248,
0x33d92a3e,0x33c32bc6,0x33a82ca5,0x33882d64,0xb364ae1f,0xb3fea248,0xb3a82ca5,0xb26d30c4,0x33642e1f,0x3388ad64,0x2f8bb30e,0xabc6b3c3,
0xb364ae1f,0xb1ed315f,0x2ca533a8,0x33ea28b2,0x33642e1f,0x2ed7b33b,0xb30eaf8b,0xb01d32dd,0xb364ae1f,0xa24833fe,0x33a8aca5,0xb0c4b26d,
0x33642e1f,0xad64b388,0xaf8b330e,0x33c3abc6,0xb364ae1f,0x315f31ed,0xaca5b3a8,0xa8b233ea,0x33642e1f,0xb33baed7,0x330e2f8b,0xb2ddb01d,
0xb364ae1f,0x33fe2248,0xb3a82ca5,0x326db0c4,0x33642e1f,0xb3882d64,0x2f8bb30e,0x2bc633c3,0xb364ae1f,0x31edb15f,0x2ca533a8,0xb3eaa8b2,
0x33642e1f,0xaed7333b,0xb30eaf8b,0x301db2dd,0xb364ae1f,0x2248b3fe,0x33a8aca5,0x30c4326d,0x33642e1f,0x2d643388,0xaf8b330e,0xb3c32bc6,
0xb364ae1f,0xb15fb1ed,0xaca5b3a8,0x28b2b3ea,0x33642e1f,0x333b2ed7,0x330e2f8b,0x32dd301d,0xb07232a7,0xabc633c3,0x264633f6,0x2ed7333b,
0xb2a7b072,0xb3ea28b2,0xb113322f,0x224833fe,0x3072b2a7,0xb01db2dd,0xb3f62646,0xad643388,0x32a73072,0x30c4b26d,0xb22fb113,0xb15f31ed,
0xb07232a7,0x33c32bc6,0xa646b3f6,0xb33b2ed7,0xb2a7b072,0x28b233ea,0x3113b22f,0xb3fe2248,0x3072b2a7,0xb2dd301d,0x33f6a646,0xb388ad64,
0x32a73072,0xb26db0c4,0x322f3113,0xb1edb15f,0xb07232a7,0x2bc6b3c3,0x264633f6,0xaed7b33b,0xb2a7b072,0x33eaa8b2,0xb113322f,0xa248b3fe,
0x3072b2a7,0x301d32dd,0xb3f62646,0x2d64b388,0x32a73072,0xb0c4326d,0xb22fb113,0x315fb1ed,0xb07232a7,0xb3c3abc6,0xa646b3f6,0x333baed7,
0xb2a7b072,0xa8b2b3ea,0x3113b22f,0x33fea248,0x3072b2a7,0x32ddb01d,0x33f6a646,0x33882d64,0x32a73072,0x326d30c4,0x322f3113,0x31ed315f,
0x3f800000,0x3f7ff0e3,0x3f7fc38f,0x3f7f7808,0x3f800000,0x3f7fba9e,0x3f7eea9d,0x3f7d906e,0x3f800000,0x3f7f5ce9,0x3f7d7474,0x3f7a4910,
0x3f800000,0x3f7ed7d4,0x3f7b61fc,0x3f75a67b,0x3f800000,0x3f7e2b72,0x3f78b47b,0x3f6faf1b,0x3f800000,0x3f7d57de,0x3f756d97,0x3f686b39,
0x3f800000,0x3f7c5d3a,0x3f718f57,0x3f5fe4e9,0x3f800000,0x3f7b3bab,0x3f6d1c1d,0x3f5627fe,0x3f800000,0x3f79f35e,0x3f6816a8,0x3f4b41fa,
0x3f800000,0x3f788486,0x3f628210,0x3f3f41fc,0x3f800000,0x3f76ef5b,0x3f5c61c7,0x3f3238aa,0x3f800000,0x3f75341d,0x3f55b993,0x3f24381a,
0x3f800000,0x3f73530e,0x3f4e8d90,0x3f1553b7,0x3f800000,0x3f714c7a,0x3f46e22a,0x3f05a02c,0x3f800000,0x3f6f20b0,0x3f3ebc1b,0x3eea6681,
0x3f800000,0x3f6cd007,0x3f36206c,0x3ec8477c,0x3f7f0e58,0x3f7e868b,0x3f7de0b1,0x3f7d1cdd,0x3f7baccd,0x3f7940c0,0x3f764d97,0x3f72d4eb,
0x3f75dec6,0x3f703b37,0x3f696591,0x3f61668a,0x3f6db293,0x3f6398ac,0x3f577026,0x3f495521,0x3f633c5a,0x3f5389c7,0x3f40d0da,0x3f2b561b,
0x3f5695e5,0x3f404c5c,0x3f26050a,0x3f084b92,0x3f47de65,0x3f2a2a80,0x3f07a136,0x3ec27b55,0x3f373a23,0x3f11796b,0x3ecc9b8b,0x3e5d4b1a,
0x3f24d225,0x3eed3063,0x3e8586ce,0x3d3c6dd5,0x3f10d3cd,0x3eb3dca5,0x3dee3876,0xbe007745,0x3ef6e0cb,0x3e6fa8b2,0xbcfb49ba,0xbe9620d2,
0x3ec9b953,0x3de7fa9a,0xbe354098,0xbee79a5d,0x3e9aa086,0xbc16cb58,0xbea395c5,0xbf19210f,0x3e541501,0xbe06b26e,0xbee900b7,0xbf39f2ac,
0x3de1bc2e,0xbe80f8c0,0xbf14b039,0xbf554aa4,0x3c490e90,0xbebca7af,0xbf31a81d,0xbf6a5ad9,0x3f7c3b28,0x3f7b3bab,0x3f7a1e84,0x3f78e3d6,
0x3f6ed89e,0x3f6a5ad9,0x3f655e0b,0x3f5fe4e9,0x3f584853,0x3f4e1689,0x3f42de29,0x3f36ad7f,0x3f396842,0x3f27ce61,0x3f14b039,0x3f003a06,
0x3f13682a,0x3ef2bd43,0x3ebb31a0,0x3e80f8c0,0x3ecf7bca,0x3e8a11f7,0x3e039502,0xbc7b514b,0x3e605c13,0x3d55880e,0xbdee3876,0xbe901c2c,
0x3cc90ab0,0xbe2bf804,0xbeb554ec,0xbf06f686,0xbe2f10a2,0xbec27b55,0xbf121eb0,0xbf3c17d9,0xbeb8442a,0xbf12c39a,0xbf40d0da,0xbf6398ac,
0xbf08f59b,0xbf3d27b8,0xbf63f473,0xbf7a9c59,0xbf3085bb,0xbf5e5aa6,0xbf796e4e,0xbf7f7808,0xbf514d3d,0xbf74bf62,0xbf7ff4e6,0xbf71d19f,
0xbf6a09a7,0xbf7f3f54,0xbf772417,0xbf52a649,0xbf79c79d,0xbf7d57de,0xbf5f8327,0xbf24381a,0xbf7fec43,0xbf6f20b0,0xbf3a7ca4,0xbed3c867,
0x3f778bc5,0x3f76167a,0x3f748422,0x3f72d4eb,0x3f59f26a,0x3f5389c7,0x3f4cae79,0x3f456439,0x3f299415,0x3f1ba299,0x3f0cead0,0x3efafef7,
0x3ed53641,0x3ea80b1f,0x3e72b651,0x3e1324ca,0x3e09cf86,0x3c7b514b,0xbdd53db9,0xbe636c83,0xbe22abb6,0xbe9920fe,0xbeddbe79,0xbf0ee0db,
0xbee0924f,0xbf1553b7,0xbf36206c,0xbf51c0c2,0xbf2e3bde,0xbf4f0417,0xbf6816a8,0xbf788486,0xbf5d2d53,0xbf73ced9,0xbf7eea9d,0xbf7dfa38,
0xbf791298,0xbf7ffd39,0xbf77f110,0xbf61668a,0xbf7f84ab,0xbf725470,0xbf53fac3,0xbf269de3,0xbf6ff573,0xbf4c3578,0xbf173c07,0xbeab0201,
0xbf4bbbf8,0xbf11796b,0xbe919ddd,0x3ce22a7a,0xbf15f6d9,0xbe901c2c,0x3d621469,0x3ec5625c,0xbea68f12,0x3d0a342f,0x3ec6d529,0x3f311722,
0xbd16c32c,0x3eb0eacb,0x3f2d1469,0x3f67c187,0,0xbcafea69,0xbd2fe007,0xbd83db0a,0,0xbd3c6dd5,0xbdbc3ac3,0xbe0cec4a,
0,0xbd9064b4,0xbe1008b7,0xbe57278f,0,0xbdc27c39,0xbe419b37,0xbe901c2c,0,0xbdf475c0,0xbe72b651,0xbeb3dca5,
0,0xbe1324ca,0xbe919ddd,0xbed6a399,0,0xbe2bf804,0xbea986c4,0xbef840c8,0,0xbe44b0b9,0xbec1071e,0xbf0c42c9,
0,0xbe5d4b1a,0xbed8106b,0xbf1ba299,0,0xbe75c35a,0xbeee9479,0xbf2a2a80,0,0xbe870ada,0xbf0242b1,0xbf37c655,
0,0xbe931f35,0xbf0cead0,0xbf446337,0,0xbe9f1cdf,0xbf173c07,0xbf4fefa8,0,0xbeab0201,0xbf212ff9,0xbf5a5ba0,
0,0xbeb6ccc3,0xbf2ac082,0xbf6398ac,0,0xbec27b55,0xbf33e7bc,0xbf6b99fb,0xbdafb680,0xbddb7d37,0xbe039502,0xbe195be0,
0xbe3b6ecf,0xbe698bba,0xbe8b9507,0xbea21887,0xbe8e9a22,0xbeb0eacb,0xbed25a09,0xbef2bd43,0xbebe1d4a,0xbeea6681,0xbf0a48ad,0xbf1e1e22,
0xbeebcbbb,0xbf102dd5,0xbf286605,0xbf3e35c5,0xbf0b9a6b,0xbf28fd41,0xbf42de29,0xbf58b3a1,0xbf1ff6cb,0xbf3f41fc,0xbf591e6a,0xbf6cd007,
0xbf32c8c9,0xbf52a649,0xbf6aab7b,0xbf79f35e,0xbf43e200,0xbf62df7b,0xbf772417,0xbf7fba9e,0xbf531849,0xbf6faf1b,0xbf7e4323,0xbf7dfa38,
0xbf604621,0xbf78e3d6,0xbf7fe129,0xbf74bf62,0xbf6b4b0c,0xbf7e5a38,0xbf7bf531,0xbf644fac,0xbf740bdd,0xbf7ffd39,0xbf7294f8,0xbf4d26fd,
0xbf7a7302,0xbf7dc68c,0xbf63f473,0xbf2ff3e6,0xbf7e70b0,0xbf77beb7,0xbf5064af,0xbf0d9281,0xbf7ffb11,0xbf6dfcf2,0xbf385216,0xbece0bea,
0xbe2f10a2,0xbe44b0b9,0xbe5a3997,0xbe6fa8b2,0xbeb8442a,0xbece0bea,0xbee363fa,0xbef840c8,0xbf08f59b,0xbf17de12,0xbf26050a,0xbf33587a,
0xbf3085bb,0xbf4154e1,0xbf5064af,0xbf5d924d,0xbf514d3d,0xbf61668a,0xbf6e46be,0xbf77beb7,0xbf6a09a7,0xbf76841b,0xbf7de0b1,0xbf7ff84a,
0xbf79c79d,0xbf7fa6e3,0xbf7e4323,0xbf75a67b,0xbf7fec43,0xbf7c5d3a,0xbf6f6830,0xbf5988ad,0xbf7c3b28,0xbf6cd007,0xbf5233c6,0xbf2da859,
0xbf6ed89e,0xbf51c0c2,0xbf286605,0xbeea6681,0xbf584853,0xbf2c800f,0xbee900b7,0xbe5101f1,0xbf396842,0xbefdbabb,0xbe667c66,0x3d83db0a,
0xbf13682a,0xbe9620d2,0x3c96c9b6,0x3ea80b1f,0xbecf7bca,0xbd9cecf9,0x3e8586ce,0x3f11796b,0xbe605c13,0x3e1324ca,0x3ef9a02d,0x3f446337,
0xbcc90ab0,0x3eb6ccc3,0x3f2f61a5,0x3f6912ae,0xbe827dc0,0xbe8d17c0,0xbe97a117,0xbea21887,0xbf064b82,0xbf102dd5,0xbf19c200,0xbf2302d3,
0xbf3fc767,0xbf4b41fa,0xbf55b993,0xbf5f20db,0xbf68bf3c,0xbf71d19f,0xbf78b47b,0xbf7d57de,0xbf7dabcc,0xbf7ff84a,0xbf7e9bc9,0xbf799b43,
0xbf7cbfc9,0xbf74484b,0xbf66becc,0xbf546b3b,0xbf660f88,0xbf4fefa8,0xbf33e7bc,0xbf12c39a,0xbf3b8f3b,0xbf16999f,0xbed8106b,0xbe75c35a,
0xbf00e7e4,0xbe9c1faf,0xbdbc3ac3,0x3e007745,0xbe6c9a7f,0x3c16cb58,0x3e7ee6e1,0x3ef2bd43,0x3d7b2b74,0x3ea5129f,0x3f0f8784,0x3f425b89,
0x3eb263ef,0x3f1a6293,0x3f4e8d90,0x3f714c7a,0x3f1b02c6,0x3f52a649,0x3f756d97,0x3f7fe705,0x3f4f7a1f,0x3f75a67b,0x3f7f9c18,0x3f6c3624,
0x3f721352,0x3f7fdaaf,0x3f6be858,0x3f38dd65,0x3f7fd397,0x3f703b37,0x3f3ca003,0x3ed97cb9,0x3f800000,0x3f800000,0x3f800000,0x3f800000,
0x3f7ff0e3,0x3f7fba9e,0x3f7f5ce9,0x3f7ed7d4,0x3f7fc38f,0x3f7eea9d,0x3f7d7474,0x3f7b61fc,0x3f7f7808,0x3f7d906e,0x3f7a4910,0x3f75a67b,
0x3f7f0e58,0x3f7baccd,0x3f75dec6,0x3f6db293,0x3f7e868b,0x3f7940c0,0x3f703b37,0x3f6398ac,0x3f7de0b1,0x3f764d97,0x3f696591,0x3f577026,
0x3f7d1cdd,0x3f72d4eb,0x3f61668a,0x3f495521,0x3f7c3b28,0x3f6ed89e,0x3f584853,0x3f396842,0x3f7b3bab,0x3f6a5ad9,0x3f4e1689,0x3f27ce61,
0x3f7a1e84,0x3f655e0b,0x3f42de29,0x3f14b039,0x3f78e3d6,0x3f5fe4e9,0x3f36ad7f,0x3f003a06,0x3f778bc5,0x3f59f26a,0x3f299415,0x3ed53641,
0x3f76167a,0x3f5389c7,0x3f1ba299,0x3ea80b1f,0x3f748422,0x3f4cae79,0x3f0cead0,0x3e72b651,0x3f72d4eb,0x3f456439,0x3efafef7,0x3e1324ca,
0x3f800000,0x3f800000,0x3f800000,0x3f800000,0x3f7e2b72,0x3f7d57de,0x3f7c5d3a,0x3f7b3bab,0x3f78b47b,0x3f756d97,0x3f718f57,0x3f6d1c1d,
0x3f6faf1b,0x3f686b39,0x3f5fe4e9,0x3f5627fe,0x3f633c5a,0x3f5695e5,0x3f47de65,0x3f373a23,0x3f5389c7,0x3f404c5c,0x3f2a2a80,0x3f11796b,
0x3f40d0da,0x3f26050a,0x3f07a136,0x3ecc9b8b,0x3f2b561b,0x3f084b92,0x3ec27b55,0x3e5d4b1a,0x3f13682a,0x3ecf7bca,0x3e605c13,0x3cc90ab0,
0x3ef2bd43,0x3e8a11f7,0x3d55880e,0xbe2bf804,0x3ebb31a0,0x3e039502,0xbdee3876,0xbeb554ec,0x3e80f8c0,0xbc7b514b,0xbe901c2c,0xbf06f686,
0x3e09cf86,0xbe22abb6,0xbee0924f,0xbf2e3bde,0x3c7b514b,0xbe9920fe,0xbf1553b7,0xbf4f0417,0xbdd53db9,0xbeddbe79,0xbf36206c,0xbf6816a8,
0xbe636c83,0xbf0ee0db,0xbf51c0c2,0xbf788486,0x3f800000,0x3f800000,0x3f800000,0x3f800000,0x3f79f35e,0x3f788486,0x3f76ef5b,0x3f75341d,
0x3f6816a8,0x3f628210,0x3f5c61c7,0x3f55b993,0x3f4b41fa,0x3f3f41fc,0x3f3238aa,0x3f24381a,0x3f24d225,0x3f10d3cd,0x3ef6e0cb,0x3ec9b953,
0x3eed3063,0x3eb3dca5,0x3e6fa8b2,0x3de7fa9a,0x3e8586ce,0x3dee3876,0xbcfb49ba,0xbe354098,0x3d3c6dd5,0xbe007745,0xbe9620d2,0xbee79a5d,
0xbe2f10a2,0xbeb8442a,0xbf08f59b,0xbf3085bb,0xbec27b55,0xbf12c39a,0xbf3d27b8,0xbf5e5aa6,0xbf121eb0,0xbf40d0da,0xbf63f473,0xbf796e4e,
0xbf3c17d9,0xbf6398ac,0xbf7a9c59,0xbf7f7808,0xbf5d2d53,0xbf791298,0xbf7f84ab,0xbf6ff573,0xbf73ced9,0xbf7ffd39,0xbf725470,0xbf4c3578,
0xbf7eea9d,0xbf77f110,0xbf53fac3,0xbf173c07,0xbf7dfa38,0xbf61668a,0xbf269de3,0xbeab0201,0x3f800000,0x3f800000,0x3f800000,0x3f800000,
0x3f73530e,0x3f714c7a,0x3f6f20b0,0x3f6cd007,0x3f4e8d90,0x3f46e22a,0x3f3ebc1b,0x3f36206c,0x3f1553b7,0x3f05a02c,0x3eea6681,0x3ec8477c,
0x3e9aa086,0x3e541501,0x3de1bc2e,0x3c490e90,0xbc16cb58,0xbe06b26e,0xbe80f8c0,0xbebca7af,0xbea395c5,0xbee900b7,0xbf14b039,0xbf31a81d,
0xbf19210f,0xbf39f2ac,0xbf554aa4,0xbf6a5ad9,0xbf514d3d,0xbf6a09a7,0xbf79c79d,0xbf7fec43,0xbf74bf62,0xbf7f3f54,0xbf7d57de,0xbf6f20b0,
0xbf7ff4e6,0xbf772417,0xbf5f8327,0xbf3a7ca4,0xbf71d19f,0xbf52a649,0xbf24381a,0xbed3c867,0xbf4bbbf8,0xbf15f6d9,0xbea68f12,0xbd16c32c,
0xbf11796b,0xbe901c2c,0x3d0a342f,0x3eb0eacb,0xbe919ddd,0x3d621469,0x3ec6d529,0x3f2d1469,0x3ce22a7a,0x3ec5625c,0x3f311722,0x3f67c187,
0,0,0,0,0x3cafea69,0x3d3c6dd5,0x3d9064b4,0x3dc27c39,0x3d2fe007,0x3dbc3ac3,0x3e1008b7,0x3e419b37,
0x3d83db0a,0x3e0cec4a,0x3e57278f,0x3e901c2c,0x3dafb680,0x3e3b6ecf,0x3e8e9a22,0x3ebe1d4a,0x3ddb7d37,0x3e698bba,0x3eb0eacb,0x3eea6681,
0x3e039502,0x3e8b9507,0x3ed25a09,0x3f0a48ad,0x3e195be0,0x3ea21887,0x3ef2bd43,0x3f1e1e22,0x3e2f10a2,0x3eb8442a,0x3f08f59b,0x3f3085bb,
0x3e44b0b9,0x3ece0bea,0x3f17de12,0x3f4154e1,0x3e5a3997,0x3ee363fa,0x3f26050a,0x3f5064af,0x3e6fa8b2,0x3ef840c8,0x3f33587a,0x3f5d924d,
0x3e827dc0,0x3f064b82,0x3f3fc767,0x3f68bf3c,0x3e8d17c0,0x3f102dd5,0x3f4b41fa,0x3f71d19f,0x3e97a117,0x3f19c200,0x3f55b993,0x3f78b47b,
0x3ea21887,0x3f2302d3,0x3f5f20db,0x3f7d57de,0,0,0,0,0x3df475c0,0x3e1324ca,0x3e2bf804,0x3e44b0b9,
0x3e72b651,0x3e919ddd,0x3ea986c4,0x3ec1071e,0x3eb3dca5,0x3ed6a399,0x3ef840c8,0x3f0c42c9,0x3eebcbbb,0x3f0b9a6b,0x3f1ff6cb,0x3f32c8c9,
0x3f102dd5,0x3f28fd41,0x3f3f41fc,0x3f52a649,0x3f286605,0x3f42de29,0x3f591e6a,0x3f6aab7b,0x3f3e35c5,0x3f58b3a1,0x3f6cd007,0x3f79f35e,
0x3f514d3d,0x3f6a09a7,0x3f79c79d,0x3f7fec43,0x3f61668a,0x3f76841b,0x3f7fa6e3,0x3f7c5d3a,0x3f6e46be,0x3f7de0b1,0x3f7e4323,0x3f6f6830,
0x3f77beb7,0x3f7ff84a,0x3f75a67b,0x3f5988ad,0x3f7dabcc,0x3f7cbfc9,0x3f660f88,0x3f3b8f3b,0x3f7ff84a,0x3f74484b,0x3f4fefa8,0x3f16999f,
0x3f7e9bc9,0x3f66becc,0x3f33e7bc,0x3ed8106b,0x3f799b43,0x3f546b3b,0x3f12c39a,0x3e75c35a,0,0,0,0,
0x3e5d4b1a,0x3e75c35a,0x3e870ada,0x3e931f35,0x3ed8106b,0x3eee9479,0x3f0242b1,0x3f0cead0,0x3f1ba299,0x3f2a2a80,0x3f37c655,0x3f446337,
0x3f43e200,0x3f531849,0x3f604621,0x3f6b4b0c,0x3f62df7b,0x3f6faf1b,0x3f78e3d6,0x3f7e5a38,0x3f772417,0x3f7e4323,0x3f7fe129,0x3f7bf531,
0x3f7fba9e,0x3f7dfa38,0x3f74bf62,0x3f644fac,0x3f7c3b28,0x3f6ed89e,0x3f584853,0x3f396842,0x3f6cd007,0x3f51c0c2,0x3f2c800f,0x3efdbabb,
0x3f5233c6,0x3f286605,0x3ee900b7,0x3e667c66,0x3f2da859,0x3eea6681,0x3e5101f1,0xbd83db0a,0x3f00e7e4,0x3e6c9a7f,0xbd7b2b74,0xbeb263ef,
0x3e9c1faf,0xbc16cb58,0xbea5129f,0xbf1a6293,0x3dbc3ac3,0xbe7ee6e1,0xbf0f8784,0xbf4e8d90,0xbe007745,0xbef2bd43,0xbf425b89,0xbf714c7a,
0,0,0,0,0x3e9f1cdf,0x3eab0201,0x3eb6ccc3,0x3ec27b55,0x3f173c07,0x3f212ff9,0x3f2ac082,0x3f33e7bc,
0x3f4fefa8,0x3f5a5ba0,0x3f6398ac,0x3f6b99fb,0x3f740bdd,0x3f7a7302,0x3f7e70b0,0x3f7ffb11,0x3f7ffd39,0x3f7dc68c,0x3f77beb7,0x3f6dfcf2,
0x3f7294f8,0x3f63f473,0x3f5064af,0x3f385216,0x3f4d26fd,0x3f2ff3e6,0x3f0d9281,0x3ece0bea,0x3f13682a,0x3ecf7bca,0x3e605c13,0x3cc90ab0,
0x3e9620d2,0x3d9cecf9,0xbe1324ca,0xbeb6ccc3,0xbc96c9b6,0xbe8586ce,0xbef9a02d,0xbf2f61a5,0xbea80b1f,0xbf11796b,0xbf446337,0xbf6912ae,
0xbf1b02c6,0xbf4f7a1f,0xbf721352,0xbf7fd397,0xbf52a649,0xbf75a67b,0xbf7fdaaf,0xbf703b37,0xbf756d97,0xbf7f9c18,0xbf6be858,0xbf3ca003,
0xbf7fe705,0xbf6c3624,0xbf38dd65,0xbed97cb9,0x3400,0x3400,0x3400,0x3400,0xad643388,0xb15f31ed,0xb33b2ed7,0xb3fe2248,
0xb113322f,0xb3f62646,0xb22fb113,0xa646b3f6,0xb2dd301d,0xb26db0c4,0x2bc6b3c3,0x33eaa8b2,0xb3d92a3e,0xaa3eb3d9,0x33d9aa3e,0x2a3e33d9,
0xb3eaa8b2,0x301db2dd,0x30c4326d,0xb3c32bc6,0xb30eaf8b,0x33a8aca5,0xaf8b330e,0xaca5b3a8,0xb15fb1ed,0x333b2ed7,0xb3fea248,0x3388ad64,
0xae1fb364,0x2e1f3364,0xae1fb364,0x2e1f3364,0xa248b3fe,0xad643388,0x315fb1ed,0xb33b2ed7,0x2ca5b3a8,0xb30e2f8b,0x33a82ca5,0xaf8bb30e,
0x30c4b26d,0xb3c3abc6,0x28b233ea,0x32ddb01d,0x32a7b072,0xb072b2a7,0xb2a73072,0x307232a7,0x33c3abc6,0x28b2b3ea,0xb2ddb01d,0xb26d30c4,
0x33f62646,0x322fb113,0x2646b3f6,0xb113b22f,0x333b2ed7,0x33fe2248,0x3388ad64,0x31edb15f,0x3400,0x3400,0x3400,0x3400,
0xb388ad64,0xb1edb15f,0xaed7b33b,0xa248b3fe,0x3113b22f,0x33f6a646,0x322f3113,0x264633f6,0x301d32dd,0xb0c4326d,0xb3c3abc6,0xa8b2b3ea,
0xb3d92a3e,0xaa3eb3d9,0x33d9aa3e,0x2a3e33d9,0x28b2b3ea,0x32dd301d,0xb26d30c4,0xabc6b3c3,0x330e2f8b,0xb3a82ca5,0x2f8bb30e,0x2ca533a8,
0xb1ed315f,0x2ed7b33b,0xa24833fe,0xad64b388,0xae1fb364,0x2e1f3364,0xae1fb364,0x2e1f3364,0x33fea248,0xb388ad64,0x31ed315f,0xaed7b33b,
0xaca533a8,0x330eaf8b,0xb3a8aca5,0x2f8b330e,0xb26db0c4,0xabc633c3,0x33eaa8b2,0xb01db2dd,0x32a7b072,0xb072b2a7,0xb2a73072,0x307232a7,
0x2bc633c3,0x33ea28b2,0x301db2dd,0xb0c4b26d,0xb3f6a646,0xb22f3113,0xa64633f6,0x3113322f,0x2ed7b33b,0x2248b3fe,0xad64b388,0xb15fb1ed,
0x3400,0x3400,0x3400,0x3400,0x2d64b388,0x315fb1ed,0x333baed7,0x33fea248,0xb113322f,0xb3f62646,0xb22fb113,0xa646b3f6,
0x32ddb01d,0x326d30c4,0xabc633c3,0xb3ea28b2,0xb3d92a3e,0xaa3eb3d9,0x33d9aa3e,0x2a3e33d9,0x33ea28b2,0xb01d32dd,0xb0c4b26d,0x33c3abc6,
0xb30eaf8b,0x33a8aca5,0xaf8b330e,0xaca5b3a8,0x315f31ed,0xb33baed7,0x33fe2248,0xb3882d64,0xae1fb364,0x2e1f3364,0xae1fb364,0x2e1f3364,
0x224833fe,0x2d64b388,0xb15f31ed,0x333baed7,0x2ca5b3a8,0xb30e2f8b,0x33a82ca5,0xaf8bb30e,0xb0c4326d,0x33c32bc6,0xa8b2b3ea,0xb2dd301d,
0x32a7b072,0xb072b2a7,0xb2a73072,0x307232a7,0xb3c32bc6,0xa8b233ea,0x32dd301d,0x326db0c4,0x33f62646,0x322fb113,0x2646b3f6,0xb113b22f,
0xb33baed7,0xb3fea248,0xb3882d64,0xb1ed315f,0x3400,0x3400,0x3400,0x3400,0x33882d64,0x31ed315f,0x2ed7333b,0x224833fe,
0x3113b22f,0x33f6a646,0x322f3113,0x264633f6,0xb01db2dd,0x30c4b26d,0x33c32bc6,0x28b233ea,0xb3d92a3e,0xaa3eb3d9,0x33d9aa3e,0x2a3e33d9,
0xa8b233ea,0xb2ddb01d,0x326db0c4,0x2bc633c3,0x330e2f8b,0xb3a82ca5,0x2f8bb30e,0x2ca533a8,0x31edb15f,0xaed7333b,0x2248b3fe,0x2d643388,
0xae1fb364,0x2e1f3364,0xae1fb364,0x2e1f3364,0xb3fe2248,0x33882d64,0xb1edb15f,0x2ed7333b,0xaca533a8,0x330eaf8b,0xb3a8aca5,0x2f8b330e,
0x326d30c4,0x2bc6b3c3,0xb3ea28b2,0x301d32dd,0x32a7b072,0xb072b2a7,0xb2a73072,0x307232a7,0xabc6b3c3,0xb3eaa8b2,0xb01d32dd,0x30c4326d,
0xb3f6a646,0xb22f3113,0xa64633f6,0x3113322f,0xaed7333b,0xa24833fe,0x2d643388,0x315f31ed,0x34000000,0x34000000,0x34000000,0x34000000,
0x33882d64,0x31ed315f,0x2ed7333b,0x224833fe,0x322f3113,0x264633f6,0xb113322f,0xb3f62646,0x301d32dd,0xb0c4326d,0xb3c3abc6,0xa8b2b3ea,
0x2a3e33d9,0xb3d92a3e,0xaa3eb3d9,0x33d9aa3e,0xa8b233ea,0xb2ddb01d,0x326db0c4,0x2bc633c3,0xaf8b330e,0xaca5b3a8,0x330e2f8b,0xb3a82ca5,
0xb1ed315f,0x2ed7b33b,0xa24833fe,0xad64b388,0xb3642e1f,0x3364ae1f,0xb3642e1f,0x3364ae1f,0xb3fe2248,0x33882d64,0xb1edb15f,0x2ed7333b,
0xb3a8aca5,0x2f8b330e,0x2ca5b3a8,0xb30e2f8b,0xb26db0c4,0xabc633c3,0x33eaa8b2,0xb01db2dd,0xb072b2a7,0xb2a73072,0x307232a7,0x32a7b072,
0xabc6b3c3,0xb3eaa8b2,0xb01d32dd,0x30c4326d,0x2646b3f6,0xb113b22f,0xb3f6a646,0xb22f3113,0x2ed7b33b,0x2248b3fe,0xad64b388,0xb15fb1ed,
0x34000000,0x34000000,0x34000000,0x34000000,0xad643388,0xb15f31ed,0xb33b2ed7,0xb3fe2248,0xb22fb113,0xa646b3f6,0x3113b22f,0x33f6a646,
0x32ddb01d,0x326d30c4,0xabc633c3,0xb3ea28b2,0x2a3e33d9,0xb3d92a3e,0xaa3eb3d9,0x33d9aa3e,0xb3eaa8b2,0x301db2dd,0x30c4326d,0xb3c32bc6,
0x2f8bb30e,0x2ca533a8,0xb30eaf8b,0x33a8aca5,0x315f31ed,0xb33baed7,0x33fe2248,0xb3882d64,0xb3642e1f,0x3364ae1f,0xb3642e1f,0x3364ae1f,
0xa248b3fe,0xad643388,0x315fb1ed,0xb33b2ed7,0x33a82ca5,0xaf8bb30e,0xaca533a8,0x330eaf8b,0xb0c4326d,0x33c32bc6,0xa8b2b3ea,0xb2dd301d,
0xb072b2a7,0xb2a73072,0x307232a7,0x32a7b072,0x33c3abc6,0x28b2b3ea,0xb2ddb01d,0xb26d30c4,0xa64633f6,0x3113322f,0x33f62646,0x322fb113,
0xb33baed7,0xb3fea248,0xb3882d64,0xb1ed315f,0x34000000,0x34000000,0x34000000,0x34000000,0xb388ad64,0xb1edb15f,0xaed7b33b,0xa248b3fe,
0x322f3113,0x264633f6,0xb113322f,0xb3f62646,0xb01db2dd,0x30c4b26d,0x33c32bc6,0x28b233ea,0x2a3e33d9,0xb3d92a3e,0xaa3eb3d9,0x33d9aa3e,
0x28b2b3ea,0x32dd301d,0xb26d30c4,0xabc6b3c3,0xaf8b330e,0xaca5b3a8,0x330e2f8b,0xb3a82ca5,0x31edb15f,0xaed7333b,0x2248b3fe,0x2d643388,
0xb3642e1f,0x3364ae1f,0xb3642e1f,0x3364ae1f,0x33fea248,0xb388ad64,0x31ed315f,0xaed7b33b,0xb3a8aca5,0x2f8b330e,0x2ca5b3a8,0xb30e2f8b,
0x326d30c4,0x2bc6b3c3,0xb3ea28b2,0x301d32dd,0xb072b2a7,0xb2a73072,0x307232a7,0x32a7b072,0x2bc633c3,0x33ea28b2,0x301db2dd,0xb0c4b26d,
0x2646b3f6,0xb113b22f,0xb3f6a646,0xb22f3113,0xaed7333b,0xa24833fe,0x2d643388,0x315f31ed,0x34000000,0x34000000,0x34000000,0x34000000,
0x2d64b388,0x315fb1ed,0x333baed7,0x33fea248,0xb22fb113,0xa646b3f6,0x3113b22f,0x33f6a646,0xb2dd301d,0xb26db0c4,0x2bc6b3c3,0x33eaa8b2,
0x2a3e33d9,0xb3d92a3e,0xaa3eb3d9,0x33d9aa3e,0x33ea28b2,0xb01d32dd,0xb0c4b26d,0x33c3abc6,0x2f8bb30e,0x2ca533a8,0xb30eaf8b,0x33a8aca5,
0xb15fb1ed,0x333b2ed7,0xb3fea248,0x3388ad64,0xb3642e1f,0x3364ae1f,0xb3642e1f,0x3364ae1f,0x224833fe,0x2d64b388,0xb15f31ed,0x333baed7,
0x33a82ca5,0xaf8bb30e,0xaca533a8,0x330eaf8b,0x30c4b26d,0xb3c3abc6,0x28b233ea,0x32ddb01d,0xb072b2a7,0xb2a73072,0x307232a7,0x32a7b072,
0xb3c32bc6,0xa8b233ea,0x32dd301d,0x326db0c4,0xa64633f6,0x3113322f,0x33f62646,0x322fb113,0x333b2ed7,0x33fe2248,0x3388ad64,0x31edb15f,
0x7280,0x9380,0x7e80,0x9674,0x7284,0x9384,0x7e84,0x9670,0x7288,0x9388,0x7e88,0x966c,
0x728c,0x938c,0x7e8c,0x9668,0x7290,0x9390,0x7e90,0x9664,0x7294,0x9394,0x7e94,0x9660,
0x7298,0x9398,0x7e98,0x965c,0x729c,0x939c,0x7e9c,0x9658,0x72a0,0x93a0,0x7ea0,0x9654,
0x72a4,0x93a4,0x7ea4,0x9650,0x72b0,0x93b0,0x7eb0,0x9644,0x72b4,0x93b4,0x7eb4,0x9640,
0x72b8,0x93b8,0x7eb8,0x963c,0x72bc,0x93bc,0x7ebc,0x9638,0x72c0,0x93c0,0x7ec0,0x9634,
0x72c4,0x93c4,0x7ec4,0x9630,0x72c8,0x93c8,0x7ec8,0x962c,0x72cc,0x93cc,0x7ecc,0x9628,
0x72d0,0x93d0,0x7ed0,0x9624,0x72d4,0x93d4,0x7ed4,0x9620,0x72e0,0x93e0,0x7ee0,0x9614,
0x72e4,0x93e4,0x7ee4,0x9610,0x72e8,0x93e8,0x7ee8,0x960c,0x72ec,0x93ec,0x7eec,0x9608,
0x72f0,0x93f0,0x7ef0,0x9604,0x72f4,0x93f4,0x7ef4,0x9600,0x72f8,0x93f8,0x7ef8,0x95fc,
0x72fc,0x93fc,0x7efc,0x95f8,0x7300,0x9400,0x7f00,0x95f4,0x7304,0x9404,0x7f04,0x95f0,
0x7310,0x9410,0x7f10,0x95e4,0x7314,0x9414,0x7f14,0x95e0,0x7318,0x9418,0x7f18,0x95dc,
0x731c,0x941c,0x7f1c,0x95d8,0x7320,0x9420,0x7f20,0x95d4,0x7324,0x9424,0x7f24,0x95d0,
0x7328,0x9428,0x7f28,0x95cc,0x732c,0x942c,0x7f2c,0x95c8,0x7330,0x9430,0x7f30,0x95c4,
0x7334,0x9434,0x7f34,0x95c0,0x7340,0x9440,0x7f40,0x95b4,0x7344,0x9444,0x7f44,0x95b0,
0x7348,0x9448,0x7f48,0x95ac,0x734c,0x944c,0x7f4c,0x95a8,0x7350,0x9450,0x7f50,0x95a4,
0x7354,0x9454,0x7f54,0x95a0,0x7358,0x9458,0x7f58,0x959c,0x735c,0x945c,0x7f5c,0x9598,
0x7360,0x9460,0x7f60,0x9594,0x7364,0x9464,0x7f64,0x9590,0x7370,0x9470,0x7f70,0x9584,
0x7374,0x9474,0x7f74,0x9580,0x7378,0x9478,0x7f78,0x957c,0x737c,0x947c,0x7f7c,0x9578,
0x7380,0x9480,0x7f80,0x9574,0x7384,0x9484,0x7f84,0x9570,0x7388,0x9488,0x7f88,0x956c,
0x738c,0x948c,0x7f8c,0x9568,0x7390,0x9490,0x7f90,0x9564,0x7394,0x9494,0x7f94,0x9560,
0x73a0,0x94a0,0x7fa0,0x9554,0x73a4,0x94a4,0x7fa4,0x9550,0x73a8,0x94a8,0x7fa8,0x954c,
0x73ac,0x94ac,0x7fac,0x9548,0x73b0,0x94b0,0x7fb0,0x9544,0x73b4,0x94b4,0x7fb4,0x9540,
0x73b8,0x94b8,0x7fb8,0x953c,0x73bc,0x94bc,0x7fbc,0x9538,0x73c0,0x94c0,0x7fc0,0x9534,
0x73c4,0x94c4,0x7fc4,0x9530,0x73d0,0x94d0,0x7fd0,0x9524,0x73d4,0x94d4,0x7fd4,0x9520,
0x73d8,0x94d8,0x7fd8,0x951c,0x73dc,0x94dc,0x7fdc,0x9518,0x73e0,0x94e0,0x7fe0,0x9514,
0x73e4,0x94e4,0x7fe4,0x9510,0x73e8,0x94e8,0x7fe8,0x950c,0x73ec,0x94ec,0x7fec,0x9508,
0x73f0,0x94f0,0x7ff0,0x9504,0x73f4,0x94f4,0x7ff4,0x9500,0x7580,0x8a80,0x8180,0x9974,
0x7584,0x8a84,0x8184,0x9970,0x7588,0x8a88,0x8188,0x996c,0x758c,0x8a8c,0x818c,0x9968,
0x7590,0x8a90,0x8190,0x9964,0x7594,0x8a94,0x8194,0x9960,0x7598,0x8a98,0x8198,0x995c,
0x759c,0x8a9c,0x819c,0x9958,0x75a0,0x8aa0,0x81a0,0x9954,0x75a4,0x8aa4,0x81a4,0x9950,
0x75b0,0x8ab0,0x81b0,0x9944,0x75b4,0x8ab4,0x81b4,0x9940,0x75b8,0x8ab8,0x81b8,0x993c,
0x75bc,0x8abc,0x81bc,0x9938,0x75c0,0x8ac0,0x81c0,0x9934,0x75c4,0x8ac4,0x81c4,0x9930,
0x75c8,0x8ac8,0x81c8,0x992c,0x75cc,0x8acc,0x81cc,0x9928,0x75d0,0x8ad0,0x81d0,0x9924,
0x75d4,0x8ad4,0x81d4,0x9920,0x75e0,0x8ae0,0x81e0,0x9914,0x75e4,0x8ae4,0x81e4,0x9910,
0x75e8,0x8ae8,0x81e8,0x990c,0x75ec,0x8aec,0x81ec,0x9908,0x75f0,0x8af0,0x81f0,0x9904,
0x75f4,0x8af4,0x81f4,0x9900,0x75f8,0x8af8,0x81f8,0x98fc,0x75fc,0x8afc,0x81fc,0x98f8,
0x7600,0x8b00,0x8200,0x98f4,0x7604,0x8b04,0x8204,0x98f0,0x7610,0x8b10,0x8210,0x98e4,
0x7614,0x8b14,0x8214,0x98e0,0x7618,0x8b18,0x8218,0x98dc,0x761c,0x8b1c,0x821c,0x98d8,
0x7620,0x8b20,0x8220,0x98d4,0x7624,0x8b24,0x8224,0x98d0,0x7628,0x8b28,0x8228,0x98cc,
0x762c,0x8b2c,0x822c,0x98c8,0x7630,0x8b30,0x8230,0x98c4,0x7634,0x8b34,0x8234,0x98c0,
0x7640,0x8b40,0x8240,0x98b4,0x7644,0x8b44,0x8244,0x98b0,0x7648,0x8b48,0x8248,0x98ac,
0x764c,0x8b4c,0x824c,0x98a8,0x7650,0x8b50,0x8250,0x98a4,0x7654,0x8b54,0x8254,0x98a0,
0x7658,0x8b58,0x8258,0x989c,0x765c,0x8b5c,0x825c,0x9898,0x7660,0x8b60,0x8260,0x9894,
0x7664,0x8b64,0x8264,0x9890,0x7670,0x8b70,0x8270,0x9884,0x7674,0x8b74,0x8274,0x9880,
0x7678,0x8b78,0x8278,0x987c,0x767c,0x8b7c,0x827c,0x9878,0x7680,0x8b80,0x8280,0x9874,
0x7684,0x8b84,0x8284,0x9870,0x7688,0x8b88,0x8288,0x986c,0x768c,0x8b8c,0x828c,0x9868,
0x7690,0x8b90,0x8290,0x9864,0x7694,0x8b94,0x8294,0x9860,0x76a0,0x8ba0,0x82a0,0x9854,
0x76a4,0x8ba4,0x82a4,0x9850,0x76a8,0x8ba8,0x82a8,0x984c,0x76ac,0x8bac,0x82ac,0x9848,
0x76b0,0x8bb0,0x82b0,0x9844,0x76b4,0x8bb4,0x82b4,0x9840,0x76b8,0x8bb8,0x82b8,0x983c,
0x76bc,0x8bbc,0x82bc,0x9838,0x76c0,0x8bc0,0x82c0,0x9834,0x76c4,0x8bc4,0x82c4,0x9830,
0x76d0,0x8bd0,0x82d0,0x9824,0x76d4,0x8bd4,0x82d4,0x9820,0x76d8,0x8bd8,0x82d8,0x981c,
0x76dc,0x8bdc,0x82dc,0x9818,0x76e0,0x8be0,0x82e0,0x9814,0x76e4,0x8be4,0x82e4,0x9810,
0x76e8,0x8be8,0x82e8,0x980c,0x76ec,0x8bec,0x82ec,0x9808,0x76f0,0x8bf0,0x82f0,0x9804,
0x76f4,0x8bf4,0x82f4,0x9800,0x7880,0x8d80,0x8480,0x9c74,0x7884,0x8d84,0x8484,0x9c70,
0x7888,0x8d88,0x8488,0x9c6c,0x788c,0x8d8c,0x848c,0x9c68,0x7890,0x8d90,0x8490,0x9c64,
0x7894,0x8d94,0x8494,0x9c60,0x7898,0x8d98,0x8498,0x9c5c,0x789c,0x8d9c,0x849c,0x9c58,
0x78a0,0x8da0,0x84a0,0x9c54,0x78a4,0x8da4,0x84a4,0x9c50,0x78b0,0x8db0,0x84b0,0x9c44,
0x78b4,0x8db4,0x84b4,0x9c40,0x78b8,0x8db8,0x84b8,0x9c3c,0x78bc,0x8dbc,0x84bc,0x9c38,
0x78c0,0x8dc0,0x84c0,0x9c34,0x78c4,0x8dc4,0x84c4,0x9c30,0x78c8,0x8dc8,0x84c8,0x9c2c,
0x78cc,0x8dcc,0x84cc,0x9c28,0x78d0,0x8dd0,0x84d0,0x9c24,0x78d4,0x8dd4,0x84d4,0x9c20,
0x78e0,0x8de0,0x84e0,0x9c14,0x78e4,0x8de4,0x84e4,0x9c10,0x78e8,0x8de8,0x84e8,0x9c0c,
0x78ec,0x8dec,0x84ec,0x9c08,0x78f0,0x8df0,0x84f0,0x9c04,0x78f4,0x8df4,0x84f4,0x9c00,
0x78f8,0x8df8,0x84f8,0x9bfc,0x78fc,0x8dfc,0x84fc,0x9bf8,0x7900,0x8e00,0x8500,0x9bf4,
0x7904,0x8e04,0x8504,0x9bf0,0x7910,0x8e10,0x8510,0x9be4,0x7914,0x8e14,0x8514,0x9be0,
0x7918,0x8e18,0x8518,0x9bdc,0x791c,0x8e1c,0x851c,0x9bd8,0x7920,0x8e20,0x8520,0x9bd4,
0x7924,0x8e24,0x8524,0x9bd0,0x7928,0x8e28,0x8528,0x9bcc,0x792c,0x8e2c,0x852c,0x9bc8,
0x7930,0x8e30,0x8530,0x9bc4,0x7934,0x8e34,0x8534,0x9bc0,0x7940,0x8e40,0x8540,0x9bb4,
0x7944,0x8e44,0x8544,0x9bb0,0x7948,0x8e48,0x8548,0x9bac,0x794c,0x8e4c,0x854c,0x9ba8,
0x7950,0x8e50,0x8550,0x9ba4,0x7954,0x8e54,0x8554,0x9ba0,0x7958,0x8e58,0x8558,0x9b9c,
0x795c,0x8e5c,0x855c,0x9b98,0x7960,0x8e60,0x8560,0x9b94,0x7964,0x8e64,0x8564,0x9b90,
0x7970,0x8e70,0x8570,0x9b84,0x7974,0x8e74,0x8574,0x9b80,0x7978,0x8e78,0x8578,0x9b7c,
0x797c,0x8e7c,0x857c,0x9b78,0x7980,0x8e80,0x8580,0x9b74,0x7984,0x8e84,0x8584,0x9b70,
0x7988,0x8e88,0x8588,0x9b6c,0x798c,0x8e8c,0x858c,0x9b68,0x7990,0x8e90,0x8590,0x9b64,
0x7994,0x8e94,0x8594,0x9b60,0x79a0,0x8ea0,0x85a0,0x9b54,0x79a4,0x8ea4,0x85a4,0x9b50,
0x79a8,0x8ea8,0x85a8,0x9b4c,0x79ac,0x8eac,0x85ac,0x9b48,0x79b0,0x8eb0,0x85b0,0x9b44,
0x79b4,0x8eb4,0x85b4,0x9b40,0x79b8,0x8eb8,0x85b8,0x9b3c,0x79bc,0x8ebc,0x85bc,0x9b38,
0x79c0,0x8ec0,0x85c0,0x9b34,0x79c4,0x8ec4,0x85c4,0x9b30,0x79d0,0x8ed0,0x85d0,0x9b24,
0x79d4,0x8ed4,0x85d4,0x9b20,0x79d8,0x8ed8,0x85d8,0x9b1c,0x79dc,0x8edc,0x85dc,0x9b18,
0x79e0,0x8ee0,0x85e0,0x9b14,0x79e4,0x8ee4,0x85e4,0x9b10,0x79e8,0x8ee8,0x85e8,0x9b0c,
0x79ec,0x8eec,0x85ec,0x9b08,0x79f0,0x8ef0,0x85f0,0x9b04,0x79f4,0x8ef4,0x85f4,0x9b00,
0x7b80,0x9080,0x8780,0x9f74,0x7b84,0x9084,0x8784,0x9f70,0x7b88,0x9088,0x8788,0x9f6c,
0x7b8c,0x908c,0x878c,0x9f68,0x7b90,0x9090,0x8790,0x9f64,0x7b94,0x9094,0x8794,0x9f60,
0x7b98,0x9098,0x8798,0x9f5c,0x7b9c,0x909c,0x879c,0x9f58,0x7ba0,0x90a0,0x87a0,0x9f54,
0x7ba4,0x90a4,0x87a4,0x9f50,0x7bb0,0x90b0,0x87b0,0x9f44,0x7bb4,0x90b4,0x87b4,0x9f40,
0x7bb8,0x90b8,0x87b8,0x9f3c,0x7bbc,0x90bc,0x87bc,0x9f38,0x7bc0,0x90c0,0x87c0,0x9f34,
0x7bc4,0x90c4,0x87c4,0x9f30,0x7bc8,0x90c8,0x87c8,0x9f2c,0x7bcc,0x90cc,0x87cc,0x9f28,
0x7bd0,0x90d0,0x87d0,0x9f24,0x7bd4,0x90d4,0x87d4,0x9f20,0x7be0,0x90e0,0x87e0,0x9f14,
0x7be4,0x90e4,0x87e4,0x9f10,0x7be8,0x90e8,0x87e8,0x9f0c,0x7bec,0x90ec,0x87ec,0x9f08,
0x7bf0,0x90f0,0x87f0,0x9f04,0x7bf4,0x90f4,0x87f4,0x9f00,0x7bf8,0x90f8,0x87f8,0x9efc,
0x7bfc,0x90fc,0x87fc,0x9ef8,0x7c00,0x9100,0x8800,0x9ef4,0x7c04,0x9104,0x8804,0x9ef0,
0x7c10,0x9110,0x8810,0x9ee4,0x7c14,0x9114,0x8814,0x9ee0,0x7c18,0x9118,0x8818,0x9edc,
0x7c1c,0x911c,0x881c,0x9ed8,0x7c20,0x9120,0x8820,0x9ed4,0x7c24,0x9124,0x8824,0x9ed0,
0x7c28,0x9128,0x8828,0x9ecc,0x7c2c,0x912c,0x882c,0x9ec8,0x7c30,0x9130,0x8830,0x9ec4,
0x7c34,0x9134,0x8834,0x9ec0,0x7c40,0x9140,0x8840,0x9eb4,0x7c44,0x9144,0x8844,0x9eb0,
0x7c48,0x9148,0x8848,0x9eac,0x7c4c,0x914c,0x884c,0x9ea8,0x7c50,0x9150,0x8850,0x9ea4,
0x7c54,0x9154,0x8854,0x9ea0,0x7c58,0x9158,0x8858,0x9e9c,0x7c5c,0x915c,0x885c,0x9e98,
0x7c60,0x9160,0x8860,0x9e94,0x7c64,0x9164,0x8864,0x9e90,0x7c70,0x9170,0x8870,0x9e84,
0x7c74,0x9174,0x8874,0x9e80,0x7c78,0x9178,0x8878,0x9e7c,0x7c7c,0x917c,0x887c,0x9e78,
0x7c80,0x9180,0x8880,0x9e74,0x7c84,0x9184,0x8884,0x9e70,0x7c88,0x9188,0x8888,0x9e6c,
0x7c8c,0x918c,0x888c,0x9e68,0x7c90,0x9190,0x8890,0x9e64,0x7c94,0x9194,0x8894,0x9e60,
0x7ca0,0x91a0,0x88a0,0x9e54,0x7ca4,0x91a4,0x88a4,0x9e50,0x7ca8,0x91a8,0x88a8,0x9e4c,
0x7cac,0x91ac,0x88ac,0x9e48,0x7cb0,0x91b0,0x88b0,0x9e44,0x7cb4,0x91b4,0x88b4,0x9e40,
0x7cb8,0x91b8,0x88b8,0x9e3c,0x7cbc,0x91bc,0x88bc,0x9e38,0x7cc0,0x91c0,0x88c0,0x9e34,
0x7cc4,0x91c4,0x88c4,0x9e30,0x7cd0,0x91d0,0x88d0,0x9e24,0x7cd4,0x91d4,0x88d4,0x9e20,
0x7cd8,0x91d8,0x88d8,0x9e1c,0x7cdc,0x91dc,0x88dc,0x9e18,0x7ce0,0x91e0,0x88e0,0x9e14,
0x7ce4,0x91e4,0x88e4,0x9e10,0x7ce8,0x91e8,0x88e8,0x9e0c,0x7cec,0x91ec,0x88ec,0x9e08,
0x7cf0,0x91f0,0x88f0,0x9e04,0x7cf4,0x91f4,0x88f4,0x9e00,0x7400,0x9f80,0x8174,0xa274,
0x7574,0x9f84,0x8170,0xa270,0x7570,0x9f88,0x816c,0xa26c,0x756c,0x9f8c,0x8168,0xa268,
0x7568,0x9f90,0x8164,0xa264,0x7564,0x9f94,0x8160,0xa260,0x7560,0x9f98,0x815c,0xa25c,
0x755c,0x9f9c,0x8158,0xa258,0x7558,0x9fa0,0x8154,0xa254,0x7554,0x9fa4,0x8150,0xa250,
0x7550,0x9fb0,0x8144,0xa244,0x7544,0x9fb4,0x8140,0xa240,0x7540,0x9fb8,0x813c,0xa23c,
0x753c,0x9fbc,0x8138,0xa238,0x7538,0x9fc0,0x8134,0xa234,0x7534,0x9fc4,0x8130,0xa230,
0x7530,0x9fc8,0x812c,0xa22c,0x752c,0x9fcc,0x8128,0xa228,0x7528,0x9fd0,0x8124,0xa224,
0x7524,0x9fd4,0x8120,0xa220,0x7520,0x9fe0,0x8114,0xa214,0x7514,0x9fe4,0x8110,0xa210,
0x7510,0x9fe8,0x810c,0xa20c,0x750c,0x9fec,0x8108,0xa208,0x7508,0x9ff0,0x8104,0xa204,
0x7504,0x9ff4,0x8100,0xa200,0x7500,0x9ff8,0x80fc,0xa1fc,0x74fc,0x9ffc,0x80f8,0xa1f8,
0x74f8,0xa000,0x80f4,0xa1f4,0x74f4,0xa004,0x80f0,0xa1f0,0x74f0,0xa010,0x80e4,0xa1e4,
0x74e4,0xa014,0x80e0,0xa1e0,0x74e0,0xa018,0x80dc,0xa1dc,0x74dc,0xa01c,0x80d8,0xa1d8,
0x74d8,0xa020,0x80d4,0xa1d4,0x74d4,0xa024,0x80d0,0xa1d0,0x74d0,0xa028,0x80cc,0xa1cc,
0x74cc,0xa02c,0x80c8,0xa1c8,0x74c8,0xa030,0x80c4,0xa1c4,0x74c4,0xa034,0x80c0,0xa1c0,
0x74c0,0xa040,0x80b4,0xa1b4,0x74b4,0xa044,0x80b0,0xa1b0,0x74b0,0xa048,0x80ac,0xa1ac,
0x74ac,0xa04c,0x80a8,0xa1a8,0x74a8,0xa050,0x80a4,0xa1a4,0x74a4,0xa054,0x80a0,0xa1a0,
0x74a0,0xa058,0x809c,0xa19c,0x749c,0xa05c,0x8098,0xa198,0x7498,0xa060,0x8094,0xa194,
0x7494,0xa064,0x8090,0xa190,0x7490,0xa070,0x8084,0xa184,0x7484,0xa074,0x8080,0xa180,
0x7480,0xa078,0x807c,0xa17c,0x747c,0xa07c,0x8078,0xa178,0x7478,0xa080,0x8074,0xa174,
0x7474,0xa084,0x8070,0xa170,0x7470,0xa088,0x806c,0xa16c,0x746c,0xa08c,0x8068,0xa168,
0x7468,0xa090,0x8064,0xa164,0x7464,0xa094,0x8060,0xa160,0x7460,0xa0a0,0x8054,0xa154,
0x7454,0xa0a4,0x8050,0xa150,0x7450,0xa0a8,0x804c,0xa14c,0x744c,0xa0ac,0x8048,0xa148,
0x7448,0xa0b0,0x8044,0xa144,0x7444,0xa0b4,0x8040,0xa140,0x7440,0xa0b8,0x803c,0xa13c,
0x743c,0xa0bc,0x8038,0xa138,0x7438,0xa0c0,0x8034,0xa134,0x7434,0xa0c4,0x8030,0xa130,
0x7430,0xa0d0,0x8024,0xa124,0x7424,0xa0d4,0x8020,0xa120,0x7420,0xa0d8,0x801c,0xa11c,
0x741c,0xa0dc,0x8018,0xa118,0x7418,0xa0e0,0x8014,0xa114,0x7414,0xa0e4,0x8010,0xa110,
0x7410,0xa0e8,0x800c,0xa10c,0x740c,0xa0ec,0x8008,0xa108,0x7408,0xa0f0,0x8004,0xa104,
0x7404,0xa0f4,0x8000,0xa100,0x7d00,0x9c80,0x8a74,0x9374,0x7e74,0x9c84,0x8a70,0x9370,
0x7e70,0x9c88,0x8a6c,0x936c,0x7e6c,0x9c8c,0x8a68,0x9368,0x7e68,0x9c90,0x8a64,0x9364,
0x7e64,0x9c94,0x8a60,0x9360,0x7e60,0x9c98,0x8a5c,0x935c,0x7e5c,0x9c9c,0x8a58,0x9358,
0x7e58,0x9ca0,0x8a54,0x9354,0x7e54,0x9ca4,0x8a50,0x9350,0x7e50,0x9cb0,0x8a44,0x9344,
0x7e44,0x9cb4,0x8a40,0x9340,0x7e40,0x9cb8,0x8a3c,0x933c,0x7e3c,0x9cbc,0x8a38,0x9338,
0x7e38,0x9cc0,0x8a34,0x9334,0x7e34,0x9cc4,0x8a30,0x9330,0x7e30,0x9cc8,0x8a2c,0x932c,
0x7e2c,0x9ccc,0x8a28,0x9328,0x7e28,0x9cd0,0x8a24,0x9324,0x7e24,0x9cd4,0x8a20,0x9320,
0x7e20,0x9ce0,0x8a14,0x9314,0x7e14,0x9ce4,0x8a10,0x9310,0x7e10,0x9ce8,0x8a0c,0x930c,
0x7e0c,0x9cec,0x8a08,0x9308,0x7e08,0x9cf0,0x8a04,0x9304,0x7e04,0x9cf4,0x8a00,0x9300,
0x7e00,0x9cf8,0x89fc,0x92fc,0x7dfc,0x9cfc,0x89f8,0x92f8,0x7df8,0x9d00,0x89f4,0x92f4,
0x7df4,0x9d04,0x89f0,0x92f0,0x7df0,0x9d10,0x89e4,0x92e4,0x7de4,0x9d14,0x89e0,0x92e0,
0x7de0,0x9d18,0x89dc,0x92dc,0x7ddc,0x9d1c,0x89d8,0x92d8,0x7dd8,0x9d20,0x89d4,0x92d4,
0x7dd4,0x9d24,0x89d0,0x92d0,0x7dd0,0x9d28,0x89cc,0x92cc,0x7dcc,0x9d2c,0x89c8,0x92c8,
0x7dc8,0x9d30,0x89c4,0x92c4,0x7dc4,0x9d34,0x89c0,0x92c0,0x7dc0,0x9d40,0x89b4,0x92b4,
0x7db4,0x9d44,0x89b0,0x92b0,0x7db0,0x9d48,0x89ac,0x92ac,0x7dac,0x9d4c,0x89a8,0x92a8,
0x7da8,0x9d50,0x89a4,0x92a4,0x7da4,0x9d54,0x89a0,0x92a0,0x7da0,0x9d58,0x899c,0x929c,
0x7d9c,0x9d5c,0x8998,0x9298,0x7d98,0x9d60,0x8994,0x9294,0x7d94,0x9d64,0x8990,0x9290,
0x7d90,0x9d70,0x8984,0x9284,0x7d84,0x9d74,0x8980,0x9280,0x7d80,0x9d78,0x897c,0x927c,
0x7d7c,0x9d7c,0x8978,0x9278,0x7d78,0x9d80,0x8974,0x9274,0x7d74,0x9d84,0x8970,0x9270,
0x7d70,0x9d88,0x896c,0x926c,0x7d6c,0x9d8c,0x8968,0x9268,0x7d68,0x9d90,0x8964,0x9264,
0x7d64,0x9d94,0x8960,0x9260,0x7d60,0x9da0,0x8954,0x9254,0x7d54,0x9da4,0x8950,0x9250,
0x7d50,0x9da8,0x894c,0x924c,0x7d4c,0x9dac,0x8948,0x9248,0x7d48,0x9db0,0x8944,0x9244,
0x7d44,0x9db4,0x8940,0x9240,0x7d40,0x9db8,0x893c,0x923c,0x7d3c,0x9dbc,0x8938,0x9238,
0x7d38,0x9dc0,0x8934,0x9234,0x7d34,0x9dc4,0x8930,0x9230,0x7d30,0x9dd0,0x8924,0x9224,
0x7d24,0x9dd4,0x8920,0x9220,0x7d20,0x9dd8,0x891c,0x921c,0x7d1c,0x9ddc,0x8918,0x9218,
0x7d18,0x9de0,0x8914,0x9214,0x7d14,0x9de4,0x8910,0x9210,0x7d10,0x9de8,0x890c,0x920c,
0x7d0c,0x9dec,0x8908,0x9208,0x7d08,0x9df0,0x8904,0x9204,0x7d04,0x9df4,0x8900,0x9200,
0x7a00,0x9980,0x8774,0x9074,0x7b74,0x9984,0x8770,0x9070,0x7b70,0x9988,0x876c,0x906c,
0x7b6c,0x998c,0x8768,0x9068,0x7b68,0x9990,0x8764,0x9064,0x7b64,0x9994,0x8760,0x9060,
0x7b60,0x9998,0x875c,0x905c,0x7b5c,0x999c,0x8758,0x9058,0x7b58,0x99a0,0x8754,0x9054,
0x7b54,0x99a4,0x8750,0x9050,0x7b50,0x99b0,0x8744,0x9044,0x7b44,0x99b4,0x8740,0x9040,
0x7b40,0x99b8,0x873c,0x903c,0x7b3c,0x99bc,0x8738,0x9038,0x7b38,0x99c0,0x8734,0x9034,
0x7b34,0x99c4,0x8730,0x9030,0x7b30,0x99c8,0x872c,0x902c,0x7b2c,0x99cc,0x8728,0x9028,
0x7b28,0x99d0,0x8724,0x9024,0x7b24,0x99d4,0x8720,0x9020,0x7b20,0x99e0,0x8714,0x9014,
0x7b14,0x99e4,0x8710,0x9010,0x7b10,0x99e8,0x870c,0x900c,0x7b0c,0x99ec,0x8708,0x9008,
0x7b08,0x99f0,0x8704,0x9004,0x7b04,0x99f4,0x8700,0x9000,0x7b00,0x99f8,0x86fc,0x8ffc,
0x7afc,0x99fc,0x86f8,0x8ff8,0x7af8,0x9a00,0x86f4,0x8ff4,0x7af4,0x9a04,0x86f0,0x8ff0,
0x7af0,0x9a10,0x86e4,0x8fe4,0x7ae4,0x9a14,0x86e0,0x8fe0,0x7ae0,0x9a18,0x86dc,0x8fdc,
0x7adc,0x9a1c,0x86d8,0x8fd8,0x7ad8,0x9a20,0x86d4,0x8fd4,0x7ad4,0x9a24,0x86d0,0x8fd0,
0x7ad0,0x9a28,0x86cc,0x8fcc,0x7acc,0x9a2c,0x86c8,0x8fc8,0x7ac8,0x9a30,0x86c4,0x8fc4,
0x7ac4,0x9a34,0x86c0,0x8fc0,0x7ac0,0x9a40,0x86b4,0x8fb4,0x7ab4,0x9a44,0x86b0,0x8fb0,
0x7ab0,0x9a48,0x86ac,0x8fac,0x7aac,0x9a4c,0x86a8,0x8fa8,0x7aa8,0x9a50,0x86a4,0x8fa4,
0x7aa4,0x9a54,0x86a0,0x8fa0,0x7aa0,0x9a58,0x869c,0x8f9c,0x7a9c,0x9a5c,0x8698,0x8f98,
0x7a98,0x9a60,0x8694,0x8f94,0x7a94,0x9a64,0x8690,0x8f90,0x7a90,0x9a70,0x8684,0x8f84,
0x7a84,0x9a74,0x8680,0x8f80,0x7a80,0x9a78,0x867c,0x8f7c,0x7a7c,0x9a7c,0x8678,0x8f78,
0x7a78,0x9a80,0x8674,0x8f74,0x7a74,0x9a84,0x8670,0x8f70,0x7a70,0x9a88,0x866c,0x8f6c,
0x7a6c,0x9a8c,0x8668,0x8f68,0x7a68,0x9a90,0x8664,0x8f64,0x7a64,0x9a94,0x8660,0x8f60,
0x7a60,0x9aa0,0x8654,0x8f54,0x7a54,0x9aa4,0x8650,0x8f50,0x7a50,0x9aa8,0x864c,0x8f4c,
0x7a4c,0x9aac,0x8648,0x8f48,0x7a48,0x9ab0,0x8644,0x8f44,0x7a44,0x9ab4,0x8640,0x8f40,
0x7a40,0x9ab8,0x863c,0x8f3c,0x7a3c,0x9abc,0x8638,0x8f38,0x7a38,0x9ac0,0x8634,0x8f34,
0x7a34,0x9ac4,0x8630,0x8f30,0x7a30,0x9ad0,0x8624,0x8f24,0x7a24,0x9ad4,0x8620,0x8f20,
0x7a20,0x9ad8,0x861c,0x8f1c,0x7a1c,0x9adc,0x8618,0x8f18,0x7a18,0x9ae0,0x8614,0x8f14,
0x7a14,0x9ae4,0x8610,0x8f10,0x7a10,0x9ae8,0x860c,0x8f0c,0x7a0c,0x9aec,0x8608,0x8f08,
0x7a08,0x9af0,0x8604,0x8f04,0x7a04,0x9af4,0x8600,0x8f00,0x7700,0x9680,0x8474,0x8d74,
0x7874,0x9684,0x8470,0x8d70,0x7870,0x9688,0x846c,0x8d6c,0x786c,0x968c,0x8468,0x8d68,
0x7868,0x9690,0x8464,0x8d64,0x7864,0x9694,0x8460,0x8d60,0x7860,0x9698,0x845c,0x8d5c,
0x785c,0x969c,0x8458,0x8d58,0x7858,0x96a0,0x8454,0x8d54,0x7854,0x96a4,0x8450,0x8d50,
0x7850,0x96b0,0x8444,0x8d44,0x7844,0x96b4,0x8440,0x8d40,0x7840,0x96b8,0x843c,0x8d3c,
0x783c,0x96bc,0x8438,0x8d38,0x7838,0x96c0,0x8434,0x8d34,0x7834,0x96c4,0x8430,0x8d30,
0x7830,0x96c8,0x842c,0x8d2c,0x782c,0x96cc,0x8428,0x8d28,0x7828,0x96d0,0x8424,0x8d24,
0x7824,0x96d4,0x8420,0x8d20,0x7820,0x96e0,0x8414,0x8d14,0x7814,0x96e4,0x8410,0x8d10,
0x7810,0x96e8,0x840c,0x8d0c,0x780c,0x96ec,0x8408,0x8d08,0x7808,0x96f0,0x8404,0x8d04,
0x7804,0x96f4,0x8400,0x8d00,0x7800,0x96f8,0x83fc,0x8cfc,0x77fc,0x96fc,0x83f8,0x8cf8,
0x77f8,0x9700,0x83f4,0x8cf4,0x77f4,0x9704,0x83f0,0x8cf0,0x77f0,0x9710,0x83e4,0x8ce4,
0x77e4,0x9714,0x83e0,0x8ce0,0x77e0,0x9718,0x83dc,0x8cdc,0x77dc,0x971c,0x83d8,0x8cd8,
0x77d8,0x9720,0x83d4,0x8cd4,0x77d4,0x9724,0x83d0,0x8cd0,0x77d0,0x9728,0x83cc,0x8ccc,
0x77cc,0x972c,0x83c8,0x8cc8,0x77c8,0x9730,0x83c4,0x8cc4,0x77c4,0x9734,0x83c0,0x8cc0,
0x77c0,0x9740,0x83b4,0x8cb4,0x77b4,0x9744,0x83b0,0x8cb0,0x77b0,0x9748,0x83ac,0x8cac,
0x77ac,0x974c,0x83a8,0x8ca8,0x77a8,0x9750,0x83a4,0x8ca4,0x77a4,0x9754,0x83a0,0x8ca0,
0x77a0,0x9758,0x839c,0x8c9c,0x779c,0x975c,0x8398,0x8c98,0x7798,0x9760,0x8394,0x8c94,
0x7794,0x9764,0x8390,0x8c90,0x7790,0x9770,0x8384,0x8c84,0x7784,0x9774,0x8380,0x8c80,
0x7780,0x9778,0x837c,0x8c7c,0x777c,0x977c,0x8378,0x8c78,0x7778,0x9780,0x8374,0x8c74,
0x7774,0x9784,0x8370,0x8c70,0x7770,0x9788,0x836c,0x8c6c,0x776c,0x978c,0x8368,0x8c68,
0x7768,0x9790,0x8364,0x8c64,0x7764,0x9794,0x8360,0x8c60,0x7760,0x97a0,0x8354,0x8c54,
0x7754,0x97a4,0x8350,0x8c50,0x7750,0x97a8,0x834c,0x8c4c,0x774c,0x97ac,0x8348,0x8c48,
0x7748,0x97b0,0x8344,0x8c44,0x7744,0x97b4,0x8340,0x8c40,0x7740,0x97b8,0x833c,0x8c3c,
0x773c,0x97bc,0x8338,0x8c38,0x7738,0x97c0,0x8334,0x8c34,0x7734,0x97c4,0x8330,0x8c30,
0x7730,0x97d0,0x8324,0x8c24,0x7724,0x97d4,0x8320,0x8c20,0x7720,0x97d8,0x831c,0x8c1c,
0x771c,0x97dc,0x8318,0x8c18,0x7718,0x97e0,0x8314,0x8c14,0x7714,0x97e4,0x8310,0x8c10,
0x7710,0x97e8,0x830c,0x8c0c,0x770c,0x97ec,0x8308,0x8c08,0x7708,0x97f0,0x8304,0x8c04,
0x7704,0x97f4,0x8300,0x8c00,0x2280,0x4e80,0x3280,0x521c,0x2290,0x4e90,0x3290,0x520c,
0x32a0,0x51fc,0x22b0,0x4eb0,0x32b0,0x51ec,0x22c0,0x4ec0,0x22d0,0x4ed0,0x32d0,0x51cc,
0x22e0,0x4ee0,0x32e0,0x51bc,0x32f0,0x51ac,0x2300,0x4f00,0x3300,0x519c,0x2310,0x4f10,
0x2284,0x4e84,0x3284,0x5218,0x2294,0x4e94,0x3294,0x5208,0x32a4,0x51f8,0x22b4,0x4eb4,
0x32b4,0x51e8,0x22c4,0x4ec4,0x22d4,0x4ed4,0x32d4,0x51c8,0x22e4,0x4ee4,0x32e4,0x51b8,
0x32f4,0x51a8,0x2304,0x4f04,0x3304,0x5198,0x2314,0x4f14,0x2288,0x4e88,0x3288,0x5214,
0x2298,0x4e98,0x3298,0x5204,0x32a8,0x51f4,0x22b8,0x4eb8,0x32b8,0x51e4,0x22c8,0x4ec8,
0x22d8,0x4ed8,0x32d8,0x51c4,0x22e8,0x4ee8,0x32e8,0x51b4,0x32f8,0x51a4,0x2308,0x4f08,
0x3308,0x5194,0x2318,0x4f18,0x228c,0x4e8c,0x328c,0x5210,0x229c,0x4e9c,0x329c,0x5200,
0x32ac,0x51f0,0x22bc,0x4ebc,0x32bc,0x51e0,0x22cc,0x4ecc,0x22dc,0x4edc,0x32dc,0x51c0,
0x22ec,0x4eec,0x32ec,0x51b0,0x32fc,0x51a0,0x230c,0x4f0c,0x330c,0x5190,0x231c,0x4f1c,
0x2380,0x4f80,0x3380,0x511c,0x2390,0x4f90,0x3390,0x510c,0x33a0,0x50fc,0x23b0,0x4fb0,
0x33b0,0x50ec,0x23c0,0x4fc0,0x23d0,0x4fd0,0x33d0,0x50cc,0x23e0,0x4fe0,0x33e0,0x50bc,
0x33f0,0x50ac,0x2400,0x5000,0x3400,0x509c,0x2410,0x5010,0x2384,0x4f84,0x3384,0x5118,
0x2394,0x4f94,0x3394,0x5108,0x33a4,0x50f8,0x23b4,0x4fb4,0x33b4,0x50e8,0x23c4,0x4fc4,
0x23d4,0x4fd4,0x33d4,0x50c8,0x23e4,0x4fe4,0x33e4,0x50b8,0x33f4,0x50a8,0x2404,0x5004,
0x3404,0x5098,0x2414,0x5014,0x2388,0x4f88,0x3388,0x5114,0x2398,0x4f98,0x3398,0x5104,
0x33a8,0x50f4,0x23b8,0x4fb8,0x33b8,0x50e4,0x23c8,0x4fc8,0x23d8,0x4fd8,0x33d8,0x50c4,
0x23e8,0x4fe8,0x33e8,0x50b4,0x33f8,0x50a4,0x2408,0x5008,0x3408,0x5094,0x2418,0x5018,
0x238c,0x4f8c,0x338c,0x5110,0x239c,0x4f9c,0x339c,0x5100,0x33ac,0x50f0,0x23bc,0x4fbc,
0x33bc,0x50e0,0x23cc,0x4fcc,0x23dc,0x4fdc,0x33dc,0x50c0,0x23ec,0x4fec,0x33ec,0x50b0,
0x33fc,0x50a0,0x240c,0x500c,0x340c,0x5090,0x241c,0x501c,0x2680,0x4280,0x3680,0x561c,
0x2690,0x4290,0x3690,0x560c,0x36a0,0x55fc,0x26b0,0x42b0,0x36b0,0x55ec,0x26c0,0x42c0,
0x26d0,0x42d0,0x36d0,0x55cc,0x26e0,0x42e0,0x36e0,0x55bc,0x36f0,0x55ac,0x2700,0x4300,
0x3700,0x559c,0x2710,0x4310,0x2684,0x4284,0x3684,0x5618,0x2694,0x4294,0x3694,0x5608,
0x36a4,0x55f8,0x26b4,0x42b4,0x36b4,0x55e8,0x26c4,0x42c4,0x26d4,0x42d4,0x36d4,0x55c8,
0x26e4,0x42e4,0x36e4,0x55b8,0x36f4,0x55a8,0x2704,0x4304,0x3704,0x5598,0x2714,0x4314,
0x2688,0x4288,0x3688,0x5614,0x2698,0x4298,0x3698,0x5604,0x36a8,0x55f4,0x26b8,0x42b8,
0x36b8,0x55e4,0x26c8,0x42c8,0x26d8,0x42d8,0x36d8,0x55c4,0x26e8,0x42e8,0x36e8,0x55b4,
0x36f8,0x55a4,0x2708,0x4308,0x3708,0x5594,0x2718,0x4318,0x268c,0x428c,0x368c,0x5610,
0x269c,0x429c,0x369c,0x5600,0x36ac,0x55f0,0x26bc,0x42bc,0x36bc,0x55e0,0x26cc,0x42cc,
0x26dc,0x42dc,0x36dc,0x55c0,0x26ec,0x42ec,0x36ec,0x55b0,0x36fc,0x55a0,0x270c,0x430c,
0x370c,0x5590,0x271c,0x431c,0x2780,0x4380,0x3780,0x551c,0x2790,0x4390,0x3790,0x550c,
0x37a0,0x54fc,0x27b0,0x43b0,0x37b0,0x54ec,0x27c0,0x43c0,0x27d0,0x43d0,0x37d0,0x54cc,
0x27e0,0x43e0,0x37e0,0x54bc,0x37f0,0x54ac,0x2800,0x4400,0x3800,0x549c,0x2810,0x4410,
0x2784,0x4384,0x3784,0x5518,0x2794,0x4394,0x3794,0x5508,0x37a4,0x54f8,0x27b4,0x43b4,
0x37b4,0x54e8,0x27c4,0x43c4,0x27d4,0x43d4,0x37d4,0x54c8,0x27e4,0x43e4,0x37e4,0x54b8,
0x37f4,0x54a8,0x2804,0x4404,0x3804,0x5498,0x2814,0x4414,0x2788,0x4388,0x3788,0x5514,
0x2798,0x4398,0x3798,0x5504,0x37a8,0x54f4,0x27b8,0x43b8,0x37b8,0x54e4,0x27c8,0x43c8,
0x27d8,0x43d8,0x37d8,0x54c4,0x27e8,0x43e8,0x37e8,0x54b4,0x37f8,0x54a4,0x2808,0x4408,
0x3808,0x5494,0x2818,0x4418,0x278c,0x438c,0x378c,0x5510,0x279c,0x439c,0x379c,0x5500,
0x37ac,0x54f0,0x27bc,0x43bc,0x37bc,0x54e0,0x27cc,0x43cc,0x27dc,0x43dc,0x37dc,0x54c0,
0x27ec,0x43ec,0x37ec,0x54b0,0x37fc,0x54a0,0x280c,0x440c,0x380c,0x5490,0x281c,0x441c,
0x2a80,0x4680,0x3a80,0x5a1c,0x2a90,0x4690,0x3a90,0x5a0c,0x3aa0,0x59fc,0x2ab0,0x46b0,
0x3ab0,0x59ec,0x2ac0,0x46c0,0x2ad0,0x46d0,0x3ad0,0x59cc,0x2ae0,0x46e0,0x3ae0,0x59bc,
0x3af0,0x59ac,0x2b00,0x4700,0x3b00,0x599c,0x2b10,0x4710,0x2a84,0x4684,0x3a84,0x5a18,
0x2a94,0x4694,0x3a94,0x5a08,0x3aa4,0x59f8,0x2ab4,0x46b4,0x3ab4,0x59e8,0x2ac4,0x46c4,
0x2ad4,0x46d4,0x3ad4,0x59c8,0x2ae4,0x46e4,0x3ae4,0x59b8,0x3af4,0x59a8,0x2b04,0x4704,
0x3b04,0x5998,0x2b14,0x4714,0x2a88,0x4688,0x3a88,0x5a14,0x2a98,0x4698,0x3a98,0x5a04,
0x3aa8,0x59f4,0x2ab8,0x46b8,0x3ab8,0x59e4,0x2ac8,0x46c8,0x2ad8,0x46d8,0x3ad8,0x59c4,
0x2ae8,0x46e8,0x3ae8,0x59b4,0x3af8,0x59a4,0x2b08,0x4708,0x3b08,0x5994,0x2b18,0x4718,
0x2a8c,0x468c,0x3a8c,0x5a10,0x2a9c,0x469c,0x3a9c,0x5a00,0x3aac,0x59f0,0x2abc,0x46bc,
0x3abc,0x59e0,0x2acc,0x46cc,0x2adc,0x46dc,0x3adc,0x59c0,0x2aec,0x46ec,0x3aec,0x59b0,
0x3afc,0x59a0,0x2b0c,0x470c,0x3b0c,0x5990,0x2b1c,0x471c,0x2b80,0x4780,0x3b80,0x591c,
0x2b90,0x4790,0x3b90,0x590c,0x3ba0,0x58fc,0x2bb0,0x47b0,0x3bb0,0x58ec,0x2bc0,0x47c0,
0x2bd0,0x47d0,0x3bd0,0x58cc,0x2be0,0x47e0,0x3be0,0x58bc,0x3bf0,0x58ac,0x2c00,0x4800,
0x3c00,0x589c,0x2c10,0x4810,0x2b84,0x4784,0x3b84,0x5918,0x2b94,0x4794,0x3b94,0x5908,
0x3ba4,0x58f8,0x2bb4,0x47b4,0x3bb4,0x58e8,0x2bc4,0x47c4,0x2bd4,0x47d4,0x3bd4,0x58c8,
0x2be4,0x47e4,0x3be4,0x58b8,0x3bf4,0x58a8,0x2c04,0x4804,0x3c04,0x5898,0x2c14,0x4814,
0x2b88,0x4788,0x3b88,0x5914,0x2b98,0x4798,0x3b98,0x5904,0x3ba8,0x58f4,0x2bb8,0x47b8,
0x3bb8,0x58e4,0x2bc8,0x47c8,0x2bd8,0x47d8,0x3bd8,0x58c4,0x2be8,0x47e8,0x3be8,0x58b4,
0x3bf8,0x58a4,0x2c08,0x4808,0x3c08,0x5894,0x2c18,0x4818,0x2b8c,0x478c,0x3b8c,0x5910,
0x2b9c,0x479c,0x3b9c,0x5900,0x3bac,0x58f0,0x2bbc,0x47bc,0x3bbc,0x58e0,0x2bcc,0x47cc,
0x2bdc,0x47dc,0x3bdc,0x58c0,0x2bec,0x47ec,0x3bec,0x58b0,0x3bfc,0x58a0,0x2c0c,0x480c,
0x3c0c,0x5890,0x2c1c,0x481c,0x2e80,0x4a80,0x3e80,0x5e1c,0x2e90,0x4a90,0x3e90,0x5e0c,
0x3ea0,0x5dfc,0x2eb0,0x4ab0,0x3eb0,0x5dec,0x2ec0,0x4ac0,0x2ed0,0x4ad0,0x3ed0,0x5dcc,
0x2ee0,0x4ae0,0x3ee0,0x5dbc,0x3ef0,0x5dac,0x2f00,0x4b00,0x3f00,0x5d9c,0x2f10,0x4b10,
0x2e84,0x4a84,0x3e84,0x5e18,0x2e94,0x4a94,0x3e94,0x5e08,0x3ea4,0x5df8,0x2eb4,0x4ab4,
0x3eb4,0x5de8,0x2ec4,0x4ac4,0x2ed4,0x4ad4,0x3ed4,0x5dc8,0x2ee4,0x4ae4,0x3ee4,0x5db8,
0x3ef4,0x5da8,0x2f04,0x4b04,0x3f04,0x5d98,0x2f14,0x4b14,0x2e88,0x4a88,0x3e88,0x5e14,
0x2e98,0x4a98,0x3e98,0x5e04,0x3ea8,0x5df4,0x2eb8,0x4ab8,0x3eb8,0x5de4,0x2ec8,0x4ac8,
0x2ed8,0x4ad8,0x3ed8,0x5dc4,0x2ee8,0x4ae8,0x3ee8,0x5db4,0x3ef8,0x5da4,0x2f08,0x4b08,
0x3f08,0x5d94,0x2f18,0x4b18,0x2e8c,0x4a8c,0x3e8c,0x5e10,0x2e9c,0x4a9c,0x3e9c,0x5e00,
0x3eac,0x5df0,0x2ebc,0x4abc,0x3ebc,0x5de0,0x2ecc,0x4acc,0x2edc,0x4adc,0x3edc,0x5dc0,
0x2eec,0x4aec,0x3eec,0x5db0,0x3efc,0x5da0,0x2f0c,0x4b0c,0x3f0c,0x5d90,0x2f1c,0x4b1c,
0x2f80,0x4b80,0x3f80,0x5d1c,0x2f90,0x4b90,0x3f90,0x5d0c,0x3fa0,0x5cfc,0x2fb0,0x4bb0,
0x3fb0,0x5cec,0x2fc0,0x4bc0,0x2fd0,0x4bd0,0x3fd0,0x5ccc,0x2fe0,0x4be0,0x3fe0,0x5cbc,
0x3ff0,0x5cac,0x3000,0x4c00,0x4000,0x5c9c,0x3010,0x4c10,0x2f84,0x4b84,0x3f84,0x5d18,
0x2f94,0x4b94,0x3f94,0x5d08,0x3fa4,0x5cf8,0x2fb4,0x4bb4,0x3fb4,0x5ce8,0x2fc4,0x4bc4,
0x2fd4,0x4bd4,0x3fd4,0x5cc8,0x2fe4,0x4be4,0x3fe4,0x5cb8,0x3ff4,0x5ca8,0x3004,0x4c04,
0x4004,0x5c98,0x3014,0x4c14,0x2f88,0x4b88,0x3f88,0x5d14,0x2f98,0x4b98,0x3f98,0x5d04,
0x3fa8,0x5cf4,0x2fb8,0x4bb8,0x3fb8,0x5ce4,0x2fc8,0x4bc8,0x2fd8,0x4bd8,0x3fd8,0x5cc4,
0x2fe8,0x4be8,0x3fe8,0x5cb4,0x3ff8,0x5ca4,0x3008,0x4c08,0x4008,0x5c94,0x3018,0x4c18,
0x2f8c,0x4b8c,0x3f8c,0x5d10,0x2f9c,0x4b9c,0x3f9c,0x5d00,0x3fac,0x5cf0,0x2fbc,0x4bbc,
0x3fbc,0x5ce0,0x2fcc,0x4bcc,0x2fdc,0x4bdc,0x3fdc,0x5cc0,0x2fec,0x4bec,0x3fec,0x5cb0,
0x3ffc,0x5ca0,0x300c,0x4c0c,0x400c,0x5c90,0x301c,0x4c1c,0x2480,0x5e80,0x361c,0x621c,
0x261c,0x5e90,0x360c,0x620c,0x35fc,0x61fc,0x25fc,0x5eb0,0x35ec,0x61ec,0x25ec,0x5ec0,
0x25dc,0x5ed0,0x35cc,0x61cc,0x25cc,0x5ee0,0x35bc,0x61bc,0x35ac,0x61ac,0x25ac,0x5f00,
0x359c,0x619c,0x259c,0x5f10,0x258c,0x5e84,0x3618,0x6218,0x2618,0x5e94,0x3608,0x6208,
0x35f8,0x61f8,0x25f8,0x5eb4,0x35e8,0x61e8,0x25e8,0x5ec4,0x25d8,0x5ed4,0x35c8,0x61c8,
0x25c8,0x5ee4,0x35b8,0x61b8,0x35a8,0x61a8,0x25a8,0x5f04,0x3598,0x6198,0x2598,0x5f14,
0x2588,0x5e88,0x3614,0x6214,0x2614,0x5e98,0x3604,0x6204,0x35f4,0x61f4,0x25f4,0x5eb8,
0x35e4,0x61e4,0x25e4,0x5ec8,0x25d4,0x5ed8,0x35c4,0x61c4,0x25c4,0x5ee8,0x35b4,0x61b4,
0x35a4,0x61a4,0x25a4,0x5f08,0x3594,0x6194,0x2594,0x5f18,0x2584,0x5e8c,0x3610,0x6210,
0x2610,0x5e9c,0x3600,0x6200,0x35f0,0x61f0,0x25f0,0x5ebc,0x35e0,0x61e0,0x25e0,0x5ecc,
0x25d0,0x5edc,0x35c0,0x61c0,0x25c0,0x5eec,0x35b0,0x61b0,0x35a0,0x61a0,0x25a0,0x5f0c,
0x3590,0x6190,0x2590,0x5f1c,0x2580,0x5f80,0x351c,0x611c,0x251c,0x5f90,0x350c,0x610c,
0x34fc,0x60fc,0x24fc,0x5fb0,0x34ec,0x60ec,0x24ec,0x5fc0,0x24dc,0x5fd0,0x34cc,0x60cc,
0x24cc,0x5fe0,0x34bc,0x60bc,0x34ac,0x60ac,0x24ac,0x6000,0x349c,0x609c,0x249c,0x6010,
0x248c,0x5f84,0x3518,0x6118,0x2518,0x5f94,0x3508,0x6108,0x34f8,0x60f8,0x24f8,0x5fb4,
0x34e8,0x60e8,0x24e8,0x5fc4,0x24d8,0x5fd4,0x34c8,0x60c8,0x24c8,0x5fe4,0x34b8,0x60b8,
0x34a8,0x60a8,0x24a8,0x6004,0x3498,0x6098,0x2498,0x6014,0x2488,0x5f88,0x3514,0x6114,
0x2514,0x5f98,0x3504,0x6104,0x34f4,0x60f4,0x24f4,0x5fb8,0x34e4,0x60e4,0x24e4,0x5fc8,
0x24d4,0x5fd8,0x34c4,0x60c4,0x24c4,0x5fe8,0x34b4,0x60b4,0x34a4,0x60a4,0x24a4,0x6008,
0x3494,0x6094,0x2494,0x6018,0x2484,0x5f8c,0x3510,0x6110,0x2510,0x5f9c,0x3500,0x6100,
0x34f0,0x60f0,0x24f0,0x5fbc,0x34e0,0x60e0,0x24e0,0x5fcc,0x24d0,0x5fdc,0x34c0,0x60c0,
0x24c0,0x5fec,0x34b0,0x60b0,0x34a0,0x60a0,0x24a0,0x600c,0x3490,0x6090,0x2490,0x601c,
0x3080,0x5a80,0x421c,0x4e1c,0x321c,0x5a90,0x420c,0x4e0c,0x41fc,0x4dfc,0x31fc,0x5ab0,
0x41ec,0x4dec,0x31ec,0x5ac0,0x31dc,0x5ad0,0x41cc,0x4dcc,0x31cc,0x5ae0,0x41bc,0x4dbc,
0x41ac,0x4dac,0x31ac,0x5b00,0x419c,0x4d9c,0x319c,0x5b10,0x318c,0x5a84,0x4218,0x4e18,
0x3218,0x5a94,0x4208,0x4e08,0x41f8,0x4df8,0x31f8,0x5ab4,0x41e8,0x4de8,0x31e8,0x5ac4,
0x31d8,0x5ad4,0x41c8,0x4dc8,0x31c8,0x5ae4,0x41b8,0x4db8,0x41a8,0x4da8,0x31a8,0x5b04,
0x4198,0x4d98,0x3198,0x5b14,0x3188,0x5a88,0x4214,0x4e14,0x3214,0x5a98,0x4204,0x4e04,
0x41f4,0x4df4,0x31f4,0x5ab8,0x41e4,0x4de4,0x31e4,0x5ac8,0x31d4,0x5ad8,0x41c4,0x4dc4,
0x31c4,0x5ae8,0x41b4,0x4db4,0x41a4,0x4da4,0x31a4,0x5b08,0x4194,0x4d94,0x3194,0x5b18,
0x3184,0x5a8c,0x4210,0x4e10,0x3210,0x5a9c,0x4200,0x4e00,0x41f0,0x4df0,0x31f0,0x5abc,
0x41e0,0x4de0,0x31e0,0x5acc,0x31d0,0x5adc,0x41c0,0x4dc0,0x31c0,0x5aec,0x41b0,0x4db0,
0x41a0,0x4da0,0x31a0,0x5b0c,0x4190,0x4d90,0x3190,0x5b1c,0x3180,0x5b80,0x411c,0x4d1c,
0x311c,0x5b90,0x410c,0x4d0c,0x40fc,0x4cfc,0x30fc,0x5bb0,0x40ec,0x4cec,0x30ec,0x5bc0,
0x30dc,0x5bd0,0x40cc,0x4ccc,0x30cc,0x5be0,0x40bc,0x4cbc,0x40ac,0x4cac,0x30ac,0x5c00,
0x409c,0x4c9c,0x309c,0x5c10,0x308c,0x5b84,0x4118,0x4d18,0x3118,0x5b94,0x4108,0x4d08,
0x40f8,0x4cf8,0x30f8,0x5bb4,0x40e8,0x4ce8,0x30e8,0x5bc4,0x30d8,0x5bd4,0x40c8,0x4cc8,
0x30c8,0x5be4,0x40b8,0x4cb8,0x40a8,0x4ca8,0x30a8,0x5c04,0x4098,0x4c98,0x3098,0x5c14,
0x3088,0x5b88,0x4114,0x4d14,0x3114,0x5b98,0x4104,0x4d04,0x40f4,0x4cf4,0x30f4,0x5bb8,
0x40e4,0x4ce4,0x30e4,0x5bc8,0x30d4,0x5bd8,0x40c4,0x4cc4,0x30c4,0x5be8,0x40b4,0x4cb4,
0x40a4,0x4ca4,0x30a4,0x5c08,0x4094,0x4c94,0x3094,0x5c18,0x3084,0x5b8c,0x4110,0x4d10,
0x3110,0x5b9c,0x4100,0x4d00,0x40f0,0x4cf0,0x30f0,0x5bbc,0x40e0,0x4ce0,0x30e0,0x5bcc,
0x30d0,0x5bdc,0x40c0,0x4cc0,0x30c0,0x5bec,0x40b0,0x4cb0,0x40a0,0x4ca0,0x30a0,0x5c0c,
0x4090,0x4c90,0x3090,0x5c1c,0x2c80,0x5680,0x3e1c,0x4a1c,0x2e1c,0x5690,0x3e0c,0x4a0c,
0x3dfc,0x49fc,0x2dfc,0x56b0,0x3dec,0x49ec,0x2dec,0x56c0,0x2ddc,0x56d0,0x3dcc,0x49cc,
0x2dcc,0x56e0,0x3dbc,0x49bc,0x3dac,0x49ac,0x2dac,0x5700,0x3d9c,0x499c,0x2d9c,0x5710,
0x2d8c,0x5684,0x3e18,0x4a18,0x2e18,0x5694,0x3e08,0x4a08,0x3df8,0x49f8,0x2df8,0x56b4,
0x3de8,0x49e8,0x2de8,0x56c4,0x2dd8,0x56d4,0x3dc8,0x49c8,0x2dc8,0x56e4,0x3db8,0x49b8,
0x3da8,0x49a8,0x2da8,0x5704,0x3d98,0x4998,0x2d98,0x5714,0x2d88,0x5688,0x3e14,0x4a14,
0x2e14,0x5698,0x3e04,0x4a04,0x3df4,0x49f4,0x2df4,0x56b8,0x3de4,0x49e4,0x2de4,0x56c8,
0x2dd4,0x56d8,0x3dc4,0x49c4,0x2dc4,0x56e8,0x3db4,0x49b4,0x3da4,0x49a4,0x2da4,0x5708,
0x3d94,0x4994,0x2d94,0x5718,0x2d84,0x568c,0x3e10,0x4a10,0x2e10,0x569c,0x3e00,0x4a00,
0x3df0,0x49f0,0x2df0,0x56bc,0x3de0,0x49e0,0x2de0,0x56cc,0x2dd0,0x56dc,0x3dc0,0x49c0,
0x2dc0,0x56ec,0x3db0,0x49b0,0x3da0,0x49a0,0x2da0,0x570c,0x3d90,0x4990,0x2d90,0x571c,
0x2d80,0x5780,0x3d1c,0x491c,0x2d1c,0x5790,0x3d0c,0x490c,0x3cfc,0x48fc,0x2cfc,0x57b0,
0x3cec,0x48ec,0x2cec,0x57c0,0x2cdc,0x57d0,0x3ccc,0x48cc,0x2ccc,0x57e0,0x3cbc,0x48bc,
0x3cac,0x48ac,0x2cac,0x5800,0x3c9c,0x489c,0x2c9c,0x5810,0x2c8c,0x5784,0x3d18,0x4918,
0x2d18,0x5794,0x3d08,0x4908,0x3cf8,0x48f8,0x2cf8,0x57b4,0x3ce8,0x48e8,0x2ce8,0x57c4,
0x2cd8,0x57d4,0x3cc8,0x48c8,0x2cc8,0x57e4,0x3cb8,0x48b8,0x3ca8,0x48a8,0x2ca8,0x5804,
0x3c98,0x4898,0x2c98,0x5814,0x2c88,0x5788,0x3d14,0x4914,0x2d14,0x5798,0x3d04,0x4904,
0x3cf4,0x48f4,0x2cf4,0x57b8,0x3ce4,0x48e4,0x2ce4,0x57c8,0x2cd4,0x57d8,0x3cc4,0x48c4,
0x2cc4,0x57e8,0x3cb4,0x48b4,0x3ca4,0x48a4,0x2ca4,0x5808,0x3c94,0x4894,0x2c94,0x5818,
0x2c84,0x578c,0x3d10,0x4910,0x2d10,0x579c,0x3d00,0x4900,0x3cf0,0x48f0,0x2cf0,0x57bc,
0x3ce0,0x48e0,0x2ce0,0x57cc,0x2cd0,0x57dc,0x3cc0,0x48c0,0x2cc0,0x57ec,0x3cb0,0x48b0,
0x3ca0,0x48a0,0x2ca0,0x580c,0x3c90,0x4890,0x2c90,0x581c,0x2880,0x5280,0x3a1c,0x461c,
0x2a1c,0x5290,0x3a0c,0x460c,0x39fc,0x45fc,0x29fc,0x52b0,0x39ec,0x45ec,0x29ec,0x52c0,
0x29dc,0x52d0,0x39cc,0x45cc,0x29cc,0x52e0,0x39bc,0x45bc,0x39ac,0x45ac,0x29ac,0x5300,
0x399c,0x459c,0x299c,0x5310,0x298c,0x5284,0x3a18,0x4618,0x2a18,0x5294,0x3a08,0x4608,
0x39f8,0x45f8,0x29f8,0x52b4,0x39e8,0x45e8,0x29e8,0x52c4,0x29d8,0x52d4,0x39c8,0x45c8,
0x29c8,0x52e4,0x39b8,0x45b8,0x39a8,0x45a8,0x29a8,0x5304,0x3998,0x4598,0x2998,0x5314,
0x2988,0x5288,0x3a14,0x4614,0x2a14,0x5298,0x3a04,0x4604,0x39f4,0x45f4,0x29f4,0x52b8,
0x39e4,0x45e4,0x29e4,0x52c8,0x29d4,0x52d8,0x39c4,0x45c4,0x29c4,0x52e8,0x39b4,0x45b4,
0x39a4,0x45a4,0x29a4,0x5308,0x3994,0x4594,0x2994,0x5318,0x2984,0x528c,0x3a10,0x4610,
0x2a10,0x529c,0x3a00,0x4600,0x39f0,0x45f0,0x29f0,0x52bc,0x39e0,0x45e0,0x29e0,0x52cc,
0x29d0,0x52dc,0x39c0,0x45c0,0x29c0,0x52ec,0x39b0,0x45b0,0x39a0,0x45a0,0x29a0,0x530c,
0x3990,0x4590,0x2990,0x531c,0x2980,0x5380,0x391c,0x451c,0x291c,0x5390,0x390c,0x450c,
0x38fc,0x44fc,0x28fc,0x53b0,0x38ec,0x44ec,0x28ec,0x53c0,0x28dc,0x53d0,0x38cc,0x44cc,
0x28cc,0x53e0,0x38bc,0x44bc,0x38ac,0x44ac,0x28ac,0x5400,0x389c,0x449c,0x289c,0x5410,
0x288c,0x5384,0x3918,0x4518,0x2918,0x5394,0x3908,0x4508,0x38f8,0x44f8,0x28f8,0x53b4,
0x38e8,0x44e8,0x28e8,0x53c4,0x28d8,0x53d4,0x38c8,0x44c8,0x28c8,0x53e4,0x38b8,0x44b8,
0x38a8,0x44a8,0x28a8,0x5404,0x3898,0x4498,0x2898,0x5414,0x2888,0x5388,0x3914,0x4514,
0x2914,0x5398,0x3904,0x4504,0x38f4,0x44f4,0x28f4,0x53b8,0x38e4,0x44e4,0x28e4,0x53c8,
0x28d4,0x53d8,0x38c4,0x44c4,0x28c4,0x53e8,0x38b4,0x44b4,0x38a4,0x44a4,0x28a4,0x5408,
0x3894,0x4494,0x2894,0x5418,0x2884,0x538c,0x3910,0x4510,0x2910,0x539c,0x3900,0x4500,
0x38f0,0x44f0,0x28f0,0x53bc,0x38e0,0x44e0,0x28e0,0x53cc,0x28d0,0x53dc,0x38c0,0x44c0,
0x28c0,0x53ec,0x38b0,0x44b0,0x38a0,0x44a0,0x28a0,0x540c,0x3890,0x4490,0x2890,0x541c,
0x22a0,0x4ea0,0x260c,0x5ea0,0x32c0,0x51dc,0x35dc,0x61dc,0x22f0,0x4ef0,0x25bc,0x5ef0,
0x3310,0x518c,0x358c,0x618c,0x22a4,0x4ea4,0x2608,0x5ea4,0x32c4,0x51d8,0x35d8,0x61d8,
0x22f4,0x4ef4,0x25b8,0x5ef4,0x3314,0x5188,0x3588,0x6188,0x22a8,0x4ea8,0x2604,0x5ea8,
0x32c8,0x51d4,0x35d4,0x61d4,0x22f8,0x4ef8,0x25b4,0x5ef8,0x3318,0x5184,0x3584,0x6184,
0x22ac,0x4eac,0x2600,0x5eac,0x32cc,0x51d0,0x35d0,0x61d0,0x22fc,0x4efc,0x25b0,0x5efc,
0x331c,0x5180,0x3580,0x6180,0x23a0,0x4fa0,0x250c,0x5fa0,0x33c0,0x50dc,0x34dc,0x60dc,
0x23f0,0x4ff0,0x24bc,0x5ff0,0x3410,0x508c,0x348c,0x608c,0x23a4,0x4fa4,0x2508,0x5fa4,
0x33c4,0x50d8,0x34d8,0x60d8,0x23f4,0x4ff4,0x24b8,0x5ff4,0x3414,0x5088,0x3488,0x6088,
0x23a8,0x4fa8,0x2504,0x5fa8,0x33c8,0x50d4,0x34d4,0x60d4,0x23f8,0x4ff8,0x24b4,0x5ff8,
0x3418,0x5084,0x3484,0x6084,0x23ac,0x4fac,0x2500,0x5fac,0x33cc,0x50d0,0x34d0,0x60d0,
0x23fc,0x4ffc,0x24b0,0x5ffc,0x341c,0x5080,0x3480,0x6080,0x26a0,0x42a0,0x320c,0x5aa0,
0x36c0,0x55dc,0x41dc,0x4ddc,0x26f0,0x42f0,0x31bc,0x5af0,0x3710,0x558c,0x418c,0x4d8c,
0x26a4,0x42a4,0x3208,0x5aa4,0x36c4,0x55d8,0x41d8,0x4dd8,0x26f4,0x42f4,0x31b8,0x5af4,
0x3714,0x5588,0x4188,0x4d88,0x26a8,0x42a8,0x3204,0x5aa8,0x36c8,0x55d4,0x41d4,0x4dd4,
0x26f8,0x42f8,0x31b4,0x5af8,0x3718,0x5584,0x4184,0x4d84,0x26ac,0x42ac,0x3200,0x5aac,
0x36cc,0x55d0,0x41d0,0x4dd0,0x26fc,0x42fc,0x31b0,0x5afc,0x371c,0x5580,0x4180,0x4d80,
0x27a0,0x43a0,0x310c,0x5ba0,0x37c0,0x54dc,0x40dc,0x4cdc,0x27f0,0x43f0,0x30bc,0x5bf0,
0x3810,0x548c,0x408c,0x4c8c,0x27a4,0x43a4,0x3108,0x5ba4,0x37c4,0x54d8,0x40d8,0x4cd8,
0x27f4,0x43f4,0x30b8,0x5bf4,0x3814,0x5488,0x4088,0x4c88,0x27a8,0x43a8,0x3104,0x5ba8,
0x37c8,0x54d4,0x40d4,0x4cd4,0x27f8,0x43f8,0x30b4,0x5bf8,0x3818,0x5484,0x4084,0x4c84,
0x27ac,0x43ac,0x3100,0x5bac,0x37cc,0x54d0,0x40d0,0x4cd0,0x27fc,0x43fc,0x30b0,0x5bfc,
0x381c,0x5480,0x4080,0x4c80,0x2aa0,0x46a0,0x2e0c,0x56a0,0x3ac0,0x59dc,0x3ddc,0x49dc,
0x2af0,0x46f0,0x2dbc,0x56f0,0x3b10,0x598c,0x3d8c,0x498c,0x2aa4,0x46a4,0x2e08,0x56a4,
0x3ac4,0x59d8,0x3dd8,0x49d8,0x2af4,0x46f4,0x2db8,0x56f4,0x3b14,0x5988,0x3d88,0x4988,
0x2aa8,0x46a8,0x2e04,0x56a8,0x3ac8,0x59d4,0x3dd4,0x49d4,0x2af8,0x46f8,0x2db4,0x56f8,
0x3b18,0x5984,0x3d84,0x4984,0x2aac,0x46ac,0x2e00,0x56ac,0x3acc,0x59d0,0x3dd0,0x49d0,
0x2afc,0x46fc,0x2db0,0x56fc,0x3b1c,0x5980,0x3d80,0x4980,0x2ba0,0x47a0,0x2d0c,0x57a0,
0x3bc0,0x58dc,0x3cdc,0x48dc,0x2bf0,0x47f0,0x2cbc,0x57f0,0x3c10,0x588c,0x3c8c,0x488c,
0x2ba4,0x47a4,0x2d08,0x57a4,0x3bc4,0x58d8,0x3cd8,0x48d8,0x2bf4,0x47f4,0x2cb8,0x57f4,
0x3c14,0x5888,0x3c88,0x4888,0x2ba8,0x47a8,0x2d04,0x57a8,0x3bc8,0x58d4,0x3cd4,0x48d4,
0x2bf8,0x47f8,0x2cb4,0x57f8,0x3c18,0x5884,0x3c84,0x4884,0x2bac,0x47ac,0x2d00,0x57ac,
0x3bcc,0x58d0,0x3cd0,0x48d0,0x2bfc,0x47fc,0x2cb0,0x57fc,0x3c1c,0x5880,0x3c80,0x4880,
0x2ea0,0x4aa0,0x2a0c,0x52a0,0x3ec0,0x5ddc,0x39dc,0x45dc,0x2ef0,0x4af0,0x29bc,0x52f0,
0x3f10,0x5d8c,0x398c,0x458c,0x2ea4,0x4aa4,0x2a08,0x52a4,0x3ec4,0x5dd8,0x39d8,0x45d8,
0x2ef4,0x4af4,0x29b8,0x52f4,0x3f14,0x5d88,0x3988,0x4588,0x2ea8,0x4aa8,0x2a04,0x52a8,
0x3ec8,0x5dd4,0x39d4,0x45d4,0x2ef8,0x4af8,0x29b4,0x52f8,0x3f18,0x5d84,0x3984,0x4584,
0x2eac,0x4aac,0x2a00,0x52ac,0x3ecc,0x5dd0,0x39d0,0x45d0,0x2efc,0x4afc,0x29b0,0x52fc,
0x3f1c,0x5d80,0x3980,0x4580,0x2fa0,0x4ba0,0x290c,0x53a0,0x3fc0,0x5cdc,0x38dc,0x44dc,
0x2ff0,0x4bf0,0x28bc,0x53f0,0x4010,0x5c8c,0x388c,0x448c,0x2fa4,0x4ba4,0x2908,0x53a4,
0x3fc4,0x5cd8,0x38d8,0x44d8,0x2ff4,0x4bf4,0x28b8,0x53f4,0x4014,0x5c88,0x3888,0x4488,
0x2fa8,0x4ba8,0x2904,0x53a8,0x3fc8,0x5cd4,0x38d4,0x44d4,0x2ff8,0x4bf8,0x28b4,0x53f8,
0x4018,0x5c84,0x3884,0x4484,0x2fac,0x4bac,0x2900,0x53ac,0x3fcc,0x5cd0,0x38d0,0x44d0,
0x2ffc,0x4bfc,0x28b0,0x53fc,0x401c,0x5c80,0x3880,0x4480,0x1400,0x1574,0x1570,0x156c,
0x1550,0x1544,0x1540,0x153c,0x1520,0x1514,0x1510,0x150c,0x14f0,0x14e4,0x14e0,0x14dc,
0x14c0,0x14b4,0x14b0,0x14ac,0x1490,0x1484,0x1480,0x147c,0x1460,0x1454,0x1450,0x144c,
0x1430,0x1424,0x1420,0x141c,0x1280,0x13f4,0x13f0,0x13ec,0x13d0,0x13c4,0x13c0,0x13bc,
0x13a0,0x1394,0x1390,0x138c,0x1370,0x1364,0x1360,0x135c,0x1340,0x1334,0x1330,0x132c,
0x1310,0x1304,0x1300,0x12fc,0x12e0,0x12d4,0x12d0,0x12cc,0x12b0,0x12a4,0x12a0,0x129c,
0x1568,0x1564,0x1560,0x155c,0x1538,0x1534,0x1530,0x152c,0x1508,0x1504,0x1500,0x14fc,
0x14d8,0x14d4,0x14d0,0x14cc,0x14a8,0x14a4,0x14a0,0x149c,0x1478,0x1474,0x1470,0x146c,
0x1448,0x1444,0x1440,0x143c,0x1418,0x1414,0x1410,0x140c,0x13e8,0x13e4,0x13e0,0x13dc,
0x13b8,0x13b4,0x13b0,0x13ac,0x1388,0x1384,0x1380,0x137c,0x1358,0x1354,0x1350,0x134c,
0x1328,0x1324,0x1320,0x131c,0x12f8,0x12f4,0x12f0,0x12ec,0x12c8,0x12c4,0x12c0,0x12bc,
0x1298,0x1294,0x1290,0x128c,0x1558,0x1554,0x1280,0x1280,0x1528,0x1524,0x1280,0x1280,
0x14f8,0x14f4,0x1280,0x1280,0x14c8,0x14c4,0x1280,0x1280,0x1498,0x1494,0x1280,0x1280,
0x1468,0x1464,0x1280,0x1280,0x1438,0x1434,0x1280,0x1280,0x1408,0x1404,0x1280,0x1280,
0x13d8,0x13d4,0x1280,0x1280,0x13a8,0x13a4,0x1280,0x1280,0x1378,0x1374,0x1280,0x1280,
0x1348,0x1344,0x1280,0x1280,0x1318,0x1314,0x1280,0x1280,0x12e8,0x12e4,0x1280,0x1280,
0x12b8,0x12b4,0x1280,0x1280,0x1288,0x1284,0x1280,0x1280,0x1280,0x1280,0x1280,0x1280,
0x1280,0x1280,0x1280,0x1280,0x1280,0x1280,0x1280,0x1280,0x1280,0x1280,0x1280,0x1280,
0x1280,0x1280,0x1280,0x1280,0x1280,0x1280,0x1280,0x1280,0x1280,0x1280,0x1280,0x1280,
0x1280,0x1280,0x1280,0x1280,0x1280,0x1280,0x1280,0x1280,0x1280,0x1280,0x1280,0x1280,
0x1280,0x1280,0x1280,0x1280,0x1280,0x1280,0x1280,0x1280,0x1280,0x1280,0x1280,0x1280,
0x1280,0x1280,0x1280,0x1280,0x1280,0x1280,0x1280,0x1280,0x1280,0x1280,0x1280,0x1280,
0x1d74,0x1d70,0x1d6c,0x1d68,0x1d44,0x1d40,0x1d3c,0x1d38,0x1d14,0x1d10,0x1d0c,0x1d08,
0x1ce4,0x1ce0,0x1cdc,0x1cd8,0x1cb4,0x1cb0,0x1cac,0x1ca8,0x1c84,0x1c80,0x1c7c,0x1c78,
0x1c54,0x1c50,0x1c4c,0x1c48,0x1c24,0x1c20,0x1c1c,0x1c18,0x1bf4,0x1bf0,0x1bec,0x1be8,
0x1bc4,0x1bc0,0x1bbc,0x1bb8,0x1b94,0x1b90,0x1b8c,0x1b88,0x1b64,0x1b60,0x1b5c,0x1b58,
0x1b34,0x1b30,0x1b2c,0x1b28,0x1b04,0x1b00,0x1afc,0x1af8,0x1ad4,0x1ad0,0x1acc,0x1ac8,
0x1aa4,0x1aa0,0x1a9c,0x1a98,0x1d64,0x1d60,0x1d5c,0x1d58,0x1d34,0x1d30,0x1d2c,0x1d28,
0x1d04,0x1d00,0x1cfc,0x1cf8,0x1cd4,0x1cd0,0x1ccc,0x1cc8,0x1ca4,0x1ca0,0x1c9c,0x1c98,
0x1c74,0x1c70,0x1c6c,0x1c68,0x1c44,0x1c40,0x1c3c,0x1c38,0x1c14,0x1c10,0x1c0c,0x1c08,
0x1be4,0x1be0,0x1bdc,0x1bd8,0x1bb4,0x1bb0,0x1bac,0x1ba8,0x1b84,0x1b80,0x1b7c,0x1b78,
0x1b54,0x1b50,0x1b4c,0x1b48,0x1b24,0x1b20,0x1b1c,0x1b18,0x1af4,0x1af0,0x1aec,0x1ae8,
0x1ac4,0x1ac0,0x1abc,0x1ab8,0x1a94,0x1a90,0x1a8c,0x1a88,0x1d54,0x1d50,0x1a80,0x1a80,
0x1d24,0x1d20,0x1a80,0x1a80,0x1cf4,0x1cf0,0x1a80,0x1a80,0x1cc4,0x1cc0,0x1a80,0x1a80,
0x1c94,0x1c90,0x1a80,0x1a80,0x1c64,0x1c60,0x1a80,0x1a80,0x1c34,0x1c30,0x1a80,0x1a80,
0x1c04,0x1c00,0x1a80,0x1a80,0x1bd4,0x1bd0,0x1a80,0x1a80,0x1ba4,0x1ba0,0x1a80,0x1a80,
0x1b74,0x1b70,0x1a80,0x1a80,0x1b44,0x1b40,0x1a80,0x1a80,0x1b14,0x1b10,0x1a80,0x1a80,
0x1ae4,0x1ae0,0x1a80,0x1a80,0x1ab4,0x1ab0,0x1a80,0x1a80,0x1a84,0x1a80,0x1a80,0x1a80,
0x1a80,0x1a80,0x1a80,0x1a80,0x1a80,0x1a80,0x1a80,0x1a80,0x1a80,0x1a80,0x1a80,0x1a80,
0x1a80,0x1a80,0x1a80,0x1a80,0x1a80,0x1a80,0x1a80,0x1a80,0x1a80,0x1a80,0x1a80,0x1a80,
0x1a80,0x1a80,0x1a80,0x1a80,0x1a80,0x1a80,0x1a80,0x1a80,0x1a80,0x1a80,0x1a80,0x1a80,
0x1a80,0x1a80,0x1a80,0x1a80,0x1a80,0x1a80,0x1a80,0x1a80,0x1a80,0x1a80,0x1a80,0x1a80,
0x1a80,0x1a80,0x1a80,0x1a80,0x1a80,0x1a80,0x1a80,0x1a80,0x1a80,0x1a80,0x1a80,0x1a80,
0x1a80,0x1a80,0x1a80,0x1a80,0x2280,0x2284,0x2288,0x228c,0x2290,0x2294,0x2298,0x229c,
0x22a0,0x22a4,0x22a8,0x22ac,0x22b0,0x22b4,0x22b8,0x22bc,0x22c0,0x22c4,0x22c8,0x22cc,
0x22d0,0x22d4,0x22d8,0x22dc,0x22e0,0x22e4,0x22e8,0x22ec,0x22f0,0x22f4,0x22f8,0x22fc,
0x2300,0x2304,0x2308,0x230c,0x2310,0x2314,0x2318,0x231c,0x2280,0x2284,0x2288,0x228c,
0x2290,0x2294,0x2298,0x229c,0x22a0,0x22a4,0x22a8,0x22ac,0x22b0,0x22b4,0x22b8,0x22bc,
0x22c0,0x22c4,0x22c8,0x22cc,0x22d0,0x22d4,0x22d8,0x22dc,0x2380,0x2384,0x2388,0x238c,
0x2390,0x2394,0x2398,0x239c,0x23a0,0x23a4,0x23a8,0x23ac,0x23b0,0x23b4,0x23b8,0x23bc,
0x23c0,0x23c4,0x23c8,0x23cc,0x23d0,0x23d4,0x23d8,0x23dc,0x23e0,0x23e4,0x23e8,0x23ec,
0x23f0,0x23f4,0x23f8,0x23fc,0x2400,0x2404,0x2408,0x240c,0x2410,0x2414,0x2418,0x241c,
0x2380,0x2384,0x2388,0x238c,0x2390,0x2394,0x2398,0x239c,0x23a0,0x23a4,0x23a8,0x23ac,
0x23b0,0x23b4,0x23b8,0x23bc,0x23c0,0x23c4,0x23c8,0x23cc,0x23d0,0x23d4,0x23d8,0x23dc,
0x2280,0x238c,0x2388,0x2384,0x241c,0x2418,0x2414,0x2410,0x240c,0x2408,0x2404,0x2400,
0x23fc,0x23f8,0x23f4,0x23f0,0x23ec,0x23e8,0x23e4,0x23e0,0x23dc,0x23d8,0x23d4,0x23d0,
0x23cc,0x23c8,0x23c4,0x23c0,0x23bc,0x23b8,0x23b4,0x23b0,0x23ac,0x23a8,0x23a4,0x23a0,
0x239c,0x2398,0x2394,0x2390,0x2280,0x238c,0x2388,0x2384,0x241c,0x2418,0x2414,0x2410,
0x240c,0x2408,0x2404,0x2400,0x23fc,0x23f8,0x23f4,0x23f0,0x23ec,0x23e8,0x23e4,0x23e0,
0x23dc,0x23d8,0x23d4,0x23d0,0x2380,0x228c,0x2288,0x2284,0x231c,0x2318,0x2314,0x2310,
0x230c,0x2308,0x2304,0x2300,0x22fc,0x22f8,0x22f4,0x22f0,0x22ec,0x22e8,0x22e4,0x22e0,
0x22dc,0x22d8,0x22d4,0x22d0,0x22cc,0x22c8,0x22c4,0x22c0,0x22bc,0x22b8,0x22b4,0x22b0,
0x22ac,0x22a8,0x22a4,0x22a0,0x229c,0x2298,0x2294,0x2290,0x2380,0x228c,0x2288,0x2284,
0x231c,0x2318,0x2314,0x2310,0x230c,0x2308,0x2304,0x2300,0x22fc,0x22f8,0x22f4,0x22f0,
0x22ec,0x22e8,0x22e4,0x22e0,0x22dc,0x22d8,0x22d4,0x22d0,0x3280,0x3284,0x3288,0x328c,
0x3290,0x3294,0x3298,0x329c,0x32a0,0x32a4,0x32a8,0x32ac,0x32b0,0x32b4,0x32b8,0x32bc,
0x32c0,0x32c4,0x32c8,0x32cc,0x32d0,0x32d4,0x32d8,0x32dc,0x32e0,0x32e4,0x32e8,0x32ec,
0x32f0,0x32f4,0x32f8,0x32fc,0x3300,0x3304,0x3308,0x330c,0x3310,0x3314,0x3318,0x331c,
0x3280,0x3284,0x3288,0x328c,0x3290,0x3294,0x3298,0x329c,0x32a0,0x32a4,0x32a8,0x32ac,
0x32b0,0x32b4,0x32b8,0x32bc,0x32c0,0x32c4,0x32c8,0x32cc,0x32d0,0x32d4,0x32d8,0x32dc,
0x3380,0x3384,0x3388,0x338c,0x3390,0x3394,0x3398,0x339c,0x33a0,0x33a4,0x33a8,0x33ac,
0x33b0,0x33b4,0x33b8,0x33bc,0x33c0,0x33c4,0x33c8,0x33cc,0x33d0,0x33d4,0x33d8,0x33dc,
0x33e0,0x33e4,0x33e8,0x33ec,0x33f0,0x33f4,0x33f8,0x33fc,0x3400,0x3404,0x3408,0x340c,
0x3410,0x3414,0x3418,0x341c,0x3380,0x3384,0x3388,0x338c,0x3390,0x3394,0x3398,0x339c,
0x33a0,0x33a4,0x33a8,0x33ac,0x33b0,0x33b4,0x33b8,0x33bc,0x33c0,0x33c4,0x33c8,0x33cc,
0x33d0,0x33d4,0x33d8,0x33dc,0x341c,0x3418,0x3414,0x3410,0x340c,0x3408,0x3404,0x3400,
0x33fc,0x33f8,0x33f4,0x33f0,0x33ec,0x33e8,0x33e4,0x33e0,0x33dc,0x33d8,0x33d4,0x33d0,
0x33cc,0x33c8,0x33c4,0x33c0,0x33bc,0x33b8,0x33b4,0x33b0,0x33ac,0x33a8,0x33a4,0x33a0,
0x339c,0x3398,0x3394,0x3390,0x338c,0x3388,0x3384,0x3380,0x341c,0x3418,0x3414,0x3410,
0x340c,0x3408,0x3404,0x3400,0x33fc,0x33f8,0x33f4,0x33f0,0x33ec,0x33e8,0x33e4,0x33e0,
0x33dc,0x33d8,0x33d4,0x33d0,0x33cc,0x33c8,0x33c4,0x33c0,0x331c,0x3318,0x3314,0x3310,
0x330c,0x3308,0x3304,0x3300,0x32fc,0x32f8,0x32f4,0x32f0,0x32ec,0x32e8,0x32e4,0x32e0,
0x32dc,0x32d8,0x32d4,0x32d0,0x32cc,0x32c8,0x32c4,0x32c0,0x32bc,0x32b8,0x32b4,0x32b0,
0x32ac,0x32a8,0x32a4,0x32a0,0x329c,0x3298,0x3294,0x3290,0x328c,0x3288,0x3284,0x3280,
0x331c,0x3318,0x3314,0x3310,0x330c,0x3308,0x3304,0x3300,0x32fc,0x32f8,0x32f4,0x32f0,
0x32ec,0x32e8,0x32e4,0x32e0,0x32dc,0x32d8,0x32d4,0x32d0,0x32cc,0x32c8,0x32c4,0x32c0,
0x2480,0x258c,0x2588,0x2584,0x261c,0x2618,0x2614,0x2610,0x260c,0x2608,0x2604,0x2600,
0x25fc,0x25f8,0x25f4,0x25f0,0x25ec,0x25e8,0x25e4,0x25e0,0x25dc,0x25d8,0x25d4,0x25d0,
0x25cc,0x25c8,0x25c4,0x25c0,0x25bc,0x25b8,0x25b4,0x25b0,0x25ac,0x25a8,0x25a4,0x25a0,
0x259c,0x2598,0x2594,0x2590,0x2480,0x258c,0x2588,0x2584,0x261c,0x2618,0x2614,0x2610,
0x260c,0x2608,0x2604,0x2600,0x25fc,0x25f8,0x25f4,0x25f0,0x25ec,0x25e8,0x25e4,0x25e0,
0x25dc,0x25d8,0x25d4,0x25d0,0x2580,0x248c,0x2488,0x2484,0x251c,0x2518,0x2514,0x2510,
0x250c,0x2508,0x2504,0x2500,0x24fc,0x24f8,0x24f4,0x24f0,0x24ec,0x24e8,0x24e4,0x24e0,
0x24dc,0x24d8,0x24d4,0x24d0,0x24cc,0x24c8,0x24c4,0x24c0,0x24bc,0x24b8,0x24b4,0x24b0,
0x24ac,0x24a8,0x24a4,0x24a0,0x249c,0x2498,0x2494,0x2490,0x2580,0x248c,0x2488,0x2484,
0x251c,0x2518,0x2514,0x2510,0x250c,0x2508,0x2504,0x2500,0x24fc,0x24f8,0x24f4,0x24f0,
0x24ec,0x24e8,0x24e4,0x24e0,0x24dc,0x24d8,0x24d4,0x24d0,0x2480,0x2484,0x2488,0x248c,
0x2490,0x2494,0x2498,0x249c,0x24a0,0x24a4,0x24a8,0x24ac,0x24b0,0x24b4,0x24b8,0x24bc,
0x24c0,0x24c4,0x24c8,0x24cc,0x24d0,0x24d4,0x24d8,0x24dc,0x24e0,0x24e4,0x24e8,0x24ec,
0x24f0,0x24f4,0x24f8,0x24fc,0x2500,0x2504,0x2508,0x250c,0x2510,0x2514,0x2518,0x251c,
0x2480,0x2484,0x2488,0x248c,0x2490,0x2494,0x2498,0x249c,0x24a0,0x24a4,0x24a8,0x24ac,
0x24b0,0x24b4,0x24b8,0x24bc,0x24c0,0x24c4,0x24c8,0x24cc,0x24d0,0x24d4,0x24d8,0x24dc,
0x2580,0x2584,0x2588,0x258c,0x2590,0x2594,0x2598,0x259c,0x25a0,0x25a4,0x25a8,0x25ac,
0x25b0,0x25b4,0x25b8,0x25bc,0x25c0,0x25c4,0x25c8,0x25cc,0x25d0,0x25d4,0x25d8,0x25dc,
0x25e0,0x25e4,0x25e8,0x25ec,0x25f0,0x25f4,0x25f8,0x25fc,0x2600,0x2604,0x2608,0x260c,
0x2610,0x2614,0x2618,0x261c,0x2580,0x2584,0x2588,0x258c,0x2590,0x2594,0x2598,0x259c,
0x25a0,0x25a4,0x25a8,0x25ac,0x25b0,0x25b4,0x25b8,0x25bc,0x25c0,0x25c4,0x25c8,0x25cc,
0x25d0,0x25d4,0x25d8,0x25dc,0x361c,0x3618,0x3614,0x3610,0x360c,0x3608,0x3604,0x3600,
0x35fc,0x35f8,0x35f4,0x35f0,0x35ec,0x35e8,0x35e4,0x35e0,0x35dc,0x35d8,0x35d4,0x35d0,
0x35cc,0x35c8,0x35c4,0x35c0,0x35bc,0x35b8,0x35b4,0x35b0,0x35ac,0x35a8,0x35a4,0x35a0,
0x359c,0x3598,0x3594,0x3590,0x358c,0x3588,0x3584,0x3580,0x361c,0x3618,0x3614,0x3610,
0x360c,0x3608,0x3604,0x3600,0x35fc,0x35f8,0x35f4,0x35f0,0x35ec,0x35e8,0x35e4,0x35e0,
0x35dc,0x35d8,0x35d4,0x35d0,0x35cc,0x35c8,0x35c4,0x35c0,0x351c,0x3518,0x3514,0x3510,
0x350c,0x3508,0x3504,0x3500,0x34fc,0x34f8,0x34f4,0x34f0,0x34ec,0x34e8,0x34e4,0x34e0,
0x34dc,0x34d8,0x34d4,0x34d0,0x34cc,0x34c8,0x34c4,0x34c0,0x34bc,0x34b8,0x34b4,0x34b0,
0x34ac,0x34a8,0x34a4,0x34a0,0x349c,0x3498,0x3494,0x3490,0x348c,0x3488,0x3484,0x3480,
0x351c,0x3518,0x3514,0x3510,0x350c,0x3508,0x3504,0x3500,0x34fc,0x34f8,0x34f4,0x34f0,
0x34ec,0x34e8,0x34e4,0x34e0,0x34dc,0x34d8,0x34d4,0x34d0,0x34cc,0x34c8,0x34c4,0x34c0,
0x3480,0x3484,0x3488,0x348c,0x3490,0x3494,0x3498,0x349c,0x34a0,0x34a4,0x34a8,0x34ac,
0x34b0,0x34b4,0x34b8,0x34bc,0x34c0,0x34c4,0x34c8,0x34cc,0x34d0,0x34d4,0x34d8,0x34dc,
0x34e0,0x34e4,0x34e8,0x34ec,0x34f0,0x34f4,0x34f8,0x34fc,0x3500,0x3504,0x3508,0x350c,
0x3510,0x3514,0x3518,0x351c,0x3480,0x3484,0x3488,0x348c,0x3490,0x3494,0x3498,0x349c,
0x34a0,0x34a4,0x34a8,0x34ac,0x34b0,0x34b4,0x34b8,0x34bc,0x34c0,0x34c4,0x34c8,0x34cc,
0x34d0,0x34d4,0x34d8,0x34dc,0x3580,0x3584,0x3588,0x358c,0x3590,0x3594,0x3598,0x359c,
0x35a0,0x35a4,0x35a8,0x35ac,0x35b0,0x35b4,0x35b8,0x35bc,0x35c0,0x35c4,0x35c8,0x35cc,
0x35d0,0x35d4,0x35d8,0x35dc,0x35e0,0x35e4,0x35e8,0x35ec,0x35f0,0x35f4,0x35f8,0x35fc,
0x3600,0x3604,0x3608,0x360c,0x3610,0x3614,0x3618,0x361c,0x3580,0x3584,0x3588,0x358c,
0x3590,0x3594,0x3598,0x359c,0x35a0,0x35a4,0x35a8,0x35ac,0x35b0,0x35b4,0x35b8,0x35bc,
0x35c0,0x35c4,0x35c8,0x35cc,0x35d0,0x35d4,0x35d8,0x35dc,
};

#define STAMPS 0
#define OUT_AUX 2
#define G_PF 0
#define G_P2 11
#define P2_N 28
#define G_UF 6
#define G_VF 3
#define P2_FIRST 1
#define PM_AT 1
#define P2_PRIO 3
#define T_FIRST 0
#define D_T 0
#define LATE_T 2
#define PW_AT 0
#define P2_AT 0
#define G2AT 10
#define G2C C_5
#define FPOS 0
#define DPOS 4
#define TLBW 0
#define PACE 0
#define PROWS 2
#define PP 13300
#define EARLY_OUT 0
#define D_OUT 0
#define RK_EARLY 0
#define R1 1
#define R2 1
#define DRU 1
#define DRV 1
#define EARLY_A 0
#define XMAP 0
#define X5 6
#define G3B 0
#define GUB 1
#define PL_S 0
#define PL_A 0
#define PL_R 0
#define PL_U 0
#define PL_V 0
#define COAL 1
#define COAL2 0
#define G1X 1
#define G1S 2
#define CODE_L (STAMPS ? 29 : 15)
#define G1M 0
#define WPRIO 1
#define NEWW 1
#define P1I 1
#define VFE 3
#define FINE 0
#define NEWP 0
#define P3I 1
#define PF2L 0
#define P2U 20
constexpr int P2U_ = P2U;
#define G2S 16
#define DNO 0
#define T_AC 190
#define T_DS 100
#define T_G1 400
#define T_U 0
#define T_G1D 55
#define GUD 0
#define D_G1L 4
#define KW 0
#define KPRI 0
#define VPOS 1
#define UPOS 0
#define D_HB 1
#define D_G1 22
#define D_U 0
#define D_V 5
#define D_RK 0
#define D_SL 8
#define D_AC 15
#define D_FFN 0
#define D_G2 0
#define D_P2 0
#define D_VF 0
#define D_G3A 0
#define D_UF 0
#define HPRIO 3
#define PF_W 1
#define PF_F 1
#define PF_N 4
#define D_PFW 0
#define FF1 8
#define FF2 20
#define G4AT 5
#define PFA 10
#define PFB 10
#define D_G3 10
#define CODE_PF 3

constexpr int NT = 768;
constexpr int WS_E = 0;
constexpr int WS_G1 = 4096;
constexpr int WS_G3 = 167936;
constexpr int WS_G4 = 176128;
constexpr int WS_G2 = 192512;
constexpr int WS_RK = 274432;
constexpr int WS_SLOT = 339968;
constexpr int SLOT_B = 2688;
constexpr int WS_ACT = 425984;
constexpr int WS_GS = 536576;
constexpr int WS_U = 589824;
constexpr int WS_V = WS_U + 2621440;
constexpr int WS_END = WS_V + 2621440;
constexpr int WS_DBG = 589824 + 2 * 4194304;
constexpr int L_CNT = 0;
constexpr int L_RANK = 32;
constexpr int L_MISC = 64;
constexpr int L_ST1 = 80;
constexpr int L_DUMP = 160;
constexpr int L_PA = 224;
constexpr int L_ST2 = 544;
constexpr int L_SC = 864;
constexpr int L_SP = 1184;
constexpr int L_A = 2208;
constexpr int L_B = 7328;
constexpr int L_X = 12448;
constexpr int L_T = 15008;
constexpr int L_TP2 = L_T + 6656;
constexpr int L_W = 23712;
constexpr int L_PAR = 26272;
constexpr int L_MW = 29472;
constexpr int L_SYM = L_MW;
constexpr int L_FAC = 39712;
constexpr int L_END = 39968;
static_assert(L_END * 4 <= 163840 && TL_SP == L_SP && TL_A == L_A && TL_B == L_B, "LDS");
enum { C_MOD, C_SM, C_SMV, C_X, C_PB, C_TAG, C_4, C_U, C_G, C_W, C_VF, C_5, C_6, C_PAR, C_7, C_8, C_9, C_R, C_MW, C_10, C_F, C_T };
constexpr u32 SENTH = 0xFDFFFDFFu;
constexpr u32 OOB = 0x80000000u;
constexpr u32 SENT = 0xFFFFFFFFu;
#define CAPSH 16
constexpr u32 SPIN_CAP = 1u << CAPSH;
constexpr u32 LDS_CAP = 1u << (CAPSH + 8);

#define DI __device__ __forceinline__
DI f4 mfma_h(u4 a, u4 b, f4 c) { return __builtin_amdgcn_mfma_f32_16x16x32_f16(__builtin_bit_cast(hh8, a), __builtin_bit_cast(hh8, b), c, 0, 0, 0); }
DI f4 mfma_b(u4 a, u4 b, f4 c) { return __builtin_amdgcn_mfma_f32_16x16x32_bf16(__builtin_bit_cast(bb8, a), __builtin_bit_cast(bb8, b), c, 0, 0, 0); }
DI u32 pk_h(float a, float b) { f2 v = {a, b}; return __builtin_bit_cast(u32, __builtin_convertvector(v, hh2)); }
DI u32 pk_b(float a, float b) { f2 v = {a, b}; return __builtin_bit_cast(u32, __builtin_convertvector(v, bb2)); }
DI float asf(u32 v) { return __builtin_bit_cast(float, v); }
DI float blo(u32 v) { return __builtin_bit_cast(float, v << 16); }
DI float bhi(u32 v) { return __builtin_bit_cast(float, v & 0xFFFF0000u); }
DI float bfr(float a) { return blo(pk_b(a, a)); }
DI f2 bfr2(f2 a) { u32 p = pk_b(a.x, a.y); return f2{blo(p), bhi(p)}; }
DI f4 bfr4(f4 a) { u32 p = pk_b(a.x, a.y), s = pk_b(a.z, a.w); return f4{blo(p), bhi(p), blo(s), bhi(s)}; }
DI f4 unp4(u2 v) { return f4{blo(v.x), bhi(v.x), blo(v.y), bhi(v.y)}; }
DI u2 pk4(f4 a) { return u2{pk_b(a.x, a.y), pk_b(a.z, a.w)}; }
DI float hlo(u32 v) { return (float)__builtin_bit_cast(hh2, v).x; }
DI float hhi(u32 v) { return (float)__builtin_bit_cast(hh2, v).y; }
DI u4 packc(f4 r, f4 i) { return u4{pk_h(r.x, i.x), pk_h(r.y, i.y), pk_h(r.z, i.z), pk_h(r.w, i.w)}; }
DI u32 nos(u32 v) { return v < 0xFFFFFFFEu ? v : 0xFFFFFFFEu; }
DI u4 nos4(u4 v) { return u4{nos(v.x), nos(v.y), nos(v.z), nos(v.w)}; }
DI u2 nos2(u2 v) { return u2{nos(v.x), nos(v.y)}; }
DI u32 mx(u32 a, u32 b) { return a > b ? a : b; }
DI u32 mx4(u4 v) { return mx(mx(v.x, v.y), mx(v.z, v.w)); }
DI void cmul(f4& r, f4& i, f4 wr, f4 wi) { f4 a = r * wr - i * wi, b = r * wi + i * wr; r = a; i = b; }
DI void cmulc(f4& r, f4& i, f4 wr, f4 wi) { f4 a = r * wr + i * wi, b = i * wr - r * wi; r = a; i = b; }
#define tab4(off, l) (*(const u4*)(lds + L_T + (off) + 4 * (l)))
#define tab4f(off, l) (*(const f4*)(lds + L_T + (off) + 4 * (l)))
template <class P> DI void glds16(P src, u32 dst) {
  u32 keep;
  asm volatile("s_mov_b32 %0, m0\n\ts_mov_b32 m0, %2\n\ts_nop 0\n\tglobal_load_lds_dwordx4 %1, off\n\ts_mov_b32 m0, %0" : "=&s"(keep) : "v"(src), "s"(dst) : "memory");
}
#define BAR() do { asm volatile("s_waitcnt lgkmcnt(0)" ::: "memory"); __builtin_amdgcn_s_barrier(); asm volatile("" ::: "memory"); } while (0)
template <int CTRL> DI float dppf(float v) { return __builtin_bit_cast(float, __builtin_amdgcn_update_dpp(0, __builtin_bit_cast(int, v), CTRL, 0xF, 0xF, true)); }
template <int CTRL> DI u32 dppu(u32 v) { return (u32)__builtin_amdgcn_update_dpp(0, (int)v, CTRL, 0xF, 0xF, true); }
constexpr int ROR8 = 0x128, ROR4 = 0x124, ROR2 = 0x122, ROR1 = 0x121, QX1 = 0xB1;
DI float row_sum(float v) { v += dppf<ROR8>(v); v += dppf<ROR4>(v); v += dppf<ROR2>(v); v += dppf<ROR1>(v); return v; }
DI float sw32(float a, float b) {
  auto r = __builtin_amdgcn_permlane32_swap(__builtin_bit_cast(u32, a), __builtin_bit_cast(u32, b), false, false);
  return __builtin_bit_cast(float, (u32)r[0]) + __builtin_bit_cast(float, (u32)r[1]);
}
DI float sw16(float a, float b) {
  auto r = __builtin_amdgcn_permlane16_swap(__builtin_bit_cast(u32, a), __builtin_bit_cast(u32, b), false, false);
  return __builtin_bit_cast(float, (u32)r[0]) + __builtin_bit_cast(float, (u32)r[1]);
}
DI u32 wave_max(u32 v) {
  v = mx(v, dppu<ROR8>(v)); v = mx(v, dppu<ROR4>(v)); v = mx(v, dppu<ROR2>(v)); v = mx(v, dppu<ROR1>(v));
  u32 a = __builtin_amdgcn_readlane(v, 0), b = __builtin_amdgcn_readlane(v, 16), c = __builtin_amdgcn_readlane(v, 32), d = __builtin_amdgcn_readlane(v, 48);
  return mx(mx(a, b), mx(c, d));
}
DI float wave_sum(float v) {
  v = row_sum(v);
  float a = __builtin_bit_cast(float, __builtin_amdgcn_readlane(__builtin_bit_cast(int, v), 0)), b = __builtin_bit_cast(float, __builtin_amdgcn_readlane(__builtin_bit_cast(int, v), 16));
  float c = __builtin_bit_cast(float, __builtin_amdgcn_readlane(__builtin_bit_cast(int, v), 32)), d = __builtin_bit_cast(float, __builtin_amdgcn_readlane(__builtin_bit_cast(int, v), 48));
  return (a + b) + (c + d);
}
DI float rdl(float v, int lane) { return __builtin_bit_cast(float, __builtin_amdgcn_readlane(__builtin_bit_cast(int, v), lane)); }
DI void wave_sync() { __builtin_amdgcn_fence(__ATOMIC_RELEASE, "wavefront"); __builtin_amdgcn_wave_barrier(); __builtin_amdgcn_fence(__ATOMIC_ACQUIRE, "wavefront"); }
#define DRAIN() asm volatile("s_waitcnt vmcnt(0)" ::: "memory")
#define SCHED() __builtin_amdgcn_sched_barrier(0)
DI u32 ldx1(rsrc_t r, u32 o) { return __builtin_amdgcn_raw_buffer_load_b32(r, o, 0, 16); }
DI u2 ldx2(rsrc_t r, u32 o) { return __builtin_amdgcn_raw_buffer_load_b64(r, o, 0, 16); }
DI u4 ldx4(rsrc_t r, u32 o) { return __builtin_amdgcn_raw_buffer_load_b128(r, o, 0, 16); }
DI void stx1(rsrc_t r, u32 o, u32 v) { __builtin_amdgcn_raw_buffer_store_b32(v, r, o, 0, 16); }
DI void stx2(rsrc_t r, u32 o, u2 v) { __builtin_amdgcn_raw_buffer_store_b64(v, r, o, 0, 16); }
DI void stx4(rsrc_t r, u32 o, u4 v) { __builtin_amdgcn_raw_buffer_store_b128(v, r, o, 0, 16); }
DI u32 ld16(rsrc_t r, u32 vo, u32 so) { return (u32)(u16)__builtin_amdgcn_raw_buffer_load_b16(r, vo, so, 0); }
DI float silu_f(float v) { return v * __builtin_amdgcn_rcpf(1.0f + __expf(-v)); }
DI float pow2i(int e) { e = e < -126 ? -126 : (e > 127 ? 127 : e); return __builtin_bit_cast(float, (u32)(e + 127) << 23); }

#define STAMP0A() do {} while (0)
#define STAMP0B() do {} while (0)
#define STAMP(i) do {} while (0)
#define STAMP_OUT() do {} while (0)
#define STAMP_INIT() do {} while (0)
#pragma clang diagnostic ignored "-Wint-to-pointer-cast"
#define AS3 __attribute__((address_space(3)))
typedef short s4v __attribute__((ext_vector_type(4)));
typedef unsigned short us2 __attribute__((ext_vector_type(2)));
template <class T> DI T ldl(u32 a) { return *(const AS3 T*)a; }
template <class T> DI void stl(u32 a, T v) { *(AS3 T*)a = v; }
DI u2 ldtr(u32 a) { return __builtin_bit_cast(u2, __builtin_amdgcn_ds_read_tr16_b64_v4i16((AS3 s4v*)a)); }
template <class P> DI void glds4(P src, u32 dst) {
  u32 keep;
  asm volatile("s_mov_b32 %0, m0\n\ts_mov_b32 m0, %2\n\ts_nop 0\n\tglobal_load_lds_dword %1, off\n\ts_mov_b32 m0, %0" : "=&s"(keep) : "v"(src), "s"(dst) : "memory");
}
#define FST(i) do {} while (0)
#define CB() asm volatile("" ::: "memory")
#define PIN(v) asm volatile("" : "+v"(v))
#define PINS(v) asm volatile("" : "+s"(v))
#define ARRIVE(c) asm volatile("s_mov_b64 exec, 1\n\tds_add_u32 %0, %0 offset:%1\n\ts_mov_b64 exec, -1" :: "v"(cv), "n"(4 * (c)) : "memory")
#define BACKOFF() asm volatile("s_setprio 0\n\ts_waitcnt lgkmcnt(0)" ::: "memory")
#define AWAIT_X(n, CNT, ...) do { int left_ = LDS_CAP; u32 thr_; bool more_; __builtin_amdgcn_s_setprio(WPRIO); \
    do { CB(); __VA_ARGS__; CB(); --left_; thr_ = left_ < 0 ? 0u : 4u * (n); PINS(left_); PINS(thr_); __builtin_amdgcn_s_waitcnt(0xC07F); \
         more_ = (u32)__builtin_amdgcn_readfirstlane(CNT) < thr_; } while (more_); \
    __builtin_amdgcn_s_setprio(2); } while (0)
#define AWAIT_RD(c, n, ...) AWAIT_X(n, cnt_, const u32 cnt_ = *(volatile AS3 u32*)(cv + 4 * (c)); CB(); __VA_ARGS__)
#define AWAIT(c, n) AWAIT_RD(c, n, (void)0)
#define AWAIT_S(c, n) for (u32 sp_ = 0;; ++sp_) { CB(); const u32 cnt_ = *(volatile AS3 u32*)(cv + 4 * (c)); CB(); \
                                 if ((u32)__builtin_amdgcn_readfirstlane(cnt_) >= 4u * (n) || sp_ > LDS_CAP) break; __builtin_amdgcn_s_sleep(1); }
#define PQ_MX0 "v_max3_u32 %[t], v144, v145, v146\n\tv_max_u32 %[t], %[t], v147\n\t"
#define PQ_MX3 "v_max3_u32 %[t], v144, v145, v146\n\tv_max3_u32 %[u], v147, v148, v149\n\tv_max3_u32 %[t], v150, v151, %[t]\n\tv_max3_u32 %[u], v152, v153, %[u]\n\t" \
               "v_max3_u32 %[t], v154, v155, %[t]\n\tv_max_u32 %[t], %[t], %[u]\n\t"
#define PQ_BODY(MX, LD) MX "v_cmp_eq_u32 vcc, %[S], %[t]\n\ts_cbranch_vccz 2f\n\ts_mov_b32 %[c], %[cap]\n" \
  "1:\n\ts_sleep %[d]\n\ts_mov_b64 exec, vcc\n\t" LD "s_mov_b64 exec, -1\n\ts_sub_u32 %[c], %[c], 1\n\ts_cselect_b64 %[m], 0, -1\n\ts_waitcnt vmcnt(0)\n\t" \
  MX "v_cmp_eq_u32 vcc, %[S], %[t]\n\ts_and_b64 vcc, vcc, %[m]\n\ts_cbranch_scc1 1b\n2:"
#define PQ_LD4(r, so, off) "buffer_load_dwordx4 v[" #r ":" #r "+3], %[vo], %[rs], %[" #so "] offen offset:" #off " sc1\n\t"
#define PQ_LD2(r, so) "buffer_load_dwordx2 v[" #r ":" #r "+1], %[vo], %[rs], %[" #so "] offen sc1\n\t"
#define PQ_OUT [t] "=&v"(t), [c] "=&s"(c), [m] "=&s"(m)
#define PQ_IN [vo] "v"(vo), [rs] "s"(rs), [so] "s"(so), [S] "s"(S), [d] "n"(D), [cap] "n"(SPIN_CAP)
template <int D> DI u32 pq1(u4& v, u4 rs, u32 vo, u32 so, u32 S) {
  u32 t, c; u64 m;
  asm volatile(PQ_BODY(PQ_MX0, PQ_LD4(144, so, 0)) : "+{v[144:147]}"(v), PQ_OUT : PQ_IN : "vcc", "scc", "memory");
  return t;
}
template <int D> DI void pq2(u4& v, u4 rs, u32 vo, u32 so, u32 s2, u32 S) {
  u32 t, c; u64 m;
  asm volatile(PQ_BODY(PQ_MX0, PQ_LD2(144, so) PQ_LD2(146, s2)) : "+{v[144:147]}"(v), PQ_OUT : PQ_IN, [s2] "s"(s2) : "vcc", "scc", "memory");
}
template <int D> DI void pq3(u4& x, u4& y, u4& z, u4 rs, u32 vo, u32 so, u32 S) {
  u32 t, u, c; u64 m;
  asm volatile(PQ_BODY(PQ_MX3, PQ_LD4(144, so, 0) PQ_LD4(148, so, 1024) PQ_LD4(152, so, 2048))
               : "+{v[144:147]}"(x), "+{v[148:151]}"(y), "+{v[152:155]}"(z), [u] "=&v"(u), PQ_OUT : PQ_IN : "vcc", "scc", "memory");
}
#define PT_CK "v_bitop3_b32 %[t], v145, v147, %[g] bitop3:0x7e\n\tv_bitop3_b32 %[u], v149, v151, %[g] bitop3:0x7e\n\t" \
              "v_cmp_ne_u32 %[a], 0, %[t]\n\tv_cmp_ne_u32 %[b], 0, %[u]\n\ts_or_b64 vcc, %[a], %[b]\n\t"
template <int D> DI void pqt(u4& x, u4& y, u4 rs, u32 vo, u32 so, u32 s2, u32 g) {
  u32 t, u, c; u64 m, a, b;
  asm volatile(PT_CK "s_cbranch_scc0 2f\n\ts_mov_b32 %[c], %[cap]\n"
               "1:\n\ts_sleep %[d]\n\ts_mov_b64 exec, %[a]\n\t" PQ_LD4(144, so, 0) "s_mov_b64 exec, %[b]\n\t" PQ_LD4(148, s2, 0)
               "s_mov_b64 exec, -1\n\ts_sub_u32 %[c], %[c], 1\n\ts_cselect_b64 %[m], 0, -1\n\ts_waitcnt vmcnt(0)\n\t"
               PT_CK "s_and_b64 vcc, vcc, %[m]\n\ts_cbranch_scc1 1b\n2:"
               : "+{v[144:147]}"(x), "+{v[148:151]}"(y), [u] "=&v"(u), [a] "=&s"(a), [b] "=&s"(b), PQ_OUT
               : [vo] "v"(vo), [rs] "s"(rs), [so] "s"(so), [s2] "s"(s2), [g] "v"(g), [d] "n"(D), [cap] "n"(SPIN_CAP) : "vcc", "scc", "memory");
}
#define UNTIL(t0, d) _Pragma("nounroll") for (int i_ = 0; i_ < 64 && (u32)__builtin_amdgcn_s_memrealtime() - (u32)(t0) < (u32)(d); ++i_) __builtin_amdgcn_s_sleep(1)
#define SLEEP(n) do { if ((n) > 0) __builtin_amdgcn_s_sleep(n); } while (0)

DI void p1_tile(u4 z, u4 f10r, u4 f10i, u4 f16r, u4 f16i, f4 t2r, f4 t2i, f4& cr, f4& ci) {
  const f4 zero = {0.f, 0.f, 0.f, 0.f};
  f4 ar = mfma_h(z, f10r, zero), ai = mfma_h(z, f10i, zero);
  cmul(ar, ai, t2r, t2i);
  u4 y = packc(ar, ai);
  cr = mfma_h(y, f16r, zero); ci = mfma_h(y, f16i, zero);
}
DI u4 p3_tile(u4 z, u4 f16r, u4 f16i, u4 f10r, u4 f10i, f4 t2r, f4 t2i, f4 t1r, f4 t1i) {
  const f4 zero = {0.f, 0.f, 0.f, 0.f};
  f4 ar = mfma_h(z, f16r, zero), ai = mfma_h(z, f16i, zero);
  cmulc(ar, ai, t2r, t2i);
  u4 y = packc(ar, ai);
  f4 br = mfma_h(f10r, y, zero), bi = mfma_h(f10i, y, zero);
  cmul(br, bi, t1r, t1i);
  return packc(br, bi);
}
DI u2 ldx2s(rsrc_t r, u32 o, u32 so) { return __builtin_amdgcn_raw_buffer_load_b64(r, o, so, 16); }
DI u4 ldx4s(rsrc_t r, u32 o, u32 so) { return __builtin_amdgcn_raw_buffer_load_b128(r, o, so, 16); }
DI u4 gpass(rsrc_t rs, u32 o, u32 so, u32 d, int w) {
  if (w < 8) return ldx4s(rs, o, so);
  const u2 a = ldx2s(rs, o, so), b = ldx2s(rs, o, so + d);
  return u4{a.x, a.y, b.x, b.y};
}
DI u4 gather(rsrc_t rs, u4 rsw, u32 o, u32 so, u32 d, int w, u4 v) {
#pragma nounroll
  for (u32 spin = 0; spin < SPIN_CAP; ++spin) {
    const bool miss = mx4(v) == SENTH;
    if (!__any(miss)) break;
    PIN(o);
    if (miss) v = gpass(rs, o, so, d, w);
  }
  return v;
}
template <class P> DI u4 mkrs(P p, u32 n) { const u64 a = (u64)p; return u4{(u32)a, (u32)(a >> 32) & 0xFFFFu, n, 0x00020000u}; }
#define PL_LD(b, i, off) "buffer_load_dwordx4 v[" #b "+4*" #i ":" #b "+4*" #i "+3], %[vo], %[rs], %[so] offen offset:" #off " sc1\n\t"
#define PL_MX(b, i) "v_max3_u32 %[t], v[" #b "+4*" #i "], v[" #b "+4*" #i "+1], %[t]\n\tv_max3_u32 %[t], v[" #b "+4*" #i "+2], v[" #b "+4*" #i "+3], %[t]\n\t"
#define PL_MX0(b) "v_max3_u32 %[t], v[" #b "], v[" #b "+1], v[" #b "+2]\n\tv_max_u32 %[t], %[t], v[" #b "+3]\n\t"
#define PL_MV(b, i, r0, r1) "v_mov_b64 %[" #r0 "], v[" #b "+4*" #i ":" #b "+4*" #i "+1]\n\tv_mov_b64 %[" #r1 "], v[" #b "+4*" #i "+2:" #b "+4*" #i "+3]\n\t"
#define PL_BODY(NL, LD0, LD1, MX0, MX1, MV0, MV1) \
  LD0 "s_sleep %[d]\n\t" LD1 "s_mov_b32 %[c], %[cap]\n" \
  "1:\n\ts_waitcnt vmcnt(" #NL ")\n\t" MX0 "v_cmp_eq_u32 vcc, %[S], %[t]\n\ts_cbranch_vccz 2f\n\ts_mov_b64 exec, vcc\n\t" LD0 "s_mov_b64 exec, -1\n\t" \
  "s_waitcnt vmcnt(" #NL ")\n\t" MX1 "v_cmp_eq_u32 vcc, %[S], %[t]\n\ts_cbranch_vccz 3f\n\ts_mov_b64 exec, vcc\n\t" LD1 "s_mov_b64 exec, -1\n\t" \
  "s_sub_u32 %[c], %[c], 1\n\ts_cbranch_scc0 1b\n\ts_waitcnt vmcnt(0)\n" \
  "3:\n\t" MV1 "s_branch 4f\n" \
  "2:\n\t" MV0 \
  "4:"
#define PL_CLOB "vcc", "scc", "memory", "v144", "v145", "v146", "v147", "v148", "v149", "v150", "v151", "v152", "v153", "v154", "v155", \
                "v156", "v157", "v158", "v159", "v160", "v161", "v162", "v163", "v164", "v165", "v166", "v167"
#define PL_IN [vo] "v"(vo), [rs] "s"(rs), [so] "s"(so), [S] "s"(S), [d] "n"(D), [cap] "n"(SPIN_CAP)
DI u4 j4(u2 a, u2 b) { return u4{a.x, a.y, b.x, b.y}; }
template <int D> DI u4 poll1(u4 rs, u32 vo, u32 so, u32 S) {
  u2 a0, a1; u32 t, c;
  asm volatile(PL_BODY(1, PL_LD(144, 0, 0), PL_LD(156, 0, 0), PL_MX0(144), PL_MX0(156), PL_MV(144, 0, a0, a1), PL_MV(156, 0, a0, a1))
               : [a0] "=&v"(a0), [a1] "=&v"(a1), [t] "=&v"(t), [c] "=&s"(c) : PL_IN : PL_CLOB);
  return j4(a0, a1);
}
template <int D> DI void poll2(u4 rs, u32 vo, u32 so, u32 S, u4& x, u4& y) {
  u2 a0, a1, b0, b1; u32 t, c;
  asm volatile(PL_BODY(2, PL_LD(144, 0, 0) PL_LD(144, 1, 64), PL_LD(156, 0, 0) PL_LD(156, 1, 64), PL_MX0(144) PL_MX(144, 1), PL_MX0(156) PL_MX(156, 1),
                       PL_MV(144, 0, a0, a1) PL_MV(144, 1, b0, b1), PL_MV(156, 0, a0, a1) PL_MV(156, 1, b0, b1))
               : [a0] "=&v"(a0), [a1] "=&v"(a1), [b0] "=&v"(b0), [b1] "=&v"(b1), [t] "=&v"(t), [c] "=&s"(c) : PL_IN : PL_CLOB);
  x = j4(a0, a1); y = j4(b0, b1);
}
template <int D> DI void poll3(u4 rs, u32 vo, u32 so, u32 S, u4& x, u4& y, u4& z) {
  u2 a0, a1, b0, b1, c0, c1; u32 t, c;
  asm volatile(PL_BODY(3, PL_LD(144, 0, 0) PL_LD(144, 1, 1024) PL_LD(144, 2, 2048), PL_LD(156, 0, 0) PL_LD(156, 1, 1024) PL_LD(156, 2, 2048),
                       PL_MX0(144) PL_MX(144, 1) PL_MX(144, 2), PL_MX0(156) PL_MX(156, 1) PL_MX(156, 2),
                       PL_MV(144, 0, a0, a1) PL_MV(144, 1, b0, b1) PL_MV(144, 2, c0, c1), PL_MV(156, 0, a0, a1) PL_MV(156, 1, b0, b1) PL_MV(156, 2, c0, c1))
               : [a0] "=&v"(a0), [a1] "=&v"(a1), [b0] "=&v"(b0), [b1] "=&v"(b1), [c0] "=&v"(c0), [c1] "=&v"(c1), [t] "=&v"(t), [c] "=&s"(c) : PL_IN : PL_CLOB);
  x = j4(a0, a1); y = j4(b0, b1); z = j4(c0, c1);
}
#define ST4S(v, r, o, so, aux) do { st4s_<aux>(v, r, o, so); } while (0)
template <int AUX> DI void st4s_(u4 v, rsrc_t r, u32 o, u32 so) {
  __builtin_amdgcn_raw_buffer_store_b128(v, r, o, so, AUX);
  SCHED();
  asm volatile("s_nop 1");
  SCHED();
}
template <int N> DI void tdma(u4 r, u32 vo, u32 so, u32 dst) {
  u32 keep;
  asm volatile("s_mov_b32 %0, m0\n\ts_mov_b32 m0, %4\n\ts_nop 0\n\tbuffer_load_dwordx4 %1, %2, %3 offen lds\n\tbuffer_load_dwordx4 %1, %2, %3 offen offset:1024 lds\n\t"
               ".if %5 > 2\n\tbuffer_load_dwordx4 %1, %2, %3 offen offset:2048 lds\n\tbuffer_load_dwordx4 %1, %2, %3 offen offset:3072 lds\n\t.endif\n\ts_mov_b32 m0, %0"
               : "=&s"(keep) : "v"(vo), "s"(r), "s"(so), "s"(dst), "n"(N) : "memory");
}
DI u32 hpk(hh2 v) { return __builtin_bit_cast(u32, v); }
DI hh2 h2v(u32 v) { return __builtin_bit_cast(hh2, v); }

typedef u32 u8v __attribute__((ext_vector_type(8)));
#define AS1 __attribute__((address_space(1)))
typedef const AS1 u16* gp16;
DI u64 mk64(u32 a, u32 b) { return (u64)a | ((u64)b << 32); }
#define KWAIT() SCHED(); asm volatile("s_waitcnt lgkmcnt(0)" ::: "memory"); SCHED(); asm volatile("" : "+s"(ka), "+s"(kb), "+s"(kc), "+s"(kd)); if (T_G1) PINS(tz_); STAMP0B(); \
  const gp16 x = (gp16)mk64(ka[0], ka[1]), fwr = (gp16)mk64(ka[2], ka[3]), fwi = (gp16)mk64(ka[4], ka[5]), rw = (gp16)mk64(ka[6], ka[7]); \
  const gp16 wg = (gp16)mk64(kb[0], kb[1]), wu = (gp16)mk64(kb[2], kb[3]), wd = (gp16)mk64(kb[4], kb[5]), cvec = (gp16)mk64(kb[6], kb[7]); \
  const gp16 wmod = (gp16)mk64(kc[0], kc[1]); AS1 u16* const out = (AS1 u16*)mk64(kc[2], kc[3]); AS1 unsigned char* const ws = (AS1 unsigned char*)mk64(kd[0], kd[1]); \
  const rsrc_t rs = __builtin_amdgcn_make_buffer_rsrc((void*)ws, 0, WS_END, 0x00020000); \
  (void)x; (void)fwr; (void)fwi; (void)rw; (void)wg; (void)wu; (void)wd; (void)cvec; (void)wmod; (void)out; (void)rs

#define PF_AT(pos) do { const int og_ = ((224 * xg) & ~63) + 64 * ((l & 3) < 3 || xg < 7 ? (l & 3) : 2), od_ = ((80 * xg) & ~63) + 64 * (l & 1); \
    if ((PP / 10000) % 10 == pos) glds4(wg + (20 * xi + (l >> 2)) * 1728 + og_, ldsb + L_DUMP * 4); \
    if ((PP / 1000) % 10 == pos) glds4(wu + (20 * xi + (l >> 2)) * 1728 + og_, ldsb + L_DUMP * 4); \
    if ((PP / 100) % 10 == pos) glds4(((l & 16) ? wu : wg) + (20 * xi + 16 + ((l >> 2) & 3)) * 1728 + og_, ldsb + L_DUMP * 4); \
    if ((PP / 10) % 10 == pos) glds4(wd + (54 * xi + (l >> 1)) * 640 + od_, ldsb + L_DUMP * 4); \
    if (PP % 10 == pos) glds4(wd + (54 * xi + 22 + (l >> 1)) * 640 + od_, ldsb + L_DUMP * 4); } while (0)

__global__ void __launch_bounds__(NT) fused_kernel(const u16*, const u16*, const u16*, const u16*, const u16*, const u16*, const u16*, const u16*, const u16*,
                                                   u16*, unsigned char*) {
  __shared__ __attribute__((aligned(16))) u32 lds[L_END + (STAMPS ? 384 : 0)];
  STAMP0A();
  u64 tz_ = 0;
  if (T_G1) asm volatile("s_memrealtime %0" : "=s"(tz_));
  u8v ka, kb; u4 kc; u2 kd;
  asm volatile("s_load_dwordx4 %2, %4, 0x40\n\ts_load_dwordx8 %1, %4, 0x20\n\ts_load_dwordx8 %0, %4, 0x0\n\ts_load_dwordx2 %3, %4, 0x50"
               : "=&s"(ka), "=&s"(kb), "=&s"(kc), "=&s"(kd) : "s"(__builtin_amdgcn_kernarg_segment_ptr()));
  SCHED();
  u64 pc;
  asm volatile("s_getpc_b64 %0" : "=s"(pc));
  float* ldf = (float*)lds;
  const int t = threadIdx.x, l = t & 63, lo = l & 15, q = l >> 4;
  const int w = __builtin_amdgcn_readfirstlane(t >> 6);
  const int xg = blockIdx.x & 7, xi = blockIdx.x >> 3;
  const int pb = (xg << 5) | xi;
  const f4 zero4 = {0.f, 0.f, 0.f, 0.f};
  const u4 sent4 = {SENT, SENT, SENT, SENT};
  const u32 ldsb = (u32)(size_t)lds;
  const bool pkd = (w & 7) == 0;
  const bool crit = xi >= 2 && xi < 22;
  const int ci = xg * 20 + xi - 2;
  const int xa = (t * 820) >> 16;
  const u32 pvo = xa * (256 * 1280) + pb * 1280;
  u32 xoff = pvo - xa * 1280 + t * 16;
  u4 wmA, xv;
  u32 cA, pivraw;
  if (w < 10) {
    u64 pw_, pc_, px_;
    asm volatile("s_load_dwordx2 %0, %3, 0x40\n\ts_load_dwordx2 %1, %3, 0x38\n\ts_load_dwordx2 %2, %3, 0x0\n\ts_setprio 2"
                 : "=&s"(pw_), "=&s"(pc_), "=&s"(px_) : "s"(__builtin_amdgcn_kernarg_segment_ptr()));
    u32 wmo = (t >> 1) * (crit ? 7680u : 0u) + ((t & 1) * 16 + (crit ? (ci & 1) * (320 * 7680) + (ci >> 1) * 32 : 0));
    u32 cao = ((t & (crit ? ~1 : ~0)) << (crit ? 0 : 1)) + (crit ? (ci & 1) * 640 : 0);
    asm volatile("s_waitcnt lgkmcnt(0)\n\tglobal_load_dwordx4 %0, %4, %8\n\tglobal_load_ushort %1, %5, %9\n\tglobal_load_dwordx4 %2, %6, %10\n\t"
                 "global_load_ushort %3, %7, %10" : "=&v"(wmA), "=&v"(cA), "=&v"(xv), "=&v"(pivraw)
                 : "v"(wmo), "v"(cao), "v"(xoff), "v"(pvo), "s"(pw_), "s"(pc_), "s"(px_) : "memory");
  } else {
    if (w == 11) lds[L_CNT + l] = 0u;
    else if (xi == 1) {
#pragma unroll
      for (int i = 0; i < CODE_PF; ++i) glds4((const char*)(pc & ~127ull) + l * 128 + i * 8192, ldsb + L_DUMP * 4);
      if (CODE_L > 0) glds4((const char*)(pc & ~127ull) + (l < CODE_L ? l : 0) * 128 + CODE_PF * 8192, ldsb + L_DUMP * 4);
    }
    asm volatile("s_setprio 3\n\ts_waitcnt lgkmcnt(0)" ::: "memory");
  }
  __builtin_amdgcn_s_barrier();
  SCHED();
  u32 cv = ldsb + 4;
  PIN(cv);
  const int pi = xg * 10 + xi - 22;
  const bool pas2 = xi >= 22;
  const bool symb = xi == 0, dblb = symb && (xg & 3) == 0;
  const int gub = xg * 14 + xi - 2, dnb = xg * 10 + xi - 16;
  const bool isgu = xi >= 2 && xi < 16 && gub < 108, isdn = xi >= 16 && xi < 26;
  STAMP_INIT();

  if (__builtin_expect(w == 10, 0)) {
    KWAIT();
    u32 tag;
    asm volatile("s_load_dword %0, %1, 0x0" : "=s"(tag) : "s"(ws + WS_E) : "memory");
    u32 er, ei;
    {
      const int o = (xg + 8 * (8 * xi + (l >> 3))) * 321 + (l & 1) * 320;
      SLEEP(D_PFW);
      asm volatile("global_load_ushort %0, %1, off" : "=v"(er) : "v"(fwr + o));
      asm volatile("global_load_ushort %0, %1, off" : "=v"(ei) : "v"(fwi + o));
    }
#define PF_W_() glds4((l < 32 ? fwr : fwi) + (((xg + 8 * (8 * xi + ((l >> 2) & 7))) * 321 + 64 + 64 * (l & 3)) & ~1), ldsb + L_DUMP * 4)
#define PF_M_() do { const int r5 = (l * 52) >> 8; const gp16 p = wmod + (20 * xi + (r5 < 10 ? r5 : 9)) * 3840 + 1280 + 320 * xg + 64 * (l - 5 * r5); \
                     glds4(p, ldsb + L_DUMP * 4); glds4(p + 10 * 3840, ldsb + L_DUMP * 4); } while (0)
    if (PW_AT == 0) PF_W_();
    if (PM_AT == 0) PF_M_();
    SCHED();
    asm volatile("s_waitcnt lgkmcnt(0)\n\ts_add_u32 %0, %0, 1" : "+s"(tag) :: "memory", "scc");
    stl<u32>(ldsb + L_MISC * 4, tag);
    ARRIVE(C_TAG); ARRIVE(C_X);
    if (crit) {
      const u32 pa = ldsb + L_PA * 4 + l * 4;
      u32 o1 = (R1 > 1 || (l & 49) == 0) ? (u32)(WS_G1 + (2 * q + (l & 1)) * 20480 + (lo >> 1) * 16) : OOB;
      if (G1X) o1 = (l >> 3) == (ci & 1) ? (u32)(WS_G1 + (l & 7) * 16) : OOB;
      PIN(o1);
      float p0, p1, p2;
#pragma nounroll
      for (u32 sp = 0; sp < LDS_CAP; ++sp) {
        CB();
        const u32 cnt = *(volatile AS3 u32*)(cv + 4 * C_MOD);
        CB();
        p0 = ldl<float>(pa); p1 = ldl<float>(pa + 256); p2 = ldl<float>(pa + 512);
        CB();
        if ((u32)__builtin_amdgcn_readfirstlane(cnt) >= 40u) break;
        asm volatile("s_waitcnt lgkmcnt(0)\n\ts_sleep 1" ::: "memory");
      }
      float s2 = (p0 + p1) + (q < 2 ? p2 : 0.f);
      s2 = sw32(s2, s2); s2 = sw16(s2, s2);
      if (G1X) ST4S((u4{__builtin_bit_cast(u32, s2), tag, __builtin_bit_cast(u32, dppf<ROR8>(s2)), tag}), rs, o1, ci * 128, 16);
      else ST4S((u4{__builtin_bit_cast(u32, dppf<0xA0>(s2)), tag, __builtin_bit_cast(u32, dppf<0xF5>(s2)), tag}), rs, o1, ci * 128, 16);
      SCHED();
    }
    __builtin_amdgcn_s_setprio(HPRIO);
    STAMP(1);
    asm volatile("s_waitcnt vmcnt(%2)" : "+v"(er), "+v"(ei) : "n"((PW_AT == 0 ? 1 : 0) + (PM_AT == 0 && !XMAP ? 2 : 0) + TLBW) : "memory");
    stx2(rs, (l & 6) == 0 ? (u32)(WS_GS + pb * 128 + (l >> 3) * 16 + (l & 1) * 8) : OOB, u2{er | (ei << 16), tag});
    if (PM_AT == 1) PF_M_();
    if (PW_AT == 1) PF_W_();
    if (symb) {
      u4 a[4], b[4];
      AWAIT_S(C_X, 11);
#pragma nounroll
      for (u32 spin = 0;; ++spin) {
        u32 m = lo + 64 * q;
        PIN(m);
        u32 bad = 0u;
#pragma unroll
        for (int r = 0; r < 4; ++r) {
          a[r] = ldx4(rs, WS_GS + xg * 4096 + (m + 16 * r) * 16);
          b[r] = ldx4(rs, WS_GS + ((8 - xg) & 7) * 4096 + (((xg ? 255 : 256) - m - 16 * r) & 255) * 16);
        }
#pragma unroll
        for (int r = 0; r < 4; ++r) bad |= (a[r].y ^ tag) | (a[r].w ^ tag) | (b[r].y ^ tag) | (b[r].w ^ tag);
        if (__all(bad == 0u) || spin > SPIN_CAP) break;
        __builtin_amdgcn_s_sleep(8);
      }
      f4 sr, si, tr, ti;
      us2 sm = {0, 0};
#pragma unroll
      for (int r = 0; r < 4; ++r) {
        const u32 p = xg < 5 ? a[r].x : a[r].z, n = xg < 5 ? b[r].x : b[r].z, p2 = dblb ? a[r].z : 0u, n2 = dblb ? b[r].z : 0u;
        sr[r] = 0.5f * (blo(p) + blo(n)); si[r] = 0.5f * (bhi(p) - bhi(n));
        tr[r] = 0.5f * (blo(p2) + blo(n2)); ti[r] = 0.5f * (bhi(p2) - bhi(n2));
        sm = __builtin_elementwise_max(sm, __builtin_bit_cast(us2, p & 0x7FFF7FFFu)); sm = __builtin_elementwise_max(sm, __builtin_bit_cast(us2, n & 0x7FFF7FFFu));
        sm = __builtin_elementwise_max(sm, __builtin_bit_cast(us2, p2 & 0x7FFF7FFFu)); sm = __builtin_elementwise_max(sm, __builtin_bit_cast(us2, n2 & 0x7FFF7FFFu));
      }
      f4* sp = (f4*)(ldf + L_SYM) + l;
      sp[0] = sr; sp[64] = si; sp[128] = tr; sp[192] = ti;
      const u32 se = wave_max(mx((u32)sm.x, (u32)sm.y)) >> 7;
      if (l == 0) lds[L_MISC + 5] = se;
      ARRIVE(C_VF);
    }
    u2 rv[3];
#pragma unroll
    for (int i = 0; i < 3; ++i) rv[i] = *(const AS1 u2*)(rw + 4 * (i < 2 ? l + 64 * i : 128 + (l & 31)));
    if (pas2) {
      AWAIT_S(C_X, 11);
      SLEEP(D_P2);
      const gp16 src = wmod + (l >> 2) * 3840 + 1280 + 32 * pi + 8 * (l & 3);
#pragma nounroll
      for (int i = 0; i < 40; i += 4) {
#pragma unroll
        for (int j = 0; j < 4; ++j) glds16(src + (i + j) * 16 * 3840, ldsb + (L_MW + (i + j) * 256) * 4);
        asm volatile("s_waitcnt vmcnt(%0)" :: "n"(P2_N) : "memory");
      }
      DRAIN();
      STAMP(7);
      __builtin_amdgcn_s_setprio(P2_PRIO);
      const u32 wa = ldsb + L_MW * 4 + (8 * q + (lo >> 2)) * 64 + (lo & 3) * 8, sa_ = ldsb + L_SC * 4 + 16 * q;
      f4 a0 = zero4, a1 = zero4;
#pragma unroll P2U_
      for (int s = 0; s < 20; ++s) {
        const u2 p0 = ldtr(wa + s * 2048), p1 = ldtr(wa + s * 2048 + 256), p2 = ldtr(wa + s * 2048 + 32), p3 = ldtr(wa + s * 2048 + 288);
        const u4 b = ldl<u4>(sa_ + s * 64);
        a0 = mfma_b(u4{p0.x, p0.y, p1.x, p1.y}, b, a0); a1 = mfma_b(u4{p2.x, p2.y, p3.x, p3.y}, b, a1);
      }
      f4 s4 = bfr4((lo & 1) ? a1 : a0);
      if (pi >= 40 && pi < 60) s4 += 1.0f;
      stx4(rs, (R2 > 1 || lo < 2) ? (u32)(WS_G2 + (lo >> 1) * 10240 + pi * 128 + (lo & 1) * 64 + q * 16) : OOB, u4{pk_b(s4.x, s4.y), tag, pk_b(s4.z, s4.w), tag});
      __builtin_amdgcn_s_setprio(HPRIO);
      STAMP(6);
    }
    STAMP(2);
    if (!(G3B && pas2)) {
    AWAIT_S(C_SM, 10);
    SLEEP(D_G3A);
#pragma nounroll
    for (u32 spin = 0;; ++spin) {
      u32 o = WS_G3 + xg * 1024 + l * 16;
      PIN(o);
      const u4 g = ldx4(rs, o);
      const u32 bad = (g.x ^ tag) | (g.y ^ tag) | (g.z ^ tag) | (g.w ^ tag);
      if (__all(bad == 0u) || spin > SPIN_CAP) break;
      __builtin_amdgcn_s_sleep(4);
    }
    ARRIVE(C_G);
    }
    STAMP(5);
    if (blockIdx.x == 0 && l == 0) stx1(rs, WS_E, tag);
    if (PM_AT == 3) PF_M_();
    AWAIT_S(G2C, G2AT);
    if (!PF2L) PF_AT(2);
    SLEEP(D_G2);
    {
      u4 g[12];
#pragma nounroll
      for (u32 spin = 0;; ++spin) {
        u32 o = WS_G2 + (R2 > 1 ? xg * 10240 : 0) + l * 16, o2 = WS_G2 + (R2 > 1 ? xg * 10240 : 0) + 2048 + (l & 31) * 16;
        PIN(o); PIN(o2);
#pragma unroll
        for (int i = 0; i < 4; ++i) { g[3 * i] = ldx4(rs, o + i * 2560); g[3 * i + 1] = ldx4(rs, o + i * 2560 + 1024); g[3 * i + 2] = ldx4(rs, o2 + i * 2560); }
        u32 bad = 0u;
#pragma unroll
        for (int i = 0; i < 12; ++i) bad |= (g[i].y ^ tag) | (g[i].w ^ tag);
        if (__all(bad == 0u) || spin > SPIN_CAP) break;
        __builtin_amdgcn_s_sleep(G2S);
      }
      if (PF2L) { SCHED(); PF_AT(2); SCHED(); }
      f4 qs = zero4, r0 = zero4;
      const u32 pp = ldsb + L_PAR * 4 + l * 16, pp2 = ldsb + L_PAR * 4 + 2048 + (l & 31) * 16;
#pragma unroll
      for (int i = 0; i < 3; ++i) {
        const u32 a = i < 2 ? pp + 1024 * i : pp2;
        const f4 wr = unp4(rv[i]), hf = i < 2 ? wr : wr * 0.5f;
        const f4 sb = unp4(u2{g[3 + i].x, g[3 + i].z}), g2 = unp4(u2{g[6 + i].x, g[6 + i].z});
        stl<f4>(a, unp4(u2{g[i].x, g[i].z})); stl<f4>(a + 2560, sb); stl<f4>(a + 5120, g2); stl<f4>(a + 7680, unp4(u2{g[9 + i].x, g[9 + i].z}));
        stl<f4>(a + 10240, g2 * wr);
        qs += g2 * hf; r0 += sb * hf;
      }
      const float Q = wave_sum((qs.x + qs.y) + (qs.z + qs.w)), R0 = wave_sum((r0.x + r0.y) + (r0.z + r0.w));
      if (l == 0) { ldf[L_MISC + 2] = Q; ldf[L_MISC + 3] = R0; }
    }
    STAMP(3);
    ARRIVE(C_PB);
    STAMP(4);
    STAMP_OUT();
    return;
  }
  if (__builtin_expect(w == 11, 0)) {
    __builtin_amdgcn_s_setprio(HPRIO);
    const u4 rT = mkrs(TAB, sizeof(TAB));
    SLEEP(D_T);
#pragma unroll
    for (int i = 0; i < 3; ++i) tdma<4>(rT, l * 16, i * 4096, ldsb + L_T * 4 + i * 4096);
    tdma<2>(rT, l * 16, 12288, ldsb + L_T * 4 + 12288);
    DRAIN();
    ARRIVE(C_T);
    STAMP(8);
    KWAIT();
    const rsrc_t rr = __builtin_amdgcn_make_buffer_rsrc((void*)fwr, 0, 2048 * 321 * 2, 0x00020000);
    const rsrc_t ri = __builtin_amdgcn_make_buffer_rsrc((void*)fwi, 0, 2048 * 321 * 2, 0x00020000);
    const u32 vo = (u32)(((xg + 8 * (lo + 64 * q)) * 321 + 10 * xi - (xg & 1)) * 2);
#define VFILL() do { const u4 senth4 = {SENTH, SENTH, SENTH, SENTH}; \
      _Pragma("unroll") for (int i = 0; i < 10; ++i) stx4(rs, WS_V + pb * 10240 + i * 1024 + l * 16, senth4); \
      stx4(rs, l < 16 ? (u32)(WS_RK + (l >> 1) * 8192 + pb * 32 + (l & 1) * 16) : ((l < 32 && isgu) ? (u32)(WS_ACT + gub * 256 + (l - 16) * 16) : OOB), sent4); \
      stx4(rs, (l < 56 && pb < 24) ? (u32)(WS_SLOT + pb * 896 + l * 16) : OOB, sent4); } while (0)
    AWAIT_S(C_X, G_UF);
    SLEEP(D_UF);
    {
      const u4 senth4 = {SENTH, SENTH, SENTH, SENTH};
#pragma unroll
      for (int i = 0; i < 10; ++i) stx4(rs, WS_U + pb * 10240 + i * 1024 + l * 16, senth4);
    }
    if (VFE == 1) VFILL();
#pragma unroll
    for (int i = 0; i < 3; ++i) tdma<4>(rT, l * 16, 14336 + i * 4096, ldsb + L_T * 4 + 14336 + i * 4096);
#pragma unroll
    for (int i = 0; i < 2; ++i) tdma<4>(rT, l * 16, 26624 + xg * 8192 + i * 4096, ldsb + L_T * 4 + 26624 + i * 4096);
    AWAIT_S(C_TAG, 1);
    const u32 tag = lds[L_MISC];
    DRAIN();
    if (l < 8) stx1(rs, WS_G3 + l * 1024 + pb * 4, tag);
    ARRIVE(C_U);
    STAMP(5);
    AWAIT_S(C_SM, G_VF);
    SLEEP(D_VF);
    if (VFE == 0) VFILL();
    STAMP(6);
    AWAIT_S(C_SM, D_HB);
    u4 a4[4], b4[4]; u2 a2[4], b2[4];
#pragma unroll
    for (int r = 0; r < 4; ++r) {
      a4[r] = __builtin_amdgcn_raw_buffer_load_b128(rr, vo, r * 82176, 0); a2[r] = __builtin_amdgcn_raw_buffer_load_b64(rr, vo + 16, r * 82176, 0);
      b4[r] = __builtin_amdgcn_raw_buffer_load_b128(ri, vo, r * 82176, 0); b2[r] = __builtin_amdgcn_raw_buffer_load_b64(ri, vo + 16, r * 82176, 0);
    }
    SCHED();
    if (VFE == 3) VFILL();
    SCHED();
    us2 m = {0, 0};
#pragma unroll
    for (int r = 0; r < 4; ++r) {
#pragma unroll
      for (int i = 0; i < 4; ++i) {
        m = __builtin_elementwise_max(m, __builtin_bit_cast(us2, a4[r][i] & 0x7FFF7FFFu));
        m = __builtin_elementwise_max(m, __builtin_bit_cast(us2, b4[r][i] & 0x7FFF7FFFu));
      }
#pragma unroll
      for (int i = 0; i < 2; ++i) {
        m = __builtin_elementwise_max(m, __builtin_bit_cast(us2, a2[r][i] & 0x7FFF7FFFu));
        m = __builtin_elementwise_max(m, __builtin_bit_cast(us2, b2[r][i] & 0x7FFF7FFFu));
      }
    }
    u32 wexp = wave_max(mx((u32)m.x, (u32)m.y)) >> 7;
    STAMP(1);
    {
      const u32 sh = (xg & 1) * 16;
      u32 ea[4][5], eb[4][5];
#pragma unroll
      for (int r = 0; r < 4; ++r) {
        const u32 da[6] = {a4[r].x, a4[r].y, a4[r].z, a4[r].w, a2[r].x, a2[r].y}, db[6] = {b4[r].x, b4[r].y, b4[r].z, b4[r].w, b2[r].x, b2[r].y};
#pragma unroll
        for (int i = 0; i < 5; ++i) { ea[r][i] = __builtin_amdgcn_alignbit(da[i + 1], da[i], sh); eb[r][i] = __builtin_amdgcn_alignbit(db[i + 1], db[i], sh); }
      }
#pragma unroll
      for (int c = 0; c < 10; ++c) {
        u4 o;
#pragma unroll
        for (int r = 0; r < 4; ++r) o[r] = __builtin_amdgcn_perm(eb[r][c >> 1], ea[r][c >> 1], (c & 1) ? 0x07060302u : 0x05040100u);
        *(u4*)(lds + L_W + c * 256 + 4 * l) = o;
      }
    }
    if (symb) { AWAIT_S(C_VF, 1); wexp = mx(wexp, lds[L_MISC + 5]); }
    if (l == 0) lds[L_MISC + 4] = wexp;
    ARRIVE(C_W);
    STAMP(3);
    if (VFE == 2) VFILL();
    DRAIN();
    if (l < 8) stx2(rs, WS_G4 + l * 2048 + pb * 8, u2{wexp, tag});
    STAMP(2);
    if (G3B && pas2) {
    AWAIT_S(C_SM, 10);
    SLEEP(D_G3A);
#pragma nounroll
    for (u32 spin = 0;; ++spin) {
      u32 o = WS_G3 + xg * 1024 + l * 16;
      PIN(o);
      const u4 g = ldx4(rs, o);
      const u32 bad = (g.x ^ tag) | (g.y ^ tag) | (g.z ^ tag) | (g.w ^ tag);
      if (__all(bad == 0u) || spin > SPIN_CAP) break;
      __builtin_amdgcn_s_sleep(4);
    }
    ARRIVE(C_G);
    }
    PF_AT(1);
    AWAIT_S(C_5, G4AT);
    {
      u4 e;
#pragma nounroll
      for (u32 spin = 0;; ++spin) {
        u32 o4 = WS_G4 + xg * 2048 + l * 32;
        PIN(o4);
        const u4 g0 = ldx4(rs, o4), g1 = ldx4(rs, o4 + 16);
        e = u4{g0.x, g0.z, g1.x, g1.z};
        const u32 bad = (g0.y ^ tag) | (g0.w ^ tag) | (g1.y ^ tag) | (g1.w ^ tag);
        if (__all(bad == 0u) || spin > SPIN_CAP) break;
        __builtin_amdgcn_s_sleep(4);
      }
      const int gexp = (int)wave_max(mx4(e));
#pragma unroll
      for (int i = 0; i < 4; ++i) { const float v = pow2i((int)e[i] - gexp); lds[L_FAC + 4 * l + i] = pk_h(v, v); }
      if (l == 0) lds[L_MISC + 1] = (u32)gexp;
      ARRIVE(C_PAR);
    }
    STAMP(7);
    AWAIT_S(C_5, PFA);
    PF_AT(3);
    AWAIT_S(C_7, PFB);
    PF_AT(4);
    if (isgu) {
      const int o = (l >> 1) * 1728 + 16 * gub + 8 * (l & 1);
#pragma nounroll
      for (int i = 0; i < 20; i += 2) {
        glds16(wg + o + i * 32 * 1728, ldsb + (L_MW + 256 * i) * 4); glds16(wg + o + (i + 1) * 32 * 1728, ldsb + (L_MW + 256 * i + 256) * 4);
        glds16(wu + o + i * 32 * 1728, ldsb + (L_MW + 5120 + 256 * i) * 4); glds16(wu + o + (i + 1) * 32 * 1728, ldsb + (L_MW + 5120 + 256 * i + 256) * 4);
      }
      DRAIN();
      ARRIVE(C_MW);
    } else if (xi < 2 || xi >= 16) {
      const gp16 p = wd + (96 * (xi < 2 ? xi : xi - 14) + (l >> 1)) * 640 + ((80 * xg) & ~63) + 64 * (l & 1);
#pragma unroll
      for (int i = 0; i < 3; ++i) glds4(p + i * 32 * 640, ldsb + L_DUMP * 4);
    }
    STAMP(9);
    AWAIT_S(C_8, 9);
    PF_AT(5);
    AWAIT_S(C_9, 10);
    PF_AT(6);
    STAMP(4);
    STAMP_OUT();
    return;
  }

  KWAIT();
  STAMP(2);
  const u4 rsw = mkrs(ws, WS_END);
  const rsrc_t rsT = __builtin_amdgcn_make_buffer_rsrc((void*)TAB, 0, sizeof(TAB), 0x00020000);
  const rsrc_t rsP = __builtin_amdgcn_make_buffer_rsrc((void*)TAB, 0, pkd ? sizeof(TAB) : 0, 0x00020000);
  const u32 t16 = ldsb + t * 16;
  stl<u4>(t16 + (L_A + 2560) * 4, u4{0u, 0u, 0u, 0u});
  const int sg0 = pkd ? (w >> 1) : (w & 3) + 8 * (w >> 2);
  u32 a1w = ldsb + (L_A + w * 320) * 4 + q * 272 + lo * 16;
  u32 a1r = ldsb + (L_A + sg0 * 4) * 4 + q * 5120 + (lo >> 2) * 272 + (lo & 3) * 4;
  const u32 lq = q < 3 ? lo * 48 + q * 16 : 768 + lo * 16;
  u32 auw = ldsb + L_B * 4 + lq + (q < 3 ? sg0 * 768 : 12288);
  u32 spw = ldsb + (L_SP + (w >> 3) * 512) * 4 + lq;
  const u32 dc4 = q * 640 + lo * 4;
  u32 og = q * 2560 + lo * (G1X ? 16 : 8);
  u32 tl = ldsb + L_T * 4 + l * 16;
  u32 tw = ldsb + (L_T + T_TW1 + w * 32) * 4 + q * 16 + (lo < 4 ? 0 : (lo < 8 ? 2560 : 1280));
  u32 pa1 = ldsb + (L_PA + 16 * w) * 4 + (lo & 1) * 32 + q * 8, st1 = ldsb + L_ST1 * 4 + (t >> 4) * 8;
  u32 sp = ldsb + L_ST1 * 4 + (lo & 7) * 40, px = ldsb + L_X * 4 + (lo & 7) * 1280, pd = ldsb + (L_B + 16 * w) * 4 + dc4;
  u32 a1s = a1r + 64;
  PIN(xoff); PIN(a1w); PIN(a1r); PIN(a1s); PIN(auw); PIN(spw); PIN(og); PIN(tl); PIN(tw); PIN(pa1); PIN(st1); PIN(sp); PIN(px); PIN(pd);
  SCHED();
  STAMP(22);
  if (crit) {
    asm volatile("s_waitcnt vmcnt(2)" : "+v"(wmA), "+v"(cA) :: "memory");
    STAMP(23);
    const float s = bfr(silu_f(blo(cA)));
    const float p[8] = {blo(wmA.x) * s, bhi(wmA.x) * s, blo(wmA.y) * s, bhi(wmA.y) * s, blo(wmA.z) * s, bhi(wmA.z) * s, blo(wmA.w) * s, bhi(wmA.w) * s};
    float v4[4];
#pragma unroll
    for (int i = 0; i < 4; ++i) v4[i] = sw32(p[i], p[i + 4]);
    float va_ = sw16(v4[0], v4[2]), vb_ = sw16(v4[1], v4[3]);
    va_ += dppf<ROR8>(va_); PIN(va_); vb_ += dppf<ROR8>(vb_); PIN(vb_);
    va_ += dppf<ROR4>(va_); PIN(va_); vb_ += dppf<ROR4>(vb_); PIN(vb_);
    va_ += dppf<ROR2>(va_); PIN(va_); vb_ += dppf<ROR2>(vb_); PIN(vb_);
    stl<f2>(pa1, f2{va_, vb_});
    ARRIVE(C_MOD);
    STAMP(1);
  }
  asm volatile("s_waitcnt vmcnt(0)" : "+v"(cA), "+v"(wmA), "+v"(xv), "+v"(pivraw) :: "memory");
  u4 uav = __builtin_amdgcn_raw_buffer_load_b128(rsT, t16, T_ADR * 4, 0);
  u4 sprv = __builtin_amdgcn_raw_buffer_load_b128(rsP, l * 16, T_ADP * 4 + w * 128, 0);
  stl<u4>(t16 + L_X * 4, xv);
  {
    const float pv = blo(pivraw);
    const f4 d0 = unp4(u2{xv.x, xv.y}) - pv, d1 = unp4(u2{xv.z, xv.w}) - pv;
    stl<f4>(2 * t16 + L_B * 4, d0); stl<f4>(2 * t16 + L_B * 4 + 16, d1);
    const f4 a1 = d0 + d1, a2 = d0 * d0 + d1 * d1;
    const f2 b1 = a1.xy + a1.zw, b2 = a2.xy + a2.zw;
    float s1 = b1.x + b1.y, s2 = b2.x + b2.y;
    PIN(s1); PIN(s2);
    s1 += dppf<ROR8>(s1); PIN(s1); s2 += dppf<ROR8>(s2); PIN(s2);
    s1 += dppf<ROR4>(s1); PIN(s1); s2 += dppf<ROR4>(s2); PIN(s2);
    s1 += dppf<ROR2>(s1); PIN(s1); s2 += dppf<ROR2>(s2); PIN(s2);
    s1 += dppf<ROR1>(s1); PIN(s1); s2 += dppf<ROR1>(s2); PIN(s2);
    stl<f2>(st1, f2{s1, s2});
  }
  if (pas2) {
    const float sv = silu_f(blo(cA));
    stl<u16>(ldsb + L_SC * 4 + 2 * t, (u16)pk_b(sv, sv));
  }
  ARRIVE(C_X);
#define TB(T_, off) ldl<T_>(tl + (off) * 4)
  u4 b1rh, b1ih, b1rl, b1il;
  AWAIT_RD(C_T, 1, b1rh = TB(u4, T_B1); b1ih = TB(u4, T_B1 + 256); b1rl = TB(u4, T_B1 + 512); b1il = TB(u4, T_B1 + 768));
  f4 t1r = ldl<f4>(tw), t1i = ldl<f4>(tw + 64);
  u4 f10r = TB(u4, T_F10), f10i = TB(u4, T_F10 + 256);
  f4 t2r = TB(f4, T_TW2), t2i = TB(f4, T_TW2 + 256);
  u4 fA, fB;
  if (pkd) {
    const u32 sw = lo >= 8 ? 1024u : 0u;
    const hh8 hf = {0.5f16, 0.5f16, 0.5f16, 0.5f16, 0.5f16, 0.5f16, 0.5f16, 0.5f16};
    fA = __builtin_bit_cast(u4, ldl<hh8>(tl + sw + T_F16 * 4) * hf); fB = __builtin_bit_cast(u4, ldl<hh8>(tl - sw + (T_F16 + 256) * 4) * hf);
  } else {
    fA = TB(u4, T_F16); fB = TB(u4, T_F16 + 256) ^ (lo >= 8 ? 0x80008000u : 0u);
  }
  PIN(b1rh); PIN(b1ih); PIN(b1rl); PIN(b1il); PIN(t1r); PIN(t1i); PIN(f10r); PIN(f10i); PIN(t2r); PIN(t2i); PIN(fA); PIN(fB);
  SCHED();
  STAMP(18);
  float m1l;
  f2 nrm[4];
  u32 tag;
  {
    f2 s0, s1, s2, s3, s4;
    u32 pvr;
    AWAIT_RD(C_X, 11, s0 = ldl<f2>(sp); s1 = ldl<f2>(sp + 8); s2 = ldl<f2>(sp + 16); s3 = ldl<f2>(sp + 24); s4 = ldl<f2>(sp + 32); pvr = ldl<u32>(px));
    STAMP(3);
#pragma unroll
    for (int a = 0; a < 4; ++a) nrm[a] = f2{ldl<float>(pd + 5120 * a), ldl<float>(pd + 5120 * a + 2560)};
    tag = ldl<u32>(ldsb + L_MISC * 4);
    const f2 s = ((s0 + s1) + (s2 + s3) + s4) * (1.0f / 640.0f);
    const float pv = blo(pvr);
    m1l = bfr(pv + s.x);
    const float var = bfr(fmaxf(s.y - s.x * s.x, 0.f));
    const float rsd = __builtin_amdgcn_rcpf(bfr(__builtin_amdgcn_sqrtf(bfr(var + 1e-5f))));
    const u32 sc = ldsb + (L_ST2 + 16 * w) * 4;
    stl<float>(sc + lo * 4, lo < 8 ? rsd : (pv - m1l) * rsd);
    CB();
    const f4 r0 = ldl<f4>(sc), r1 = ldl<f4>(sc + 16), c0 = ldl<f4>(sc + 32), c1 = ldl<f4>(sc + 48);
    nrm[0] = nrm[0] * r0.xy + c0.xy; nrm[1] = nrm[1] * r0.zw + c0.zw; nrm[2] = nrm[2] * r1.xy + c1.xy; nrm[3] = nrm[3] * r1.zw + c1.zw;
  }
#pragma unroll
  for (int a = 0; a < 4; ++a) PIN(nrm[a]);
  PIN(tag); PIN(uav); PIN(sprv);
  if (T_G1) {
    const u32 el = (u32)__builtin_amdgcn_s_memrealtime() - (u32)tz_ + T_G1D, dl = (T_G1D && el < T_G1) ? el : T_G1;
    UNTIL(tz_, dl);
  } else if ((G1M >> xg) & 1) SLEEP(D_G1L); else SLEEP(D_G1);
  STAMP(4);

  int e1;
  {
    u4 a0 = ldx4s(rs, og, WS_G1 + w * 256), c0 = ldx4s(rs, og, WS_G1 + 10240 + w * 256);
#pragma nounroll
    for (u32 spin = 0; spin < SPIN_CAP; ++spin) {
      const bool ma = ((a0.y ^ tag) | (a0.w ^ tag)) != 0u, mc = ((c0.y ^ tag) | (c0.w ^ tag)) != 0u;
      if (!__any(ma || mc)) break;
      __builtin_amdgcn_s_sleep(G1S);
      PIN(og);
      if (ma) a0 = ldx4s(rs, og, WS_G1 + w * 256);
      if (mc) c0 = ldx4s(rs, og, WS_G1 + 10240 + w * 256);
    }
    STAMP(5);
    const f2 m = bfr2(f2{asf(a0.x) + dppf<ROR8>(asf(a0.z)), asf(c0.x) + dppf<ROR8>(asf(c0.z))});
    const float sa = m.x, g1 = bfr(1.0f + m.y);
    u32 mm = __builtin_bit_cast(u32, fmaxf(fabsf(sa), fabsf(g1)));
    mm = mx(mm, dppu<ROR8>(mm)); mm = mx(mm, dppu<ROR4>(mm)); mm = mx(mm, dppu<ROR2>(mm)); mm = mx(mm, dppu<ROR1>(mm));
    asm volatile("s_mov_b64 exec, %2\n\tds_max_u32 %0, %1 offset:%3\n\ts_mov_b64 exec, 1\n\tds_add_u32 %0, %0 offset:%4\n\ts_mov_b64 exec, -1"
                 :: "v"(cv), "v"(mm), "s"(0x0001000100010001ull), "n"(4 * C_SMV), "n"(4 * C_SM) : "memory");
    u4 hf;
#pragma unroll
    for (int a = 0; a < 4; ++a) { const f2 p = nrm[a] * g1 + sa; hf[a] = pk_b(p.x, p.y); }
    f4 cr = mfma_b(hf, b1rl, zero4), ci_ = mfma_b(hf, b1il, zero4);
    cr = mfma_b(hf, b1rh, cr); ci_ = mfma_b(hf, b1ih, ci_);
    cmul(cr, ci_, t1r, t1i);
    PIN(cr); PIN(ci_);
    FST(25);
    u2 cm;
    AWAIT_X(10, cm.x, cm = *(volatile AS3 u2*)(cv + 4 * C_SM));
    FST(19);
    e1 = 128 - (int)((u32)__builtin_amdgcn_readfirstlane(cm.y) >> 23);
    e1 = e1 > 100 ? 100 : e1;
    PINS(e1);
    e1 = e1 < -126 ? -126 : e1;
    const float s1 = asf((u32)(e1 + 127) << 23);
    stl<u4>(a1w, packc(cr * s1, ci_ * s1));
  }
  ARRIVE(C_4);
  if (w != 9) {
    if (!pkd) {
      u4 za, zb;
      AWAIT_RD(C_4, 10, za = u4{ldl<u32>(a1r), ldl<u32>(a1r + 1280), ldl<u32>(a1r + 2560), ldl<u32>(a1r + 3840)};
                        zb = u4{ldl<u32>(a1s), ldl<u32>(a1s + 1280), ldl<u32>(a1s + 2560), ldl<u32>(a1s + 3840)});
      FST(20);
      f4 ar, ai, br, bi;
      ar = mfma_h(za, f10r, zero4); ai = mfma_h(za, f10i, zero4); br = mfma_h(zb, f10r, zero4); bi = mfma_h(zb, f10i, zero4);
      SCHED();
      cmul(ar, ai, t2r, t2i);
      za = packc(ar, ai);
      ar = mfma_h(za, fA, zero4); ai = mfma_h(za, fB, zero4);
      SCHED();
      cmul(br, bi, t2r, t2i);
      zb = packc(br, bi);
      br = mfma_h(zb, fA, zero4); bi = mfma_h(zb, fB, zero4);
      SCHED();
      stl<u4>(auw, packc(ar, ai));
      SCHED();
      stl<u4>(auw + 3072, packc(br, bi));
    } else {
      u4 za;
      AWAIT_RD(C_4, 10, za = u4{ldl<u32>(a1r), ldl<u32>(a1r + 1280), ldl<u32>(a1r + 2560), ldl<u32>(a1r + 3840)});
      f4 ar, ai;
      p1_tile(za, f10r, f10i, fA, fB, t2r, t2i, ar, ai);
      stl<f4>(spw, ar); stl<f4>(spw + 1024, ai);
      CB();
      f4 ox, oy;
#pragma unroll
      for (int r = 0; r < 4; ++r) { ox[r] = ldl<float>(sprv[r]); oy[r] = ldl<float>(sprv[r] + 1024); }
      const float da = 2.0f * ar.x, db = 2.0f * oy.x;
      ar += oy; ai -= ox;
      if (w == 0 && (l & 55) == 0) { ar.x = l ? db : da; ai.x = l ? da : db; }
      stl<u4>(auw, packc(ar, ai));
    }
    ARRIVE(C_U);
    FST(26);
  }
  {
    u4 v;
    AWAIT_RD(C_U, 10, v = u4{ldl<u32>(uav.x), ldl<u32>(uav.y), ldl<u32>(uav.z), ldl<u32>(uav.w)});
    FST(21);
    ST4S(v, rs, t16, WS_U + pb * 10240, 16);
    SCHED();
  }
  STAMP(6);
  const u32 t6 = T_U ? (u32)__builtin_amdgcn_s_memrealtime() : 0u;
  u4 vav = __builtin_amdgcn_raw_buffer_load_b128(rsT, t16, (T_ADR + 2560) * 4, 0);
  u4 pAv = __builtin_amdgcn_raw_buffer_load_b128(rsP, l * 16, (T_ADP + 512) * 4 + w * 128, 0);
  u4 pBv = __builtin_amdgcn_raw_buffer_load_b128(rsP, l * 16, (T_ADP + 1024) * 4 + w * 128, 0);
  u4 bar = TB(u4, 6656), bai = TB(u4, 6656 + 256);
  f4 tar = TB(f4, 6656 + 512), tai = TB(f4, 6656 + 768);
  f4 tbr = TB(f4, 6656 + 1024), tbi = TB(f4, 6656 + 1280);
  u4 aar = TB(u4, 6656 + 1536), aai = TB(u4, 6656 + 1792);
  u4 g16r = TB(u4, T_F16I), g16i = TB(u4, T_F16I + 256), f16r = TB(u4, T_F16), f16i = TB(u4, T_F16 + 256);
  const u32 gj = w < 8 ? t >> 1 : t - 512;
  const u32 gof = w < 8 ? 16 * (t & 1) : 32;
  u32 go0 = (((gj & 15) << 4) | (gj >> 4)) * 10240 + gof;
  u32 ainw = ldsb + L_A * 4 + gj * 4 + gof * 260, ainw2 = ainw + (w < 8 ? 2080 : 512);
  u32 ainr = ldsb + (L_A + w * 260) * 4 + lo * 64 + q * 16;
  u32 avw = ldsb + (L_B + w) * 4 + q * 2560 + lo * 40;
  const bool dblw = dblb && w == 0;
  PIN(bar); PIN(bai); PIN(tar); PIN(tai); PIN(tbr); PIN(tbi); PIN(aar); PIN(aai); PIN(g16r); PIN(g16i); PIN(f16r); PIN(f16i);
  PIN(go0); PIN(ainw); PIN(ainw2); PIN(ainr); PIN(avw);
  PIN(vav); PIN(pAv); PIN(pBv);
  u4 gu_;
  AWAIT(C_W, 1);
  const int wexp = __builtin_amdgcn_readfirstlane(lds[L_MISC + 4]);
  f4 fr, fi;
  {
    const u4 wv = ldl<u4>(tl + (L_W - L_T + w * 256) * 4);
    const float sl = pow2i(126 - wexp);
    fr = f4{blo(wv.x), blo(wv.y), blo(wv.z), blo(wv.w)}; fi = f4{bhi(wv.x), bhi(wv.y), bhi(wv.z), bhi(wv.w)};
    if (symb && w == 0) { fr = ((const f4*)(ldf + L_SYM))[l]; fi = ((const f4*)(ldf + L_SYM))[64 + l]; }
    fr *= sl; fi *= sl;
  }
  PIN(fr); PIN(fi);
  if (DRU) DRAIN();
  AWAIT(C_G, 1);
  SLEEP(D_U);
  if (T_U) { UNTIL(t6, T_U); }
  STAMP(7);
  gu_ = gpass(rs, go0, WS_U + pb * 40, 81920, w);

  {
    const u4 v = gather(rs, rsw, go0, WS_U + pb * 40, 81920, w, gu_);
    stl<u32>(ainw, v.x); stl<u32>(ainw + 1040, v.y); stl<u32>(ainw2, v.z); stl<u32>(ainw2 + 1040, v.w);
  }
  ARRIVE(C_5);
  STAMP(8);
  {
    u4 z;
    AWAIT_RD(C_5, 10, z = ldl<u4>(ainr));
    f4 yr, yi;
    if (!dblw) {
      f4 ar = mfma_h(z, bar, zero4), ai = mfma_h(z, bai, zero4);
      cmul(ar, ai, tar, tai);
      const u4 y = packc(ar, ai);
      yr = mfma_h(f16r, y, zero4); yi = mfma_h(f16i, y, zero4);
      cmul(yr, yi, fr, fi);
    } else {
      const f4* sy = (const f4*)(ldf + L_SYM) + l;
      const float sl = pow2i(126 - wexp);
      const u4 za = z & 0x0000FFFFu, zb = z & 0xFFFF0000u;
      f4 ar = mfma_h(za, bar, zero4), ai = mfma_h(za, bai, zero4), br = mfma_h(zb, bar, zero4), bi = mfma_h(zb, bai, zero4);
      cmul(ar, ai, tar, tai); cmul(br, bi, tar, tai);
      const u4 ya = packc(ar, ai), yb = packc(br, bi);
      yr = mfma_h(f16r, ya, zero4); yi = mfma_h(f16i, ya, zero4);
      f4 vr = mfma_h(f16r, yb, zero4), vi = mfma_h(f16i, yb, zero4);
      cmul(yr, yi, fr, fi); cmul(vr, vi, sy[128] * sl, sy[192] * sl);
      yr += vr; yi += vi;
    }
    const u4 y2 = packc(yr, yi);
    f4 er = mfma_h(y2, g16r, zero4), ei = mfma_h(y2, g16i, zero4);
    cmul(er, ei, tbr, tbi);
    const u4 y3 = packc(er, ei);
    const f4 gr = mfma_h(aar, y3, zero4), gi = mfma_h(aai, y3, zero4);
    const u4 o = packc(gr, gi);
    stl<u32>(avw, o.x); stl<u32>(avw + 640, o.y); stl<u32>(avw + 1280, o.z); stl<u32>(avw + 1920, o.w);
  }
  ARRIVE(C_6);
  {
    u4 v;
    AWAIT_RD(C_6, 10, v = ldl<u4>(t16 + L_B * 4));
    ST4S(v, rs, t16, WS_V + pb * 10240, 16);
    SCHED();
  }
  STAMP(9);
  const u32 ec2 = (lo >> 3) * 320 + q * 8 + w * 32;
  const u32 arw = ldsb + (lo & 7) * 1280 + ec2, apar = ldsb + L_PAR * 4 + 2 * ec2;
  f4 x0 = unp4(ldl<u2>(arw + L_X * 4)) - m1l, x1v = unp4(ldl<u2>(arw + L_X * 4 + 640)) - m1l;
  u4 g10r = TB(u4, T_F10I), g10i = TB(u4, T_F10I + 256), fin0 = TB(u4, T_T1I), fin1 = TB(u4, T_T1I + 256);
  const u32 tA = tl + ((w == 8) ? 4096 : (w < 4 ? 0 : 2048)), tB = tl + (w < 4 ? 4096 : 2048);
  f4 u1r = ldl<f4>(tA + T_TW1I * 4), u1i = ldl<f4>(tA + (T_TW1I + 256) * 4);
  f4 u2r = ldl<f4>(tB + T_TW1I * 4), u2i = ldl<f4>(tB + (T_TW1I + 256) * 4);
  g16r ^= (q >= 2 ? 0x80000000u : 0u); g16i ^= (q >= 2 ? 0x80000000u : 0u);
  go0 = gj * 10240 + gof;
  u4 gv;
#define VPASS() do { SCHED(); AWAIT(C_PAR, 1); SLEEP(D_V); gv = gpass(rs, go0, WS_V + pb * 40, 1310720, w); SCHED(); } while (0)
  const int l10 = lo < 10 ? lo : lo - 10;
  u32 a3r = ldsb + (L_A + sg0 * 256) * 4 + q * 256 + l10 * 16;
  u32 a3w = ldsb + (L_B + (pkd ? w >> 3 : 2 * w)) * 4 + q * 5120 + lo * 80;
  u32 a4r = ldsb + (L_B + 320 * w) * 4 + lo * 80 + q * 16;
  u32 as2w = ldsb + (L_ST2 + w * 32) * 4 + (lo & 7) * 16 + q * 4;
  u32 as2r = ldsb + L_ST2 * 4 + (lo >> 3) * 640 + (lo & 7) * 16;
  u32 pcs = q < 2 ? 0x3C00BC00u : 0xBC003C00u;
  u32 keyl = (u32)(2047 - pb - 256 * (lo & 7));
  u32 ork = (l & 3) == 0 ? (u32)((l >> 3) * 8192 + (l & 4) * 4) : OOB;
  PIN(g10r); PIN(g10i); PIN(fin0); PIN(fin1); PIN(u1r); PIN(u1i); PIN(u2r); PIN(u2i); PIN(g16r); PIN(g16i); PIN(x0); PIN(x1v);
  PIN(a3r); PIN(a3w); PIN(a4r); PIN(as2w); PIN(as2r); PIN(pcs); PIN(keyl); PIN(ork); PIN(go0);
  VPASS();
  const int ef = __builtin_amdgcn_readfirstlane(lds[L_MISC + 1]) - 126 - e1;
  const float fs = 0.17677669529663687f * pow2i(ef >> 1), fs2 = pow2i(ef - (ef >> 1));
  u32 fa0 = ldl<u32>(ldsb + L_FAC * 4 + gj * 4), fa1 = ldl<u32>(ldsb + L_FAC * 4 + gj * 4 + (w < 8 ? 0 : 512));
  PIN(fa0); PIN(fa1);
  f4 gs0, gs1, q0, q1;
  f2 qr;
  CB();
  const bool par_ = __builtin_amdgcn_readfirstlane(*(volatile AS3 u32*)(cv + 4 * C_PB)) != 0;
  CB();
#define PARS() do { gs0 = (ldl<f4>(apar) * fs) * fs2; gs1 = (ldl<f4>(apar + 1280) * fs) * fs2; q0 = ldl<f4>(apar + 10240); q1 = ldl<f4>(apar + 11520); \
                    qr = ldl<f2>(ldsb + (L_MISC + 2) * 4); PIN(gs0); PIN(gs1); PIN(q0); PIN(q1); PIN(qr); } while (0)
  PARS();
  PIN(g10r); PIN(g10i); PIN(fin0); PIN(fin1); PIN(u1r); PIN(u1i); PIN(u2r); PIN(u2i); PIN(g16r); PIN(g16i); PIN(x0); PIN(x1v);
  PIN(a3r); PIN(a3w); PIN(a4r); PIN(as2w); PIN(as2r); PIN(pcs); PIN(keyl); PIN(ork); PIN(go0);
  STAMP(10);

  {
    const u4 v = gather(rs, rsw, go0, WS_V + pb * 40, 1310720, w, gv);
    stl<u32>(vav.x, hpk(h2v(v.x) * h2v(fa0))); stl<u32>(vav.y, hpk(h2v(v.y) * h2v(fa0)));
    stl<u32>(vav.z, hpk(h2v(v.z) * h2v(fa1))); stl<u32>(vav.w, hpk(h2v(v.w) * h2v(fa1)));
  }
  ARRIVE(C_7);
  STAMP(11);
  if (w != 9) {
    if (!pkd) {
      u4 za, zb;
      AWAIT_RD(C_7, 10, za = ldl<u4>(a3r); zb = ldl<u4>(a3r + 4096));
      f4 ar = mfma_h(za, g16r, zero4), ai = mfma_h(za, g16i, zero4), br = mfma_h(zb, g16r, zero4), bi = mfma_h(zb, g16i, zero4);
      SCHED();
      cmulc(ar, ai, t2r, t2i);
      za = packc(ar, ai);
      ar = mfma_h(g10r, za, zero4); ai = mfma_h(g10i, za, zero4);
      SCHED();
      cmulc(br, bi, t2r, t2i);
      zb = packc(br, bi);
      br = mfma_h(g10r, zb, zero4); bi = mfma_h(g10i, zb, zero4);
      SCHED();
      cmul(ar, ai, u1r, u1i);
      const u4 oa = packc(ar, ai);
      SCHED();
      cmul(br, bi, u2r, u2i);
      const u4 ob = packc(br, bi);
#pragma unroll
      for (int r = 0; r < 4; ++r) stl<u2>(a3w + 1280 * r, u2{oa[r], ob[r]});
    } else {
      u4 za, zb;
      AWAIT_RD(C_7, 10, za = u4{ldl<u32>(pAv.x), ldl<u32>(pAv.y), ldl<u32>(pAv.z), ldl<u32>(pAv.w)};
                        zb = u4{ldl<u32>(pBv.x), ldl<u32>(pBv.y), ldl<u32>(pBv.z), ldl<u32>(pBv.w)});
      u4 z;
#pragma unroll
      for (int r = 0; r < 4; ++r) { const hh2 b = h2v(zb[r]); z[r] = hpk(__builtin_elementwise_fma(hh2{b.y, b.x}, h2v(pcs), h2v(za[r]))); }
      if (w == 0 && l10 == 0 && (q & 1) == 0)
        z.x = q ? __builtin_amdgcn_perm(zb.x, za.x, 0x07060302u) ^ 0x80000000u : __builtin_amdgcn_perm(zb.x, za.x, 0x05040100u);
      const u4 oa = p3_tile(z, g16r, g16i, g10r, g10i, t2r, t2i, u1r, u1i);
#pragma unroll
      for (int r = 0; r < 4; ++r) stl<u32>(a3w + 1280 * r, oa[r]);
    }
    ARRIVE(C_8);
  }
  if (!par_) { AWAIT(C_PB, 1); PARS(); }
  f4 d0, d1;
  {
    u4 z;
    AWAIT_RD(C_8, 9, z = ldl<u4>(a4r));
    d0 = mfma_h(z, fin0, zero4) * gs0 + x0; d1 = mfma_h(z, fin1, zero4) * gs1 + x1v;
    const f4 a1 = d0 + d1, a2 = d0 * d0 + d1 * d1, a3 = d0 * q0 + d1 * q1;
    const f2 b1 = a1.xy + a1.zw, b2 = a2.xy + a2.zw, b3 = a3.xy + a3.zw;
    float v = sw16(sw32(b1.x + b1.y, b2.x + b2.y), sw32(b3.x, b3.y));
    v += dppf<ROR8>(v);
    stl<float>(as2w, v);
  }
  ARRIVE(C_9);
  float m2l, rs2l;
  u32 key;
  {
    f4 s0, s1_, s2_, s3_, s4_;
    AWAIT_RD(C_9, 10, s0 = ldl<f4>(as2r); s1_ = ldl<f4>(as2r + 128); s2_ = ldl<f4>(as2r + 256); s3_ = ldl<f4>(as2r + 384); s4_ = ldl<f4>(as2r + 512));
    if (KPRI && w == KW) __builtin_amdgcn_s_setprio(3);
    const f4 s = (s0 + s1_) + (s2_ + s3_) + s4_;
    const float s3h = s.y + s.w;
    const float s1 = (s.x + dppf<ROR8>(s.x)) * (1.0f / 640.0f), s3 = s3h + dppf<ROR8>(s3h), s2 = (s.z + dppf<ROR8>(s.z)) * (1.0f / 640.0f);
    m2l = bfr(m1l + s1);
    const float var = bfr(fmaxf(s2 - s1 * s1, 0.f));
    rs2l = __builtin_amdgcn_rcpf(bfr(__builtin_amdgcn_sqrtf(bfr(var + 1e-5f))));
    const float rr = (s3 + (m1l - m2l) * qr.x) * rs2l + qr.y;
    const u32 rb = pk_b(rr, rr);
    u32 ord = (rb ^ ((u32)((int)rb >> 31) | 0x80000000u)) & 0xFFFF0000u;
    if (rr != rr) ord = 0u;
    key = ord | keyl;
    if (w == KW) { ST4S((u4{key, dppu<0x39>(key), dppu<0x4E>(key), dppu<0x93>(key)}), rs, ork, WS_RK + pb * 32, 16); STAMP(24); if (KPRI) __builtin_amdgcn_s_setprio(2); }
    SCHED();
  }
  STAMP(12);
  u32 ok = w < 8 ? WS_RK + xg * 8192 + t * 16 : OOB;
  u4 k;
  if (RK_EARLY == 1) { k = ldx4(rs, ok); SCHED(); }
  if (!GUB && isgu) {
    const int o = (64 * w + (l >> 1)) * 1728 + 16 * gub + 8 * (l & 1);
    glds16(wg + o, ldsb + (L_MW + 512 * w) * 4); glds16(wg + o + 32 * 1728, ldsb + (L_MW + 512 * w + 256) * 4);
    glds16(wu + o, ldsb + (L_MW + 5120 + 512 * w) * 4); glds16(wu + o + 32 * 1728, ldsb + (L_MW + 5120 + 512 * w + 256) * 4);
  }
  {
    const float dl = (m1l - m2l) * rs2l;
    const f4 h0 = (d0 * rs2l + dl) * ldl<f4>(apar + 5120) + ldl<f4>(apar + 2560), h1 = (d1 * rs2l + dl) * ldl<f4>(apar + 6400) + ldl<f4>(apar + 3840);
    const f4 o0 = h0 * ldl<f4>(apar + 7680) + (d0 + m1l), o1 = h1 * ldl<f4>(apar + 8960) + (d1 + m1l);
    stl<u2>(arw + L_A * 4, pk4(h0)); stl<u2>(arw + L_A * 4 + 640, pk4(h1));
    stl<u2>(arw + L_B * 4, pk4(o0)); stl<u2>(arw + L_B * 4 + 640, pk4(o1));
    ARRIVE(C_R);
  }
  if (RK_EARLY == 2) { SLEEP(D_RK); k = ldx4(rs, ok); SCHED(); }

  u4 v0, v1, v2;
  const rsrc_t rsO = __builtin_amdgcn_make_buffer_rsrc((void*)out, 0, 2048 * 1280, 0x00020000);
  u32 sb0_ = WS_SLOT + l * 16, sb2_ = l < 40 ? (u32)(WS_SLOT + 2048 + l * 16) : OOB;
  if (w < 8) {
    u32 ks[8];
#pragma unroll
    for (int a = 0; a < 8; ++a) ks[a] = __builtin_amdgcn_readlane(key, a);
#define CX(i, j) { const u32 hi_ = mx(ks[i], ks[j]), lo_ = ks[i] ^ ks[j] ^ hi_; ks[i] = hi_; ks[j] = lo_; }
    CX(0, 1) CX(2, 3) CX(4, 5) CX(6, 7) CX(0, 2) CX(1, 3) CX(4, 6) CX(5, 7) CX(1, 2) CX(5, 6) CX(0, 4) CX(3, 7) CX(1, 5) CX(2, 6) CX(1, 4) CX(3, 6) CX(2, 4) CX(3, 5) CX(3, 4)
#pragma unroll
    for (int a = 0; a < 8; ++a) PINS(ks[a]);
    u32 cnt = 8u, ark = ldsb + (L_RANK + (l & 7)) * 4;
    const u32 rp = ldsb + 1280 * w + 16 * l;
    const u32 rp1 = rp + (l < 16 ? L_A * 4 + 1024 : L_B * 4 - 256);
    if (RK_EARLY == 3) { SCHED(); SLEEP(D_RK); k = ldx4(rs, ok); SCHED(); }
    AWAIT_RD(C_R, 10, v0 = ldl<u4>(rp + L_A * 4); v1 = ldl<u4>(rp1); v2 = ldl<u4>(rp + L_B * 4 + 768));
    const u32 kw = __builtin_amdgcn_readlane(key, w & 7);
    if (l >= 32) v2 = u4{kw, kw, kw, kw};
    PIN(v0); PIN(v1); PIN(v2); PIN(ok); PIN(cnt); PIN(ark); PIN(sb0_); PIN(sb2_);
    if (RK_EARLY == 0) SLEEP(D_RK);
    if (RK_EARLY == 0) k = ldx4(rs, ok);
    u32 km;
#pragma nounroll
    for (u32 spin = 0; spin < SPIN_CAP; ++spin) {
      CB();
      km = mx4(k);
      if (!__any(km == SENT)) break;
      PIN(ok);
      if (km == SENT) k = ldx4(rs, ok);
    }
    STAMP(13);
#pragma unroll
    for (int a = 0; a < 8; ++a) {
      if (__popcll(__ballot(km > ks[a])) >= 8) break;
      const u32 c = __popcll(__ballot(k.x > ks[a])) + __popcll(__ballot(k.y > ks[a])) + __popcll(__ballot(k.z > ks[a])) + __popcll(__ballot(k.w > ks[a]));
      cnt = key == ks[a] ? c : cnt;
    }
    asm volatile("s_mov_b64 exec, 0xff\n\tds_add_u32 %0, %1\n\ts_mov_b64 exec, -1" :: "v"(ark), "v"(cnt) : "memory");
  }
  ARRIVE(C_10);
  u32 sel;
  {
    u32 rank;
    AWAIT_RD(C_10, 10, rank = *(volatile AS3 u32*)(ldsb + (L_RANK + (l & 7)) * 4));
    const u32 rkw = __builtin_amdgcn_readlane(rank, w & 7);
    if (w < 8 && rkw < 8u) {
      ST4S(nos4(v0), rs, sb0_, rkw * SLOT_B, 16);
      ST4S(nos4(v1), rs, sb0_ + 1024, rkw * SLOT_B, 16);
      ST4S(nos4(v2), rs, sb2_, rkw * SLOT_B, 16);
      SCHED();
    }
    sel = (u32)__ballot(rank < 8u);
  }
  STAMP(14);
  const u32 t14 = (u32)__builtin_amdgcn_s_memrealtime();
  if (isdn && w < 9) {
    const gp16 sd_ = wd + (192 * w + l) * 640 + 8 * dnb;
#pragma unroll
    for (int i = 0; i < 3; ++i) glds16(sd_ + i * 64 * 640, ldsb + (L_MW + (3 * w + i) * 256) * 4);
  }
  if (!isgu && !isdn) SLEEP(D_OUT);
  if (!EARLY_OUT && !isgu && !(DNO && isdn) && !((sel >> xa) & 1u)) __builtin_amdgcn_raw_buffer_store_b128(ldl<u4>(t16 + L_B * 4), rsO, xoff, 0, OUT_AUX);

  if (isgu) {
    if (!GUB || GUD) DRAIN();
    if (GUB) AWAIT(C_MW, 1);
    u4 wf[4];
    {
      const u32 wp = ldsb + (L_MW + (64 * w + 8 * q + (lo >> 2)) * 8 + (lo & 3) * 2) * 4;
#pragma unroll
      for (int i = 0; i < 4; ++i) {
        const u2 a = ldtr(wp + (i >> 1) * 20480 + (i & 1) * 1024), b = ldtr(wp + (i >> 1) * 20480 + (i & 1) * 1024 + 128);
        wf[i] = u4{a.x, a.y, b.x, b.y};
        PIN(wf[i]);
      }
    }
    u32 so = COAL ? WS_SLOT + (l >> 3) * SLOT_B + w * 128 + (l & 7) * 16 : WS_SLOT + (lo & 7) * SLOT_B + (64 * w + 8 * q) * 2;
    u32 sxw = ldsb + (L_T + 4096 + w * 288) * 4 + (l >> 3) * 144 + (l & 7) * 16, sxr = ldsb + (L_T + 4096 + w * 288) * 4 + (lo & 7) * 144 + q * 16;
    PIN(sxw); PIN(sxr);
    u32 afw = ldsb + (L_T + (w * 8 + (lo & 7)) * 36 + (lo >> 3) * 16 + 4 * q) * 4;
    u32 afr = ldsb + (L_T + ((lo & 1) * 40 + 4 * w + q) * 36 + 2 * (lo >> 1)) * 4;
    u32 oac = (lo & 7) == 0 ? (u32)(WS_ACT + gub * 256 + (COAL2 ? (4 * w + q) * 16 + lo * 16 : (4 * w + q) * 32 + lo * 2)) : OOB;
    PIN(so); PIN(afw); PIN(afr); PIN(oac);
    SLEEP(D_SL);
    u4 b0 = ldx4(rs, so);
#pragma nounroll
    for (u32 spin = 0; spin < SPIN_CAP; ++spin) {
      CB();
      const bool miss = mx4(b0) == SENT;
      if (!__any(miss)) break;
      __builtin_amdgcn_s_sleep(1);
      PIN(so);
      if (miss) b0 = ldx4(rs, so);
    }
    stl<u4>(sxw, b0);
    CB();
    b0 = ldl<u4>(sxr);
    u4 b1 = ldl<u4>(sxr + 64);
    STAMP(15);
    f4 ag = mfma_b(wf[0], b0, zero4), au = mfma_b(wf[2], b0, zero4);
    ag = mfma_b(wf[1], b1, ag); au = mfma_b(wf[3], b1, au);
    stl<f4>(afw, lo < 8 ? ag : au);
    ARRIVE(C_F);
    if (w < 2) {
      f2 gp[5], up[5];
      AWAIT_RD(C_F, 10, _Pragma("unroll") for (int i = 0; i < 5; ++i) { gp[i] = ldl<f2>(afr + 1152 * i); up[i] = ldl<f2>(afr + 1152 * i + 64); });
      f2 g = ((gp[0] + gp[1]) + (gp[2] + gp[3])) + gp[4], u = ((up[0] + up[1]) + (up[2] + up[3])) + up[4];
      g.x += dppf<QX1>(g.x); g.y += dppf<QX1>(g.y); u.x += dppf<QX1>(u.x); u.y += dppf<QX1>(u.y);
      g = bfr2(g); u = bfr2(u);
      g = bfr2(f2{silu_f(g.x), silu_f(g.y)});
      const u32 a0 = nos(pk_b(g.x * u.x, g.y * u.y));
      stx4(rs, oac, u4{a0, dppu<0x102>(a0), dppu<0x104>(a0), dppu<0x106>(a0)});
    }
    STAMP(16);
    if (!EARLY_OUT && !((sel >> xa) & 1u)) __builtin_amdgcn_raw_buffer_store_b128(ldl<u4>(t16 + L_B * 4), rsO, xoff, 0, OUT_AUX);
  } else if (isdn) {
    DRAIN();
    if (DNO == 1 && !((sel >> xa) & 1u)) __builtin_amdgcn_raw_buffer_store_b128(ldl<u4>(t16 + L_B * 4), rsO, xoff, 0, OUT_AUX);
    if (w < 9) {
      u4 wf[3], b[3];
      const u32 wp = ldsb + (L_MW + (192 * w + 32 * ((lo >> 1) & 1) + 8 * q + (lo >> 2)) * 4 + (lo & 1) * 2) * 4;
#pragma unroll
      for (int i = 0; i < 3; ++i) {
        const u2 a = ldtr(wp + i * 1024), c = ldtr(wp + i * 1024 + 64);
        wf[i] = u4{a.x, a.y, c.x, c.y};
        PIN(wf[i]);
      }
      u32 ao = WS_ACT + (12 * w + 2 * (lo >> 3) + (q >> 1)) * 256 + (COAL2 ? (lo & 7) * 16 + (q & 1) * 128 : (lo & 7) * 32 + (q & 1) * 16);
      u32 afw = ldsb + ((lo >> 3) == (q >> 1) ? L_T + ((2 * w + (lo >> 3)) * 8 + (lo & 7)) * 8 + 4 * (q & 1) : L_T + 2048 + 4 * l) * 4;
      PIN(ao); PIN(afw);
      if (T_AC) { UNTIL(t14, T_AC); } else { SLEEP(D_AC); SLEEP(D_AC); }
#pragma unroll
      for (int i = 0; i < 3; ++i) b[i] = ldx4(rs, ao + i * 1024);
#pragma nounroll
      for (u32 spin = 0; spin < SPIN_CAP; ++spin) {
        CB();
        const bool miss = mx(mx4(b[0]), mx(mx4(b[1]), mx4(b[2]))) == SENT;
        if (!__any(miss)) break;
        __builtin_amdgcn_s_sleep(1);
        PIN(ao);
        if (miss) {
#pragma unroll
          for (int i = 0; i < 3; ++i) b[i] = ldx4(rs, ao + i * 1024);
        }
      }
      STAMP(16);
      f4 acc = zero4;
#pragma unroll
      for (int i = 0; i < 3; ++i) acc = mfma_b(wf[i], b[i], acc);
      stl<f4>(afw, acc);
      ARRIVE(C_F);
      if (DNO == 2 && !((sel >> xa) & 1u)) __builtin_amdgcn_raw_buffer_store_b128(ldl<u4>(t16 + L_B * 4), rsO, xoff, 0, OUT_AUX);
    } else {
      const int tok = l >> 3, cp = (l >> 1) & 3, hf = l & 1;
      u32 sx = SENT, sk = SENT;
      u32 so = WS_SLOT + tok * SLOT_B + (8 * dnb + 2 * cp) * 2, sko = WS_SLOT + tok * SLOT_B + 2560;
      if (T_AC) { UNTIL(t14, T_DS); } else SLEEP(D_AC);
#pragma nounroll
      for (u32 spin = 0;; ++spin) {
        PIN(so); PIN(sko);
        if (sx == SENT) sx = ldx1(rs, so + 1280);
        if (sk == SENT) sk = ldx1(rs, sko);
        if (__all(mx(sx, sk) != SENT) || spin > SPIN_CAP) break;
        __builtin_amdgcn_s_sleep(2);
      }
      STAMP(15);
      if (DNO == 2 && !((sel >> xa) & 1u)) __builtin_amdgcn_raw_buffer_store_b128(ldl<u4>(t16 + L_B * 4), rsO, xoff, 0, OUT_AUX);
      const u32 ord = sk >> 16;
      const float rr = blo((ord & 0x8000u) ? (ord ^ 0x8000u) : (~ord & 0xFFFFu));
      const float sg = bfr(__builtin_amdgcn_rcpf(1.0f + __expf(-rr)));
      f2 gb = *(const f2*)(ldf + L_PAR + 1920 + 8 * dnb + 2 * cp) * sg;
      u32 oo = ((2047 - (int)(sk & 0xFFFFu)) & 2047) * 640 + 8 * dnb + 2 * cp;
      u32 afr = ldsb + (L_T + (hf * 72 + tok) * 8 + 2 * cp) * 4;
      f2 xv2 = {blo(sx), bhi(sx)};
      PIN(gb); PIN(oo); PIN(afr); PIN(xv2);
      f2 p[9];
      AWAIT_RD(C_F, 9, _Pragma("unroll") for (int i = 0; i < 9; ++i) p[i] = ldl<f2>(afr + 256 * i));
      f2 y = (((p[0] + p[1]) + (p[2] + p[3])) + ((p[4] + p[5]) + (p[6] + p[7]))) + p[8];
      y.x += dppf<QX1>(y.x); y.y += dppf<QX1>(y.y);
      const f2 z = y * gb + xv2;
      if (hf == 0) *(AS1 u32*)(out + oo) = pk_b(z.x, z.y);
      STAMP(17);
    }
  }
  STAMP_OUT();
}

extern "C" void kernel_launch(void* const* d_in, const int* in_sizes, int n_in, void* d_out, int out_size, void* d_ws, size_t ws_size,
                              hipStream_t stream) {
  (void)in_sizes; (void)n_in; (void)out_size; (void)ws_size;
  fused_kernel<<<256, NT, 0, stream>>>((const u16*)d_in[0], (const u16*)d_in[1], (const u16*)d_in[2], (const u16*)d_in[4], (const u16*)d_in[5],
                                       (const u16*)d_in[6], (const u16*)d_in[7], (const u16*)d_in[8], (const u16*)d_in[9], (u16*)d_out,
                                       (unsigned char*)d_ws);
}
```
